```python
import torch

def _softmax(x, axis=-1):
    return torch.softmax(x, dim=axis)
def _gelu(x):
    return torch.nn.functional.gelu(x, approximate="tanh")
def _silu(x):
    return torch.nn.functional.silu(x)
def _sigmoid(x):
    return torch.sigmoid(x)
def _layernorm(x, axis=-1, eps=1e-5):
    m = x.mean(dim=axis, keepdim=True)
    v = x.var(dim=axis, keepdim=True, unbiased=False)
    return (x - m) / torch.sqrt(v + eps)
def _scatter_add(tgt, idx, src, axis=0):
    return tgt.clone().index_add_(axis, idx, src)
def _segment_sum(src, seg, n):
    out = torch.zeros((n,) + src.shape[1:], dtype=src.dtype)
    return out.index_add_(0, seg, src)
def _pad(x, pw):
    flat = tuple(v for pair in reversed(pw) for v in pair)
    return torch.nn.functional.pad(x, flat)

kernel_name = 'gated_mix__sliding_local_dilated__dense_activation_squared_relu__prompt'

def setup_inputs():

    _w = (lambda a: (lambda t: t.to(torch.bfloat16) if t.is_floating_point() else t)(torch.as_tensor(a)))
    def _A_setup():
        B, S, D = 1, 2048, 448
        return {
            "x": _w((lambda *s: torch.randn(*s, dtype=torch.bfloat16) * 0.05)(B, S, D)),
            "wqkv": _w((lambda *s: torch.randn(*s, dtype=torch.bfloat16) * 0.8)(D, 3 * D)),
            "wo": _w((lambda *s: torch.randn(*s, dtype=torch.bfloat16) * 0.05)(D, D)),
        }
    _A_in = _A_setup()

    def _B_setup():
        B, S, D, F = 1, 2048, 448, 1792
        return {
            "x": _w((lambda *s: torch.randn(*s, dtype=torch.bfloat16) * 0.05)(B, S, D)),
            "w1": _w((lambda *s: torch.randn(*s, dtype=torch.bfloat16) * 0.1)(D, F)),
            "b1": _w((lambda *s: torch.randn(*s, dtype=torch.bfloat16) * 0.1)(F)),
            "w2": _w((lambda *s: torch.randn(*s, dtype=torch.bfloat16) * 0.1)(F, D)),
            "b2": _w((lambda *s: torch.randn(*s, dtype=torch.bfloat16) * 0.1)(D)),
        }
    _B_in = _B_setup()

    return {
        "A_x": _A_in["x"],
        "A_wqkv": _A_in["wqkv"],
        "A_wo": _A_in["wo"],
        "B_x": _B_in["x"],
        "B_w1": _B_in["w1"],
        "B_b1": _B_in["b1"],
        "B_w2": _B_in["w2"],
        "B_b2": _B_in["b2"],
        "gate_w": _w((lambda *s: torch.randn(*s, dtype=torch.bfloat16) * 0.05)(448) * 16.0),
    }

def reference(A_x, A_wqkv, A_wo, B_x, B_w1, B_b1, B_w2, B_b2, gate_w):
    def _A_ref(x, wqkv, wo):
        B, S, D = x.shape
        H = 16; Dh = D // H
        W, dil = 8, 2
        qkv = (lambda x, *d: x.permute(*d))((x @ wqkv).reshape(B, S, 3, H, Dh), 2, 0, 3, 1, 4)
        q, k, v = qkv[0], qkv[1], qkv[2]
        scale = Dh ** -0.5
        pos = torch.arange(S)
        off = torch.arange(W) * dil
        idx = pos[:, None] - off[None, :]
        mask = idx >= 0
        idx_c = torch.where(mask, idx, 0).reshape(-1)
        kg = k[:, :, idx_c, :].reshape(B, H, S, W, Dh)
        vg = v[:, :, idx_c, :].reshape(B, H, S, W, Dh)
        logits = torch.einsum("bhsd,bhswd->bhsw", q, kg) * scale
        logits = torch.where(mask[None, None, :, :], logits, -1e9)
        attn = _softmax(logits, axis=-1)
        o = torch.einsum("bhsw,bhswd->bhsd", attn, vg)
        return (lambda x, *d: x.permute(*d))(o, 0, 2, 1, 3).reshape(B, S, D) @ wo

    def _B_ref(x, w1, b1, w2, b2):
        h = torch.einsum("bsd,df->bsf", x, w1) + b1
        zero = torch.zeros_like(h)
        r = torch.where(h > zero, h, zero)
        h = r * r
        out = torch.einsum("bsf,fd->bsd", h, w2) + b2
        return out

    x = A_x
    g = _sigmoid((x * gate_w).sum(-1, keepdims=True))
    a = _A_ref(x, A_wqkv, A_wo)
    b = _B_ref(x, B_w1, B_b1, B_w2, B_b2)
    return g * a + (1 - g) * b
```

```cpp
#include <hip/hip_runtime.h>
#include <hip/hip_bf16.h>
#include <stdint.h>

typedef unsigned short u16;
__device__ __forceinline__ float bf2f(u16 b){ return __uint_as_float(((unsigned)b)<<16); }
__device__ __forceinline__ u16 f2bf(float f){ __hip_bfloat16 h = __float2bfloat16(f); return *reinterpret_cast<u16*>(&h); }
__device__ __forceinline__ float rbf(float f){ return bf2f(f2bf(f)); }

constexpr int S=2048, D=448, F=1792, H=16, DH=28;

__global__ void gemm_naive(const u16* A, const u16* B, u16* C, int M, int N, int K){
  int n = blockIdx.x*blockDim.x+threadIdx.x; int m = blockIdx.y;
  if(n>=N) return;
  float acc=0.f;
  for(int k=0;k<K;++k) acc += bf2f(A[(size_t)m*K+k])*bf2f(B[(size_t)k*N+n]);
  C[(size_t)m*N+n]=f2bf(acc);
}
__global__ void act_k(u16* h, const u16* b1){
  int i = blockIdx.x*blockDim.x+threadIdx.x; if(i>=S*F) return;
  float v = rbf(bf2f(h[i])+bf2f(b1[i%F])); v = v>0.f?v:0.f; h[i]=f2bf(v*v);
}
__global__ void attn_k(const u16* qkv, u16* o){
  int i = blockIdx.x*blockDim.x+threadIdx.x; if(i>=S*H) return;
  int s=i/H, hh=i%H;
  const float scale = rbf(0.18898223650461362f);
  float lg[8]; float mx=-3e38f;
  for(int w=0;w<8;++w){
    int t=s-2*w; float v;
    if(t>=0){ float a=0.f; for(int d=0;d<DH;++d) a+=bf2f(qkv[(size_t)s*1344+hh*DH+d])*bf2f(qkv[(size_t)t*1344+448+hh*DH+d]); v=rbf(rbf(a)*0.18898223650461362f);} else v=rbf(-1e9f);
    lg[w]=v; mx=fmaxf(mx,v);
  }
  (void)scale;
  float sum=0.f; for(int w=0;w<8;++w){ lg[w]=expf(lg[w]-mx); sum+=lg[w]; }
  for(int w=0;w<8;++w) lg[w]=rbf(lg[w]/sum);
  for(int d=0;d<DH;++d){ float a=0.f; for(int w=0;w<8;++w){ int t=s-2*w; if(t<0) t=0; a+=lg[w]*bf2f(qkv[(size_t)t*1344+896+hh*DH+d]); } o[(size_t)s*D+hh*DH+d]=f2bf(a);}
}
__global__ void final_k(const u16* x, const u16* gw, const u16* a, const u16* b, const u16* b2, u16* out){
  int s=blockIdx.x; int t=threadIdx.x; __shared__ float red[64];
  float p=0.f; for(int d=t; d<D; d+=64) p+=rbf(bf2f(x[(size_t)s*D+d])*bf2f(gw[d]));
  red[t]=p; __syncthreads();
  float z=0.f; for(int i=0;i<64;++i) z+=red[i];
  z=rbf(z); float g=rbf(1.f/(1.f+expf(-z))); float og=rbf(1.f-g);
  for(int d=t; d<D; d+=64){ float av=bf2f(a[(size_t)s*D+d]); float bv=rbf(bf2f(b[(size_t)s*D+d])+bf2f(b2[d])); out[(size_t)s*D+d]=f2bf(rbf(g*av)+rbf(og*bv)); }
}

extern "C" void kernel_launch(void* const* d_in, const int* in_sizes, int n_in,
                              void* d_out, int out_size, void* d_ws, size_t ws_size,
                              hipStream_t stream) {
  const u16* x=(const u16*)d_in[0]; const u16* wqkv=(const u16*)d_in[1]; const u16* wo=(const u16*)d_in[2];
  const u16* w1=(const u16*)d_in[4]; const u16* b1=(const u16*)d_in[5]; const u16* w2=(const u16*)d_in[6]; const u16* b2=(const u16*)d_in[7]; const u16* gw=(const u16*)d_in[8];
  u16* ws=(u16*)d_ws; u16* qkv=ws; u16* o=qkv+(size_t)S*1344; u16* h=o+(size_t)S*D; u16* a=h+(size_t)S*F; u16* b=a+(size_t)S*D;
  gemm_naive<<<dim3((1344+63)/64,S),64,0,stream>>>(x,wqkv,qkv,S,1344,D);
  gemm_naive<<<dim3((F+63)/64,S),64,0,stream>>>(x,w1,h,S,F,D);
  act_k<<<(S*F+255)/256,256,0,stream>>>(h,b1);
  attn_k<<<(S*H+63)/64,64,0,stream>>>(qkv,o);
  gemm_naive<<<dim3((D+63)/64,S),64,0,stream>>>(o,wo,a,S,D,D);
  gemm_naive<<<dim3((D+63)/64,S),64,0,stream>>>(h,w2,b,S,D,F);
  final_k<<<S,64,0,stream>>>(x,gw,a,b,b2,(u16*)d_out);
}
```

```cpp
#include <hip/hip_runtime.h>
#include <stdint.h>

typedef short bf16x8 __attribute__((ext_vector_type(8)));
typedef short s16x4 __attribute__((ext_vector_type(4)));
typedef float f32x4 __attribute__((ext_vector_type(4)));
typedef unsigned v4u __attribute__((ext_vector_type(4)));
typedef unsigned v2u __attribute__((ext_vector_type(2)));
typedef __bf16 bf2_t __attribute__((ext_vector_type(2)));
typedef float f32x2 __attribute__((ext_vector_type(2)));
#define LAS __attribute__((address_space(3)))
#define GAS __attribute__((address_space(1)))
#define DI __device__ __forceinline__
typedef unsigned long long u64;
typedef __amdgpu_buffer_rsrc_t rsrc_t;
typedef u64 v8q __attribute__((ext_vector_type(8)));
typedef u64 v2q __attribute__((ext_vector_type(2)));

#define CODE_PF 16384
#define B_PF 0
#define WINDOW 2
#define PRIO 1
#define POLL_WAVE 0
#define POLL_SLEEP 4

extern "C" __device__ u64 gm_dispatch_id() __asm("llvm.amdgcn.dispatch.id");

namespace gm {
constexpr int S = 2048, D = 448, F = 1792, NQKV = 1344;
constexpr int KS_A = 16, KS = 72;
constexpr unsigned A2_TT = KS * 1024;
constexpr unsigned WS_G = 128 * A2_TT, WS_FH = WS_G + 8192, WS_FO = WS_FH + 8192, WS_END = WS_FO + 76 * 128;
constexpr unsigned REG = 20480, STG = 6144, SPARE = 3 * STG;
constexpr int LDS_BYTES = 163840;
constexpr unsigned GW_OFF = 5120;
constexpr int QROWS = 114;
constexpr unsigned SPIN_CAP = 1u << 16;

struct Params {
  const void *x, *wqkv, *wo, *w1, *b1, *w2, *b2, *gw;
  void *out, *ws;
};
template <int N> struct IC { static constexpr int value = N; };

DI rsrc_t mk(const void* p, unsigned n) { return __builtin_amdgcn_make_buffer_rsrc((void*)p, 0, n, 0x00020000); }
template <unsigned C> DI void set_m0(unsigned base) { asm volatile("s_add_u32 m0, %0, %1" ::"s"(base), "i"(C) : "scc", "memory"); }
template <unsigned IMM> DI void dma_go(unsigned voff, rsrc_t rs, unsigned soff) {
  asm volatile("buffer_load_dwordx4 %0, %1, %2 offen offset:%3 lds" ::"v"(voff), "s"(rs), "s"(soff), "i"(IMM) : "memory");
}
template <unsigned IMM> DI void dma_go0(unsigned voff, rsrc_t rs) {
  asm volatile("buffer_load_dwordx4 %0, %1, 0 offen offset:%2 lds" ::"v"(voff), "s"(rs), "i"(IMM) : "memory");
}
DI void dma_touch(unsigned voff, rsrc_t rs, unsigned soff) {
  asm volatile("buffer_load_dword %0, %1, %2 offen lds" ::"v"(voff), "s"(rs), "s"(soff) : "memory");
}
#define NOP1() asm volatile("s_nop 0")
DI f32x4 mfma(bf16x8 a, bf16x8 b, f32x4 c) { return __builtin_amdgcn_mfma_f32_16x16x32_bf16(a, b, c, 0, 0, 0); }
DI void mma(f32x4& c, bf16x8 a, bf16x8 b) { asm volatile("v_mfma_f32_16x16x32_bf16 %0, %1, %2, %0" : "+v"(c) : "v"(a), "v"(b)); }
DI void mma0(f32x4& c, bf16x8 a, bf16x8 b) { asm volatile("v_mfma_f32_16x16x32_bf16 %0, %1, %2, 0" : "=&v"(c) : "v"(a), "v"(b)); }
DI void mmac(f32x4& d, bf16x8 a, bf16x8 b, f32x4 c) { asm volatile("v_mfma_f32_16x16x32_bf16 %0, %1, %2, %3" : "=&v"(d) : "v"(a), "v"(b), "v"(c)); }
DI float relu(float v) {
  float r;
  asm("v_max_f32 %0, 0, %1" : "=v"(r) : "v"(v));
  return r;
}
DI float fmx(float a, float b) {
  float r;
  asm("v_max_f32 %0, %1, %2" : "=v"(r) : "v"(a), "v"(b));
  return r;
}
DI unsigned pk(float lo, float hi) {
  f32x2 v = {lo, hi};
  return __builtin_bit_cast(unsigned, __builtin_convertvector(v, bf2_t));
}
DI float rbf(float f) { return __uint_as_float(pk(f, 0.f) << 16); }
DI float bflo(unsigned u) { return __uint_as_float(u << 16); }
DI float bfhi(unsigned u) { return __uint_as_float(u & 0xffff0000u); }
DI bf16x8 asfrag(unsigned a, unsigned b, unsigned c, unsigned d) {
  v4u v = {a, b, c, d};
  return __builtin_bit_cast(bf16x8, v);
}
DI void st128(v4u v, rsrc_t rs, unsigned voff, unsigned soff) {
  __builtin_amdgcn_raw_buffer_store_b128(v, rs, voff, soff, 16);
  __builtin_amdgcn_sched_barrier(0);
  asm volatile("s_nop 1");
  __builtin_amdgcn_sched_barrier(0);
}
DI bf16x8 cat(s16x4 lo, s16x4 hi) { return __builtin_shufflevector(lo, hi, 0, 1, 2, 3, 4, 5, 6, 7); }
DI bf16x8 lds128(LAS char* L, unsigned a) { return *(LAS bf16x8*)(L + a); }
DI s16x4 ldstr(LAS char* L, unsigned a) { return __builtin_amdgcn_ds_read_tr16_b64_v4i16((LAS s16x4*)(L + a)); }
DI unsigned opaque(unsigned v) {
  asm volatile("" : "+v"(v));
  return v;
}
DI float xmax(float v) {
  auto a = __builtin_amdgcn_permlane32_swap(__float_as_uint(v), __float_as_uint(v), false, false);
  v = fmx(__uint_as_float(a[0]), __uint_as_float(a[1]));
  auto b = __builtin_amdgcn_permlane16_swap(__float_as_uint(v), __float_as_uint(v), false, false);
  return fmx(__uint_as_float(b[0]), __uint_as_float(b[1]));
}
DI float xsum(float v) {
  auto a = __builtin_amdgcn_permlane32_swap(__float_as_uint(v), __float_as_uint(v), false, false);
  v = __uint_as_float(a[0]) + __uint_as_float(a[1]);
  auto b = __builtin_amdgcn_permlane16_swap(__float_as_uint(v), __float_as_uint(v), false, false);
  return __uint_as_float(b[0]) + __uint_as_float(b[1]);
}
#define SCHED() __builtin_amdgcn_sched_barrier(0)
#define STAMP(k)
#define STAMP_FLUSH()
#define FSTAMP(k)
template <int N> DI void wait_v() { asm volatile("s_waitcnt vmcnt(%0)" ::"i"(N) : "memory"); }
DI void wait_l() {
  SCHED();
  __builtin_amdgcn_s_waitcnt(0xC07F);
  asm volatile("" ::: "memory");
  SCHED();
}
DI void bar() { asm volatile("s_barrier" ::: "memory"); }

constexpr unsigned hloc(int h) {
  h = h >= 10 ? h - 10 : h;
  return h == 0 ? SPARE : (h < 4 ? STG + (h - 1) * 2048u : (h < 7 ? 2 * STG + (h - 4) * 2048u : (h - 7) * 2048u));
}
constexpr unsigned ppos(int i) { return i < 2 ? i + 1 : (i == 2 ? 0 : i); }
template <int I, int N, class Fn> DI void rep(Fn&& f) {
  if constexpr (I < N) {
    f(IC<I>{});
    rep<I + 1, N>(f);
  }
}

__global__ __launch_bounds__(512, 2) void gm_kernel(Params pin) {
  __shared__ __attribute__((aligned(1024))) char smem[LDS_BYTES];
  LAS char* L = (LAS char*)smem;
  v8q ka;
  v2q kb2;
  asm volatile("s_load_dwordx16 %0, %2, 0x0\n\ts_load_dwordx4 %1, %2, 0x40" : "=&s"(ka), "=&s"(kb2) : "s"(__builtin_amdgcn_kernarg_segment_ptr()));
  const unsigned tid = threadIdx.x, lane = tid & 63;
  const unsigned b = blockIdx.x, xcd = b & 7, slot = b >> 3;
  if (tid < CODE_PF / 1024) {
    u64 pc;
    asm volatile("s_getpc_b64 %0" : "=s"(pc));
    const unsigned vo = ((slot & 7) * (CODE_PF / 1024) + tid) * 128u;
    asm volatile("s_mov_b32 m0, %0\n\ts_nop 0\n\tglobal_load_lds_dword %1, %2" ::"s"(SPARE + 1024u), "v"(vo), "s"(pc) : "memory");
  }
  const unsigned w = __builtin_amdgcn_readfirstlane(tid >> 6);
  const unsigned wreg = w * REG;
  const unsigned li = lane & 15, g = lane >> 4;
  STAMP(0);

  const unsigned rq = xcd >> 1, side = xcd & 1;
  const unsigned nffn = 16 - 4 * side;
  const bool ffn = slot < nffn;
  const unsigned rb = 4 * rq + (slot & 3), cs = side * 4 + (slot >> 2);
  const unsigned jq = slot - nffn;
  const unsigned hp = ((rq & 1) ^ side ^ 1) * 4 + (jq & 3);
  const unsigned rt = 9 * (rq >> 1) + (jq >> 2) + (rq & 1) * (5 - side);
  const bool has2 = slot < 28;

  const unsigned rr = lane >> 3, pos = lane & 7;
  const int row0 = ffn ? (int)(128 * rb) : (int)(QROWS * rt) - 14;
  unsigned vx[2];
#pragma unroll
  for (int h = 0; h < 2; ++h) {
    int r = row0 + (int)(16 * w + 8 * h + rr);
    r = r < 0 ? 0 : (r > S - 1 ? S - 1 : r);
    vx[h] = (unsigned)r * 896u + ((pos ^ (4u * h + (rr >> 1))) << 4);
  }
  const unsigned ldw = ffn ? 3584u : 2688u;
  unsigned wch = pos ^ ((((rr >> 1) & 1) | ((w & 1) << 1)) << 1);
  wch = min(wch, ffn ? 7u : 6u);
  const unsigned vw = (8 * w + rr) * ldw + (wch << 4) + (ffn ? cs * 512u : hp * 112u);
  const unsigned wstep = 64u * ldw;

  SCHED();
  asm volatile("s_waitcnt lgkmcnt(0)" : "+s"(ka), "+s"(kb2));
  Params p;
  p.x = (const void*)ka[0]; p.wqkv = (const void*)ka[1]; p.wo = (const void*)ka[2]; p.w1 = (const void*)ka[3];
  p.b1 = (const void*)ka[4]; p.w2 = (const void*)ka[5]; p.b2 = (const void*)ka[6]; p.gw = (const void*)ka[7];
  p.out = (void*)kb2[0]; p.ws = (void*)kb2[1];
  const rsrc_t rx = mk(p.x, S * D * 2);
  const rsrc_t rw = mk(ffn ? p.w1 : p.wqkv, ffn ? D * F * 2 : D * NQKV * 2);

  auto st_m0 = [&](auto I_, auto ST_, auto WUS_) {
    constexpr int I = decltype(I_)::value, ST = decltype(ST_)::value, WUS = decltype(WUS_)::value;
    constexpr unsigned imm = I < 2 ? ST * 128u : (I - 2) * WUS;
    set_m0<(ST % 3) * STG + ppos(I) * 1024u - imm>(wreg);
  };
  auto st_go = [&](auto I_, auto ST_, auto WUS_) {
    constexpr int I = decltype(I_)::value, ST = decltype(ST_)::value, WUS = decltype(WUS_)::value;
    if constexpr (I < 2) dma_go0<ST * 128u>(vx[I], rx);
    else if constexpr (ST == 0) dma_go0<(I - 2) * WUS>(vw, rw);
    else dma_go<(I - 2) * WUS>(vw, rw, ST * wstep);
  };
  auto st_all = [&](auto ST_) {
    if (ffn) rep<0, 6>([&](auto I_) { st_m0(I_, ST_, IC<128>{}); NOP1(); st_go(I_, ST_, IC<128>{}); });
    else rep<0, 5>([&](auto I_) { st_m0(I_, ST_, IC<896>{}); NOP1(); st_go(I_, ST_, IC<896>{}); });
  };
  st_all(IC<0>{});
  if (w == 0) {
    set_m0<0>(ffn ? SPARE : GW_OFF);
    NOP1();
    dma_go0<0>(lane * 16, mk(ffn ? (const char*)p.b1 + cs * 512u : (const char*)p.gw, ffn ? 512 : 896));
  }
  SCHED();

  const unsigned tb = 4 * xcd + slot / 7, ct = slot % 7;
  const rsrc_t rws = mk(p.ws, WS_END);
  const rsrc_t rbl = mk(w == 0 ? p.wo : p.w2, w == 0 ? D * D * 2 : F * D * 2);
  const rsrc_t rbh = mk(w <= 1 ? p.wo : p.w2, w <= 1 ? D * D * 2 : F * D * 2);
  const unsigned bsl = (w == 0 ? 28u : 32u) * 896u, bsh = (w <= 1 ? 28u : 32u) * 896u;
  const unsigned bol = w <= 1 ? 0u : (9 * w - 16) * 32u * 896u;
  const unsigned boh = w == 1 ? 7u * 28u * 896u : bol;
  unsigned vb[2];
  vb[0] = opaque(rr * 896u + ((pos ^ (((rr >> 1) & 3) << 1)) << 4) + ct * 128u);
  vb[1] = opaque(vb[0] + 8u * 896u);
  auto b_m0 = [&](auto N_) {
    constexpr int N = decltype(N_)::value;
    set_m0<hloc(N >> 1) + (N & 1) * 1024u>(wreg);
  };
  auto b_go = [&](auto N_) {
    constexpr int N = decltype(N_)::value, j = N >> 2;
    if constexpr (j < 2) dma_go<0>(vb[N & 1], rbl, bol + j * bsl + ((N >> 1) & 1) * (16u * 896u));
    else dma_go<0>(vb[N & 1], rbh, boh + j * bsh + ((N >> 1) & 1) * (16u * 896u));
  };
  const rsrc_t rpf = mk(w == 7 ? p.wo : p.w2, w == 7 ? D * D * 2 : F * D * 2);
  const unsigned vpf = opaque(lane < (w == 7 ? 28u : 16u) ? (4 * lane + (tb & 3) + (w == 7 ? 0u : w * 64u)) * 896u + ct * 128u : 0x80000000u);
  const unsigned pfs = (w == 7 ? 112u : 448u) * 896u;
  auto pf_go = [&](auto S_) {
    constexpr int SS = decltype(S_)::value;
    dma_touch(vpf, rpf, SS * pfs);
  };

  const unsigned q4 = (lane >> 2) & 3, p4 = lane & 3;
  const unsigned fsw = (q4 >> 1) | ((g & 1) << 1);
  const unsigned xa0 = 1024u + (li >> 3) * 1024u + (li & 7) * 128u + ((g ^ ((li >> 1) & 7)) << 4);
  unsigned ba[4];
#pragma unroll
  for (int c = 0; c < 4; ++c) ba[c] = opaque(wreg + (4 * g + q4) * 128u + ((2u * (c ^ fsw) + (p4 >> 1)) << 4) + (p4 & 1) * 8u);
  s16x4 bl[4][4], bh[4][4];
  auto b_rd = [&](auto J_, auto C_, auto E_) {
    constexpr int J = decltype(J_)::value, C = decltype(C_)::value, E = decltype(E_)::value;
    if constexpr (E == 0) bl[J & 3][C] = ldstr(L, ba[C] + hloc(2 * J));
    else bh[J & 3][C] = ldstr(L, ba[C] + hloc(2 * J + 1));
  };

  auto sched = [](int hb, int k, int np) -> int {
    const int s = hb >> 1;
    if (hb == 0) return k < np ? 8 * 2 + k : -1;
    if ((hb & 1) == 1) {
      if (s < 4) return k < 3 ? 8 * (s + 3) + k : (k == 3 && B_PF ? 200 + s : -1);
      if (s == 4) return k < 4 ? 100 + k : -1;
      if (s == 5) return k < 3 ? 108 + k : -1;
      return k < 2 ? 114 + k : -1;
    }
    if (s <= 4) return 3 + k < np ? 8 * (s + 2) + 3 + k : -1;
    if (s == 5) return k < 4 ? 104 + k : -1;
    return k < 3 ? 111 + k : -1;
  };
  auto d_m0 = [&](auto E_, auto WUS_) {
    constexpr int E = decltype(E_)::value;
    if constexpr (E >= 200) set_m0<SPARE + 1024u>(wreg);
    else if constexpr (E >= 100) b_m0(IC<E - 100>{});
    else if constexpr (E >= 0) st_m0(IC<(E & 7)>{}, IC<(E >> 3)>{}, WUS_);
  };
  auto d_go = [&](auto E_, auto WUS_) {
    constexpr int E = decltype(E_)::value;
    if constexpr (E >= 200) pf_go(IC<E - 200>{});
    else if constexpr (E >= 100) b_go(IC<E - 100>{});
    else if constexpr (E >= 0) st_go(IC<(E & 7)>{}, IC<(E >> 3)>{}, WUS_);
  };

  if (ffn) {
    const unsigned mh = w >> 2, nq = w & 3;
    unsigned xa[2][2], wa[2][4];
#pragma unroll
    for (int hs = 0; hs < 2; ++hs) {
#pragma unroll
      for (int t2 = 0; t2 < 2; ++t2) xa[hs][t2] = opaque((xa0 ^ (hs * 64u)) + (mh * 4u + t2 * 2u) * REG);
#pragma unroll
      for (int c = 0; c < 4; ++c)
        wa[hs][c] = opaque((4u * hs + g) * REG + (nq ? 2u + nq : 0u) * 1024u + q4 * 128u + ((2u * (c ^ fsw) + (p4 >> 1)) << 4) + (p4 & 1) * 8u);
    }
    const unsigned so = (8 * rb + 4 * mh) * A2_TT + (KS_A + 8 * cs + 2 * nq) * 1024u;
    SCHED();
    bar();
    rep<0, 6>([&](auto I_) { st_m0(I_, IC<1>{}, IC<128>{}); NOP1(); st_go(I_, IC<1>{}, IC<128>{}); });
    wait_v<6>();
    bar();
    STAMP(1);
    bf16x8 xf[2][4];
    s16x4 wl[2][4], wh[2][4];
    f32x4 acc[4][4];
    auto rd = [&](auto I_, auto HB_) {
      constexpr int I = decltype(I_)::value, HB = decltype(HB_)::value, hs = HB & 1;
      constexpr unsigned bo = ((HB >> 1) % 3) * STG;
      if constexpr (I >= 2 && I < 6) xf[hs][I - 2] = lds128(L, xa[hs][(I - 2) >> 1] + ((I - 2) & 1) * REG + bo);
      else if constexpr (I < 12) {
        constexpr int c = I < 2 ? 0 : (I - 4) >> 1;
        if constexpr ((I & 1) == 0) wl[hs][c] = ldstr(L, wa[hs][c] + bo);
        else wh[hs][c] = ldstr(L, wa[hs][c] + bo + 512u);
      }
    };
    rep<0, 12>([&](auto I_) { rd(I_, IC<0>{}); });
    f32x4 bv[4];
#pragma unroll
    for (int c = 0; c < 4; ++c) {
      const v2u bb = *(LAS v2u*)(L + SPARE + nq * 128u + c * 32u + g * 8u);
      bv[c] = f32x4{bflo(bb.x), bfhi(bb.x), bflo(bb.y), bfhi(bb.y)};
      asm volatile("" : "+v"(bv[c]));
    }
    auto epi = [&](int t, int u) {
      float hv[8];
#pragma unroll
      for (int j = 0; j < 8; ++j) {
        const float r = relu(acc[2 * u + (j >> 2)][t][j & 3]);
        hv[j] = r * r;
      }
      const v4u o = {pk(hv[0], hv[1]), pk(hv[2], hv[3]), pk(hv[4], hv[5]), pk(hv[6], hv[7])};
      st128(o, rws, lane * 16u + u * 1024u, so + t * A2_TT);
    };
    rep<0, 14>([&](auto HB_) {
      constexpr int HB = decltype(HB_)::value, hs = HB & 1, s = HB >> 1;
      if constexpr (hs == 0) {
        wait_l();
        if constexpr (HB == 0) asm volatile("s_nop 1");
      }
      rep<0, 16>([&](auto I_) {
        constexpr int I = decltype(I_)::value;
        constexpr int K = HB == 0 ? ((I & 1) == 0 ? I >> 1 : -1) : ((I & 3) == 1 ? I >> 2 : -1);
        constexpr int E = K >= 0 ? sched(HB, K, 6) : -1;
        d_m0(IC<E>{}, IC<128>{});
        if constexpr (HB < 13) {
          if constexpr (HB == 0) mmac(acc[I >> 2][I & 3], cat(wl[hs][I >> 2], wh[hs][I >> 2]), xf[hs][I & 3], bv[I >> 2]);
          else mma(acc[I >> 2][I & 3], cat(wl[hs][I >> 2], wh[hs][I >> 2]), xf[hs][I & 3]);
          SCHED();
          rd(I_, IC<HB + 1>{});
        } else {
          mma(acc[I & 3][I >> 2], cat(wl[hs][I & 3], wh[hs][I & 3]), xf[hs][I >> 2]);
          SCHED();
          if constexpr (I < 4) rep<0, 4>([&](auto Q_) { constexpr int Q = decltype(Q_)::value; b_rd(IC<(Q >> 1)>{}, I_, IC<(Q & 1)>{}); });
          if constexpr (I >= 6 && (I & 1) == 0) epi((I - 6) >> 2, ((I - 6) >> 1) & 1);
        }
        SCHED();
        d_go(IC<E>{}, IC<128>{});
      });
      if constexpr (hs == 0) {
        FSTAMP(16 + 3 * s);
        wait_v<(s == 5 ? 8 : 6)>();
        wait_l();
        FSTAMP(17 + 3 * s);
        bar();
        FSTAMP(18 + 3 * s);
      }
    });
    STAMP(2);
    asm volatile("s_nop 7" ::: "memory");
    SCHED();
    epi(2, 1);
    epi(3, 0);
    epi(3, 1);
  } else {
    const unsigned tq = w & 3, hd = w >> 2;
    unsigned xa[2], wa[2][2];
#pragma unroll
    for (int hs = 0; hs < 2; ++hs) {
      xa[hs] = opaque((xa0 ^ (hs * 64u)) + tq * 2u * REG);
#pragma unroll
      for (int c = 0; c < 2; ++c) {
        const unsigned bo = 56u * hd + 32u * c + 8u * p4;
        wa[hs][c] = opaque((4u * hs + g) * REG + q4 * 128u + (((bo >> 4) ^ (fsw << 1)) << 4) + (bo & 8u));
      }
    }
    const unsigned ga = opaque(GW_OFF + g * 16u);
    float mb[2][4];
    unsigned oa[2];
#pragma unroll
    for (int t = 0; t < 2; ++t) {
#pragma unroll
      for (int j = 0; j < 4; ++j) {
        const int kap = 16 * (j >> 1) + (int)(4 * g) + 2 * (j & 1) + (int)(li & 1) - 16;
        const int dl = (int)li - kap;
        const bool ok = (dl >= 0) && (dl <= 14) && (row0 + (int)(32 * tq) + 16 * t + kap >= 0);
        mb[t][j] = __uint_as_float(opaque(ok ? 0u : 0xff800000u));
      }
      const int lr = (int)(32 * tq + li) + 16 * t, tok = row0 + lr;
      oa[t] = opaque((lr >= 14 && tok < S) ? (unsigned)(tok >> 4) * A2_TT + (g * 16u + (unsigned)(tok & 15)) * 16u : 0x80000000u);
    }
    SCHED();
    bar();
    rep<0, 5>([&](auto I_) { st_m0(I_, IC<1>{}, IC<896>{}); NOP1(); st_go(I_, IC<1>{}, IC<896>{}); });
    wait_v<5>();
    bar();
    STAMP(1);
    f32x4 ac[7][2];
    bf16x8 xf[2][2], gf[2];
    s16x4 wl[2][6], wh[2][6];
    auto rd = [&](auto I_, auto HB_) {
      constexpr int I = decltype(I_)::value, HB = decltype(HB_)::value, hs = HB & 1;
      constexpr unsigned bo = ((HB >> 1) % 3) * STG;
      if constexpr (I == 2 || I == 3) xf[hs][I - 2] = lds128(L, xa[hs] + (I - 2) * REG + bo);
      else if constexpr (I < 14) {
        constexpr int c = I < 2 ? 0 : (I - 2) >> 1;
        if constexpr ((I & 1) == 0) wl[hs][c] = ldstr(L, wa[hs][c & 1] + ppos(2 + (c >> 1)) * 1024u + bo);
        else wh[hs][c] = ldstr(L, wa[hs][c & 1] + ppos(2 + (c >> 1)) * 1024u + bo + 512u);
      } else if constexpr (I == 14) gf[hs] = lds128(L, ga + HB * 64u);
    };
    rep<0, 15>([&](auto I_) { rd(I_, IC<0>{}); });
    rep<0, 14>([&](auto HB_) {
      constexpr int HB = decltype(HB_)::value, hs = HB & 1, s = HB >> 1;
      if constexpr (hs == 0) wait_l();
      rep<0, 14>([&](auto I_) {
        constexpr int I = decltype(I_)::value, c = I >> 1, t = I & 1;
        constexpr int K = HB == 0 ? ((I & 1) == 0 ? I >> 1 : -1) : (I % 3 == 1 ? I / 3 : -1);
        constexpr int E = K >= 0 ? sched(HB, K, 5) : -1;
        d_m0(IC<E>{}, IC<896>{});
        const bf16x8 wf = c < 6 ? cat(wl[hs][c < 6 ? c : 0], wh[hs][c < 6 ? c : 0]) : gf[hs];
        if constexpr (HB == 0) {
          if constexpr (c == 4 || c == 5) mma0(ac[c][t], xf[hs][t], wf);
          else mma0(ac[c][t], wf, xf[hs][t]);
        } else {
          if constexpr (c == 4 || c == 5) mma(ac[c][t], xf[hs][t], wf);
          else mma(ac[c][t], wf, xf[hs][t]);
        }
        SCHED();
        if constexpr (HB < 13) {
          if constexpr (I < 5) { rd(IC<2 * I>{}, IC<HB + 1>{}); rd(IC<2 * I + 1>{}, IC<HB + 1>{}); }
          else rd(IC<I + 5>{}, IC<HB + 1>{});
        } else if constexpr (I < 8) {
          b_rd(IC<(I >> 2)>{}, IC<(I & 3)>{}, IC<0>{});
          b_rd(IC<(I >> 2)>{}, IC<(I & 3)>{}, IC<1>{});
        }
        SCHED();
        d_go(IC<E>{}, IC<896>{});
      });
      if constexpr (hs == 0) {
        FSTAMP(16 + 3 * s);
        wait_v<(s == 5 ? 8 : (s == 6 ? 6 : 5))>();
        wait_l();
        FSTAMP(17 + 3 * s);
        bar();
        FSTAMP(18 + 3 * s);
      }
    });
    STAMP(2);
    asm volatile("s_nop 7" ::: "memory");
    SCHED();
    const unsigned pm = g == 3 ? 0u : 0xffffffffu;
    bf16x8 qf[2], kf[2];
    unsigned vh[2][2][2];
#pragma unroll
    for (int t = 1; t >= 0; --t) {
      kf[t] = asfrag(pk(ac[2][t][0], ac[2][t][1]), pk(ac[2][t][2], ac[2][t][3]), pm & pk(ac[3][t][0], ac[3][t][1]), pm & pk(ac[3][t][2], ac[3][t][3]));
#pragma unroll
      for (int c = 0; c < 2; ++c) {
        vh[t][c][0] = pk(ac[4 + c][t][0], ac[4 + c][t][1]);
        vh[t][c][1] = pk(ac[4 + c][t][2], ac[4 + c][t][3]);
      }
      if (t == 1) {
        const unsigned nw = ((w + 1) & 7) * REG;
        *(LAS bf16x8*)(L + nw + 4096u + lane * 16u) = kf[1];
        *(LAS v4u*)(L + nw + 5120u + lane * 16u) = v4u{vh[1][0][0], vh[1][0][1], vh[1][1][0], vh[1][1][1]};
      }
      qf[t] = asfrag(pk(ac[0][t][0], ac[0][t][1]), pk(ac[0][t][2], ac[0][t][3]), pm & pk(ac[1][t][0], ac[1][t][1]), pm & pk(ac[1][t][2], ac[1][t][3]));
    }
    wait_l();
    bar();
    const bf16x8 kp = lds128(L, wreg + 4096u + lane * 16u);
    const v4u vp = *(LAS v4u*)(L + wreg + 5120u + lane * 16u);
    const unsigned head = 2 * hp + hd;
    const bool odd = li & 1;
    const unsigned psh = odd ? 16u : 0u;
#pragma unroll
    for (int t = 1; t >= 0; --t) {
      const f32x4 z = {0.f, 0.f, 0.f, 0.f};
      const f32x4 s0 = mfma(t == 0 ? kp : kf[0], qf[t], z);
      const f32x4 s1 = mfma(kf[t], qf[t], z);
      const float gv = rbf(__builtin_amdgcn_rcpf(1.0f + __builtin_amdgcn_exp2f(rbf(ac[6][t][0]) * -1.4426950408889634f)));
      float sv[4];
      sv[0] = odd ? s0[1] : s0[0];
      sv[1] = odd ? s0[3] : s0[2];
      sv[2] = odd ? s1[1] : s1[0];
      sv[3] = odd ? s1[3] : s1[2];
      const float SC = 0.18898223650461362f * 1.4426950408889634f;
#pragma unroll
      for (int j = 0; j < 4; ++j) sv[j] = __builtin_fmaf(sv[j], SC, mb[t][j]);
      const float m = xmax(fmaxf(fmaxf(sv[0], sv[1]), fmaxf(sv[2], sv[3])));
#pragma unroll
      for (int j = 0; j < 4; ++j) sv[j] = __builtin_amdgcn_exp2f(sv[j] - m);
      const float inv = __builtin_amdgcn_rcpf(xsum((sv[0] + sv[1]) + (sv[2] + sv[3])));
      const bf16x8 pf = asfrag(pk(sv[0] * inv, 0.f) << psh, pk(sv[1] * inv, 0.f) << psh, pk(sv[2] * inv, 0.f) << psh, pk(sv[3] * inv, 0.f) << psh);
      f32x4 o[2];
#pragma unroll
      for (int c = 0; c < 2; ++c) {
        const bf16x8 vf = t == 0 ? asfrag(vp[2 * c], vp[2 * c + 1], vh[0][c][0], vh[0][c][1]) : asfrag(vh[0][c][0], vh[0][c][1], vh[1][c][0], vh[1][c][1]);
        o[c] = mfma(vf, pf, z) * gv;
      }
      const v4u ov = {pk(o[0][0], o[0][1]), pk(o[0][2], o[0][3]), pm & pk(o[1][0], o[1][1]), pm & pk(o[1][2], o[1][3])};
      st128(ov, rws, oa[t], head * 1024u);
      if (head == 0) {
        const unsigned tok = (unsigned)(row0 + (int)(32 * tq + li) + 16 * t);
        __builtin_amdgcn_raw_buffer_store_b32(__float_as_uint(gv), rws, (g == 0 && oa[t] < 0x80000000u) ? tok * 4u : 0x80000000u, WS_G, 16);
      }
    }
  }
  STAMP(3);
  const u64 tag = (gm_dispatch_id() + 1) * 0x9E3779B97F4A7C15ull ^ (u64)__builtin_amdgcn_queue_ptr();
  const unsigned fo_pub = ffn ? WS_FH + (2 * rb + (w >> 2)) * 256u + (4 * cs + (w & 3)) * 8u : WS_FO + (4 * rt + (w & 3)) * 128u + (2 * hp + (w >> 2)) * 8u;
  wait_v<0>();
  STAMP(4);
  if (lane == 0) __hip_atomic_store((GAS u64*)((char*)p.ws + fo_pub), tag, __ATOMIC_RELAXED, __HIP_MEMORY_SCOPE_AGENT);
  if (!has2) {
    STAMP_FLUSH();
    return;
  }
  rep<0, 16>([&](auto I_) { constexpr int I = decltype(I_)::value; b_rd(IC<2 + (I >> 3)>{}, IC<((I >> 1) & 3)>{}, IC<(I & 1)>{}); });
  SCHED();
  bar();
  wait_l();
  rep<16, 36>([&](auto N_) { b_m0(N_); NOP1(); b_go(N_); });
  STAMP(5);

  const unsigned T0 = 64 * tb;
  if (w == POLL_WAVE) {
    const unsigned g0 = 4 * (T0 / QROWS) + (T0 % QROWS + 14) / 32;
    const unsigned T1 = T0 + 63;
    const unsigned g1 = 4 * (T1 / QROWS) + (T1 % QROWS + 14) / 32;
    GAS u64* fa = (GAS u64*)((char*)p.ws + WS_FO + min(g0 + g, g1) * 128u + li * 8u);
    GAS u64* fh = (GAS u64*)((char*)p.ws + WS_FH + tb * 256u + min(lane, 27u) * 8u);
#pragma unroll 1
    for (unsigned spin = 0; spin < SPIN_CAP; ++spin) {
      const u64 v0 = __hip_atomic_load(fa, __ATOMIC_RELAXED, __HIP_MEMORY_SCOPE_AGENT);
      const u64 v1 = __hip_atomic_load(fh, __ATOMIC_RELAXED, __HIP_MEMORY_SCOPE_AGENT);
      if (__all(v0 == tag && v1 == tag)) break;
      __builtin_amdgcn_s_sleep(POLL_SLEEP);
    }
  }
  unsigned va[4];
#pragma unroll
  for (int t = 0; t < 4; ++t) va[t] = opaque(lane * 16u + t * A2_TT);
  const unsigned aol = 4 * tb * A2_TT + (w == 1 ? 16u : 9u * w) * 1024u, aoh = 4 * tb * A2_TT + (w == 1 ? 7u : 9u * w) * 1024u;
  SCHED();
  bar();
  STAMP(6);
  auto a_load = [&](int j, int t) {
    return j < 2 ? __builtin_amdgcn_raw_buffer_load_b128(rws, va[t] + j * 1024u, aol, 16)
                 : __builtin_amdgcn_raw_buffer_load_b128(rws, va[t] + ((j - 2) & 3) * 1024u, aoh + (j < 6 ? 2048u : 6144u), 16);
  };
  constexpr int WIN = WINDOW;
  v4u af[WIN][4];
  float gq[4] = {0.f, 0.f, 0.f, 0.f};
  if (PRIO) __builtin_amdgcn_s_setprio(3);
#pragma unroll
  for (int j = 0; j < WIN; ++j) {
#pragma unroll
    for (int t = 0; t < 4; ++t) af[j][t] = a_load(j, t);
    if (j == 1 && w == 1) {
#pragma unroll
      for (int t = 0; t < 4; ++t) gq[t] = __uint_as_float(__builtin_amdgcn_raw_buffer_load_b32(rws, (T0 + 16 * t + li) * 4u, WS_G, 16));
    }
    SCHED();
  }
  f32x4 acc[4][4];
  v4u b2v;
  rep<0, 9>([&](auto J_) {
    constexpr int j = decltype(J_)::value;
    if (PRIO && j + WIN == 4) __builtin_amdgcn_s_setprio(2);
    if (PRIO && j + WIN == 6) __builtin_amdgcn_s_setprio(1);
    if (PRIO && j + WIN == 8) __builtin_amdgcn_s_setprio(0);
    if (j == 2 && w == 1) {
      asm volatile("s_nop 7" ::: "memory");
      SCHED();
#pragma unroll
      for (int t = 0; t < 4; ++t) {
        const float og = rbf(1.0f - gq[t]);
#pragma unroll
        for (int c = 0; c < 4; ++c) acc[c][t] *= og;
      }
      SCHED();
      asm volatile("s_nop 1" ::: "memory");
    }
    if constexpr (j >= 4) wait_l();
    rep<0, 4>([&](auto T_) {
      constexpr int t = decltype(T_)::value;
      const bf16x8 a = __builtin_bit_cast(bf16x8, af[j % WIN][t]);
#pragma unroll
      for (int c = 0; c < 4; ++c) {
        if constexpr (j == 0) mma0(acc[c][t], cat(bl[0][c], bh[0][c]), a);
        else mma(acc[c][t], cat(bl[j & 3][c], bh[j & 3][c]), a);
      }
      SCHED();
      if constexpr (j + WIN < 9) af[j % WIN][t] = a_load(j + WIN, t);
      if constexpr (j >= 1 && j + 3 < 9) {
        b_rd(IC<j + 3>{}, T_, IC<0>{});
        b_rd(IC<j + 3>{}, T_, IC<1>{});
      }
      SCHED();
    });
    if constexpr (j == 6) b2v = *(const GAS v4u*)((const char*)p.b2 + ct * 128u + (tid & 7) * 16u);
  });
  STAMP(7);
  asm volatile("s_nop 7" ::: "memory");
  SCHED();
#pragma unroll
  for (int t = 0; t < 4; ++t)
#pragma unroll
    for (int c = 0; c < 4; ++c) {
      const unsigned lp = g * 16u + ((li + 4u * ((2u * c + (g >> 1)) & 3u)) & 15u);
      *(LAS f32x4*)(L + wreg + (t * 4 + c) * 1024u + lp * 16u) = acc[c][t];
    }
  if (w == 1 && g == 0) {
#pragma unroll
    for (int t = 0; t < 4; ++t) *(LAS float*)(L + REG + SPARE + (t * 16 + li) * 4u) = gq[t];
  }
  wait_l();
  bar();
  STAMP(8);
  {
    const unsigned tk = tid >> 3, q = tid & 7;
    const unsigned n = (tk >> 4) * 4u + (q >> 1), g0 = 2 * (q & 1), i = tk & 15;
    const unsigned ad = n * 1024u + (g0 * 16u + ((i + 4u * (q & 3u)) & 15u)) * 16u;
    f32x4 alo = *(LAS f32x4*)(L + ad) + *(LAS f32x4*)(L + ad + REG);
    f32x4 ahi = *(LAS f32x4*)(L + ad + 256u) + *(LAS f32x4*)(L + ad + REG + 256u);
    f32x4 lo = {bflo(b2v[0]), bfhi(b2v[0]), bflo(b2v[1]), bfhi(b2v[1])}, hi = {bflo(b2v[2]), bfhi(b2v[2]), bflo(b2v[3]), bfhi(b2v[3])};
#pragma unroll
    for (int k = 2; k < 8; ++k) {
      lo += *(LAS f32x4*)(L + k * REG + ad);
      hi += *(LAS f32x4*)(L + k * REG + ad + 256u);
    }
    const float og = rbf(1.0f - *(LAS float*)(L + REG + SPARE + tk * 4u));
    alo += og * lo;
    ahi += og * hi;
    const v4u o = {pk(alo[0], alo[1]), pk(alo[2], alo[3]), pk(ahi[0], ahi[1]), pk(ahi[2], ahi[3])};
    *(GAS v4u*)((char*)p.out + (64 * tb + tk) * 896u + ct * 128u + q * 16u) = o;
  }
  STAMP(9);
  STAMP_FLUSH();
}
}

extern "C" void kernel_launch(void* const* d_in, const int* in_sizes, int n_in, void* d_out, int out_size, void* d_ws, size_t ws_size,
                              hipStream_t stream) {
  gm::Params p;
  p.x = d_in[0];
  p.wqkv = d_in[1];
  p.wo = d_in[2];
  p.w1 = d_in[4];
  p.b1 = d_in[5];
  p.w2 = d_in[6];
  p.b2 = d_in[7];
  p.gw = d_in[8];
  p.out = d_out;
  p.ws = d_ws;
  gm::gm_kernel<<<256, 512, 0, stream>>>(p);
}
```

```cpp
#include <hip/hip_runtime.h>
#include <stdint.h>
#include <stdio.h>

typedef short bf16x8 __attribute__((ext_vector_type(8)));
typedef short s16x4 __attribute__((ext_vector_type(4)));
typedef float f32x4 __attribute__((ext_vector_type(4)));
typedef unsigned v4u __attribute__((ext_vector_type(4)));
typedef unsigned v2u __attribute__((ext_vector_type(2)));
typedef __bf16 bf2_t __attribute__((ext_vector_type(2)));
typedef float f32x2 __attribute__((ext_vector_type(2)));
#define LAS __attribute__((address_space(3)))
#define GAS __attribute__((address_space(1)))
#define DI __device__ __forceinline__
typedef unsigned long long u64;
typedef __amdgpu_buffer_rsrc_t rsrc_t;
typedef u64 v8q __attribute__((ext_vector_type(8)));
typedef u64 v2q __attribute__((ext_vector_type(2)));

#define CODE_PF 16384
#define B_PF 0
#define WINDOW 2
#define PRIO 1
#define POLL_WAVE 0
#define POLL_SLEEP 4

extern "C" __device__ u64 gm_dispatch_id() __asm("llvm.amdgcn.dispatch.id");

namespace gm {
constexpr int S = 2048, D = 448, F = 1792, NQKV = 1344;
constexpr int KS_A = 16, KS = 72;
constexpr unsigned A2_TT = KS * 1024;
constexpr unsigned WS_G = 128 * A2_TT, WS_FH = WS_G + 8192, WS_FO = WS_FH + 8192, WS_END = WS_FO + 76 * 128;
constexpr unsigned REG = 20480, STG = 6144, SPARE = 3 * STG;
constexpr int LDS_BYTES = 163840;
constexpr unsigned GW_OFF = 5120;
constexpr int QROWS = 114;
constexpr unsigned SPIN_CAP = 1u << 16;

struct Params {
  const void *x, *wqkv, *wo, *w1, *b1, *w2, *b2, *gw;
  void *out, *ws;
};
template <int N> struct IC { static constexpr int value = N; };

DI rsrc_t mk(const void* p, unsigned n) { return __builtin_amdgcn_make_buffer_rsrc((void*)p, 0, n, 0x00020000); }
template <unsigned C> DI void set_m0(unsigned base) { asm volatile("s_add_u32 m0, %0, %1" ::"s"(base), "i"(C) : "scc", "memory"); }
template <unsigned IMM> DI void dma_go(unsigned voff, rsrc_t rs, unsigned soff) {
  asm volatile("buffer_load_dwordx4 %0, %1, %2 offen offset:%3 lds" ::"v"(voff), "s"(rs), "s"(soff), "i"(IMM) : "memory");
}
template <unsigned IMM> DI void dma_go0(unsigned voff, rsrc_t rs) {
  asm volatile("buffer_load_dwordx4 %0, %1, 0 offen offset:%2 lds" ::"v"(voff), "s"(rs), "i"(IMM) : "memory");
}
DI void dma_touch(unsigned voff, rsrc_t rs, unsigned soff) {
  asm volatile("buffer_load_dword %0, %1, %2 offen lds" ::"v"(voff), "s"(rs), "s"(soff) : "memory");
}
#define NOP1() asm volatile("s_nop 0")
DI f32x4 mfma(bf16x8 a, bf16x8 b, f32x4 c) { return __builtin_amdgcn_mfma_f32_16x16x32_bf16(a, b, c, 0, 0, 0); }
DI void mma(f32x4& c, bf16x8 a, bf16x8 b) { asm volatile("v_mfma_f32_16x16x32_bf16 %0, %1, %2, %0" : "+v"(c) : "v"(a), "v"(b)); }
DI void mma0(f32x4& c, bf16x8 a, bf16x8 b) { asm volatile("v_mfma_f32_16x16x32_bf16 %0, %1, %2, 0" : "=&v"(c) : "v"(a), "v"(b)); }
DI void mmac(f32x4& d, bf16x8 a, bf16x8 b, f32x4 c) { asm volatile("v_mfma_f32_16x16x32_bf16 %0, %1, %2, %3" : "=&v"(d) : "v"(a), "v"(b), "v"(c)); }
DI float relu(float v) {
  float r;
  asm("v_max_f32 %0, 0, %1" : "=v"(r) : "v"(v));
  return r;
}
DI float fmx(float a, float b) {
  float r;
  asm("v_max_f32 %0, %1, %2" : "=v"(r) : "v"(a), "v"(b));
  return r;
}
DI unsigned pk(float lo, float hi) {
  f32x2 v = {lo, hi};
  return __builtin_bit_cast(unsigned, __builtin_convertvector(v, bf2_t));
}
DI float rbf(float f) { return __uint_as_float(pk(f, 0.f) << 16); }
DI float bflo(unsigned u) { return __uint_as_float(u << 16); }
DI float bfhi(unsigned u) { return __uint_as_float(u & 0xffff0000u); }
DI bf16x8 asfrag(unsigned a, unsigned b, unsigned c, unsigned d) {
  v4u v = {a, b, c, d};
  return __builtin_bit_cast(bf16x8, v);
}
DI void st128(v4u v, rsrc_t rs, unsigned voff, unsigned soff) {
  __builtin_amdgcn_raw_buffer_store_b128(v, rs, voff, soff, 16);
  __builtin_amdgcn_sched_barrier(0);
  asm volatile("s_nop 1");
  __builtin_amdgcn_sched_barrier(0);
}
DI bf16x8 cat(s16x4 lo, s16x4 hi) { return __builtin_shufflevector(lo, hi, 0, 1, 2, 3, 4, 5, 6, 7); }
DI bf16x8 lds128(LAS char* L, unsigned a) { return *(LAS bf16x8*)(L + a); }
DI s16x4 ldstr(LAS char* L, unsigned a) { return __builtin_amdgcn_ds_read_tr16_b64_v4i16((LAS s16x4*)(L + a)); }
DI unsigned opaque(unsigned v) {
  asm volatile("" : "+v"(v));
  return v;
}
DI float xmax(float v) {
  auto a = __builtin_amdgcn_permlane32_swap(__float_as_uint(v), __float_as_uint(v), false, false);
  v = fmx(__uint_as_float(a[0]), __uint_as_float(a[1]));
  auto b = __builtin_amdgcn_permlane16_swap(__float_as_uint(v), __float_as_uint(v), false, false);
  return fmx(__uint_as_float(b[0]), __uint_as_float(b[1]));
}
DI float xsum(float v) {
  auto a = __builtin_amdgcn_permlane32_swap(__float_as_uint(v), __float_as_uint(v), false, false);
  v = __uint_as_float(a[0]) + __uint_as_float(a[1]);
  auto b = __builtin_amdgcn_permlane16_swap(__float_as_uint(v), __float_as_uint(v), false, false);
  return __uint_as_float(b[0]) + __uint_as_float(b[1]);
}
#define SCHED() __builtin_amdgcn_sched_barrier(0)
#define STAMP(k)
#define STAMP_FLUSH()
#define FSTAMP(k)
template <int N> DI void wait_v() { asm volatile("s_waitcnt vmcnt(%0)" ::"i"(N) : "memory"); }
DI void wait_l() {
  SCHED();
  __builtin_amdgcn_s_waitcnt(0xC07F);
  asm volatile("" ::: "memory");
  SCHED();
}
DI void bar() { asm volatile("s_barrier" ::: "memory"); }

constexpr unsigned hloc(int h) {
  h = h >= 10 ? h - 10 : h;
  return h == 0 ? SPARE : (h < 4 ? STG + (h - 1) * 2048u : (h < 7 ? 2 * STG + (h - 4) * 2048u : (h - 7) * 2048u));
}
constexpr unsigned ppos(int i) { return i < 2 ? i + 1 : (i == 2 ? 0 : i); }
template <int I, int N, class Fn> DI void rep(Fn&& f) {
  if constexpr (I < N) {
    f(IC<I>{});
    rep<I + 1, N>(f);
  }
}

__global__ __launch_bounds__(512, 2) void gm_kernel(Params pin) {
  __shared__ __attribute__((aligned(1024))) char smem[LDS_BYTES];
  LAS char* L = (LAS char*)smem;
  v8q ka;
  v2q kb2;
  asm volatile("s_load_dwordx16 %0, %2, 0x0\n\ts_load_dwordx4 %1, %2, 0x40" : "=&s"(ka), "=&s"(kb2) : "s"(__builtin_amdgcn_kernarg_segment_ptr()));
  const unsigned tid = threadIdx.x, lane = tid & 63;
  const unsigned b = blockIdx.x, xcd = b & 7, slot = b >> 3;
  if (tid < CODE_PF / 1024) {
    u64 pc;
    asm volatile("s_getpc_b64 %0" : "=s"(pc));
    const unsigned vo = ((slot & 7) * (CODE_PF / 1024) + tid) * 128u;
    asm volatile("s_mov_b32 m0, %0\n\ts_nop 0\n\tglobal_load_lds_dword %1, %2" ::"s"(SPARE + 1024u), "v"(vo), "s"(pc) : "memory");
  }
  const unsigned w = __builtin_amdgcn_readfirstlane(tid >> 6);
  const unsigned wreg = w * REG;
  const unsigned li = lane & 15, g = lane >> 4;
  STAMP(0);

  const unsigned rq = xcd >> 1, side = xcd & 1;
  const unsigned nffn = 16 - 4 * side;
  const bool ffn = slot < nffn;
  const unsigned rb = 4 * rq + (slot & 3), cs = side * 4 + (slot >> 2);
  const unsigned jq = slot - nffn;
  const unsigned hp = ((rq & 1) ^ side ^ 1) * 4 + (jq & 3);
  const unsigned rt = 9 * (rq >> 1) + (jq >> 2) + (rq & 1) * (5 - side);
  const bool has2 = slot < 28;

  const unsigned rr = lane >> 3, pos = lane & 7;
  const int row0 = ffn ? (int)(128 * rb) : (int)(QROWS * rt) - 14;
  unsigned vx[2];
#pragma unroll
  for (int h = 0; h < 2; ++h) {
    int r = row0 + (int)(16 * w + 8 * h + rr);
    r = r < 0 ? 0 : (r > S - 1 ? S - 1 : r);
    vx[h] = (unsigned)r * 896u + ((pos ^ (4u * h + (rr >> 1))) << 4);
  }
  const unsigned ldw = ffn ? 3584u : 2688u;
  unsigned wch = pos ^ ((((rr >> 1) & 1) | ((w & 1) << 1)) << 1);
  wch = min(wch, ffn ? 7u : 6u);
  const unsigned vw = (8 * w + rr) * ldw + (wch << 4) + (ffn ? cs * 512u : hp * 112u);
  const unsigned wstep = 64u * ldw;

  SCHED();
  asm volatile("s_waitcnt lgkmcnt(0)" : "+s"(ka), "+s"(kb2));
  Params p;
  p.x = (const void*)ka[0]; p.wqkv = (const void*)ka[1]; p.wo = (const void*)ka[2]; p.w1 = (const void*)ka[3];
  p.b1 = (const void*)ka[4]; p.w2 = (const void*)ka[5]; p.b2 = (const void*)ka[6]; p.gw = (const void*)ka[7];
  p.out = (void*)kb2[0]; p.ws = (void*)kb2[1];
  const rsrc_t rx = mk(p.x, S * D * 2);
  const rsrc_t rw = mk(ffn ? p.w1 : p.wqkv, ffn ? D * F * 2 : D * NQKV * 2);

  auto st_m0 = [&](auto I_, auto ST_, auto WUS_) {
    constexpr int I = decltype(I_)::value, ST = decltype(ST_)::value, WUS = decltype(WUS_)::value;
    constexpr unsigned imm = I < 2 ? ST * 128u : (I - 2) * WUS;
    set_m0<(ST % 3) * STG + ppos(I) * 1024u - imm>(wreg);
  };
  auto st_go = [&](auto I_, auto ST_, auto WUS_) {
    constexpr int I = decltype(I_)::value, ST = decltype(ST_)::value, WUS = decltype(WUS_)::value;
    if constexpr (I < 2) dma_go0<ST * 128u>(vx[I], rx);
    else if constexpr (ST == 0) dma_go0<(I - 2) * WUS>(vw, rw);
    else dma_go<(I - 2) * WUS>(vw, rw, ST * wstep);
  };
  auto st_all = [&](auto ST_) {
    if (ffn) rep<0, 6>([&](auto I_) { st_m0(I_, ST_, IC<128>{}); NOP1(); st_go(I_, ST_, IC<128>{}); });
    else rep<0, 5>([&](auto I_) { st_m0(I_, ST_, IC<896>{}); NOP1(); st_go(I_, ST_, IC<896>{}); });
  };
  st_all(IC<0>{});
  if (w == 0) {
    set_m0<0>(ffn ? SPARE : GW_OFF);
    NOP1();
    dma_go0<0>(lane * 16, mk(ffn ? (const char*)p.b1 + cs * 512u : (const char*)p.gw, ffn ? 512 : 896));
  }
  SCHED();

  const unsigned tb = 4 * xcd + slot / 7, ct = slot % 7;
  const rsrc_t rws = mk(p.ws, WS_END);
  const rsrc_t rbl = mk(w == 0 ? p.wo : p.w2, w == 0 ? D * D * 2 : F * D * 2);
  const rsrc_t rbh = mk(w <= 1 ? p.wo : p.w2, w <= 1 ? D * D * 2 : F * D * 2);
  const unsigned bsl = (w == 0 ? 28u : 32u) * 896u, bsh = (w <= 1 ? 28u : 32u) * 896u;
  const unsigned bol = w <= 1 ? 0u : (9 * w - 16) * 32u * 896u;
  const unsigned boh = w == 1 ? 7u * 28u * 896u : bol;
  unsigned vb[2];
  vb[0] = opaque(rr * 896u + ((pos ^ (((rr >> 1) & 3) << 1)) << 4) + ct * 128u);
  vb[1] = opaque(vb[0] + 8u * 896u);
  auto b_m0 = [&](auto N_) {
    constexpr int N = decltype(N_)::value;
    set_m0<hloc(N >> 1) + (N & 1) * 1024u>(wreg);
  };
  auto b_go = [&](auto N_) {
    constexpr int N = decltype(N_)::value, j = N >> 2;
    if constexpr (j < 2) dma_go<0>(vb[N & 1], rbl, bol + j * bsl + ((N >> 1) & 1) * (16u * 896u));
    else dma_go<0>(vb[N & 1], rbh, boh + j * bsh + ((N >> 1) & 1) * (16u * 896u));
  };
  const rsrc_t rpf = mk(w == 7 ? p.wo : p.w2, w == 7 ? D * D * 2 : F * D * 2);
  const unsigned vpf = opaque(lane < (w == 7 ? 28u : 16u) ? (4 * lane + (tb & 3) + (w == 7 ? 0u : w * 64u)) * 896u + ct * 128u : 0x80000000u);
  const unsigned pfs = (w == 7 ? 112u : 448u) * 896u;
  auto pf_go = [&](auto S_) {
    constexpr int SS = decltype(S_)::value;
    dma_touch(vpf, rpf, SS * pfs);
  };

  const unsigned q4 = (lane >> 2) & 3, p4 = lane & 3;
  const unsigned fsw = (q4 >> 1) | ((g & 1) << 1);
  const unsigned xa0 = 1024u + (li >> 3) * 1024u + (li & 7) * 128u + ((g ^ ((li >> 1) & 7)) << 4);
  unsigned ba[4];
#pragma unroll
  for (int c = 0; c < 4; ++c) ba[c] = opaque(wreg + (4 * g + q4) * 128u + ((2u * (c ^ fsw) + (p4 >> 1)) << 4) + (p4 & 1) * 8u);
  s16x4 bl[4][4], bh[4][4];
  auto b_rd = [&](auto J_, auto C_, auto E_) {
    constexpr int J = decltype(J_)::value, C = decltype(C_)::value, E = decltype(E_)::value;
    if constexpr (E == 0) bl[J & 3][C] = ldstr(L, ba[C] + hloc(2 * J));
    else bh[J & 3][C] = ldstr(L, ba[C] + hloc(2 * J + 1));
  };

  auto sched = [](int hb, int k, int np) -> int {
    const int s = hb >> 1;
    if (hb == 0) return k < np ? 8 * 2 + k : -1;
    if ((hb & 1) == 1) {
      if (s < 4) return k < 3 ? 8 * (s + 3) + k : (k == 3 && B_PF ? 200 + s : -1);
      if (s == 4) return k < 4 ? 100 + k : -1;
      if (s == 5) return k < 3 ? 108 + k : -1;
      return k < 2 ? 114 + k : -1;
    }
    if (s <= 4) return 3 + k < np ? 8 * (s + 2) + 3 + k : -1;
    if (s == 5) return k < 4 ? 104 + k : -1;
    return k < 3 ? 111 + k : -1;
  };
  auto d_m0 = [&](auto E_, auto WUS_) {
    constexpr int E = decltype(E_)::value;
    if constexpr (E >= 200) set_m0<SPARE + 1024u>(wreg);
    else if constexpr (E >= 100) b_m0(IC<E - 100>{});
    else if constexpr (E >= 0) st_m0(IC<(E & 7)>{}, IC<(E >> 3)>{}, WUS_);
  };
  auto d_go = [&](auto E_, auto WUS_) {
    constexpr int E = decltype(E_)::value;
    if constexpr (E >= 200) pf_go(IC<E - 200>{});
    else if constexpr (E >= 100) b_go(IC<E - 100>{});
    else if constexpr (E >= 0) st_go(IC<(E & 7)>{}, IC<(E >> 3)>{}, WUS_);
  };

  if (ffn) {
    const unsigned mh = w >> 2, nq = w & 3;
    unsigned xa[2][2], wa[2][4];
#pragma unroll
    for (int hs = 0; hs < 2; ++hs) {
#pragma unroll
      for (int t2 = 0; t2 < 2; ++t2) xa[hs][t2] = opaque((xa0 ^ (hs * 64u)) + (mh * 4u + t2 * 2u) * REG);
#pragma unroll
      for (int c = 0; c < 4; ++c)
        wa[hs][c] = opaque((4u * hs + g) * REG + (nq ? 2u + nq : 0u) * 1024u + q4 * 128u + ((2u * (c ^ fsw) + (p4 >> 1)) << 4) + (p4 & 1) * 8u);
    }
    const unsigned so = (8 * rb + 4 * mh) * A2_TT + (KS_A + 8 * cs + 2 * nq) * 1024u;
    SCHED();
    bar();
    rep<0, 6>([&](auto I_) { st_m0(I_, IC<1>{}, IC<128>{}); NOP1(); st_go(I_, IC<1>{}, IC<128>{}); });
    wait_v<6>();
    bar();
    STAMP(1);
    bf16x8 xf[2][4];
    s16x4 wl[2][4], wh[2][4];
    f32x4 acc[4][4];
    auto rd = [&](auto I_, auto HB_) {
      constexpr int I = decltype(I_)::value, HB = decltype(HB_)::value, hs = HB & 1;
      constexpr unsigned bo = ((HB >> 1) % 3) * STG;
      if constexpr (I >= 2 && I < 6) xf[hs][I - 2] = lds128(L, xa[hs][(I - 2) >> 1] + ((I - 2) & 1) * REG + bo);
      else if constexpr (I < 12) {
        constexpr int c = I < 2 ? 0 : (I - 4) >> 1;
        if constexpr ((I & 1) == 0) wl[hs][c] = ldstr(L, wa[hs][c] + bo);
        else wh[hs][c] = ldstr(L, wa[hs][c] + bo + 512u);
      }
    };
    rep<0, 12>([&](auto I_) { rd(I_, IC<0>{}); });
    f32x4 bv[4];
#pragma unroll
    for (int c = 0; c < 4; ++c) {
      const v2u bb = *(LAS v2u*)(L + SPARE + nq * 128u + c * 32u + g * 8u);
      bv[c] = f32x4{bflo(bb.x), bfhi(bb.x), bflo(bb.y), bfhi(bb.y)};
      asm volatile("" : "+v"(bv[c]));
    }
    auto epi = [&](int t, int u) {
      float hv[8];
#pragma unroll
      for (int j = 0; j < 8; ++j) {
        const float r = relu(acc[2 * u + (j >> 2)][t][j & 3]);
        hv[j] = r * r;
      }
      const v4u o = {pk(hv[0], hv[1]), pk(hv[2], hv[3]), pk(hv[4], hv[5]), pk(hv[6], hv[7])};
      st128(o, rws, lane * 16u + u * 1024u, so + t * A2_TT);
    };
    rep<0, 14>([&](auto HB_) {
      constexpr int HB = decltype(HB_)::value, hs = HB & 1, s = HB >> 1;
      if constexpr (hs == 0) {
        wait_l();
        if constexpr (HB == 0) asm volatile("s_nop 1");
      }
      rep<0, 16>([&](auto I_) {
        constexpr int I = decltype(I_)::value;
        constexpr int K = HB == 0 ? ((I & 1) == 0 ? I >> 1 : -1) : ((I & 3) == 1 ? I >> 2 : -1);
        constexpr int E = K >= 0 ? sched(HB, K, 6) : -1;
        d_m0(IC<E>{}, IC<128>{});
        if constexpr (HB < 13) {
          if constexpr (HB == 0) mmac(acc[I >> 2][I & 3], cat(wl[hs][I >> 2], wh[hs][I >> 2]), xf[hs][I & 3], bv[I >> 2]);
          else mma(acc[I >> 2][I & 3], cat(wl[hs][I >> 2], wh[hs][I >> 2]), xf[hs][I & 3]);
          SCHED();
          rd(I_, IC<HB + 1>{});
        } else {
          mma(acc[I & 3][I >> 2], cat(wl[hs][I & 3], wh[hs][I & 3]), xf[hs][I >> 2]);
          SCHED();
          if constexpr (I < 4) rep<0, 4>([&](auto Q_) { constexpr int Q = decltype(Q_)::value; b_rd(IC<(Q >> 1)>{}, I_, IC<(Q & 1)>{}); });
          if constexpr (I >= 6 && (I & 1) == 0) epi((I - 6) >> 2, ((I - 6) >> 1) & 1);
        }
        SCHED();
        d_go(IC<E>{}, IC<128>{});
      });
      if constexpr (hs == 0) {
        FSTAMP(16 + 3 * s);
        wait_v<(s == 5 ? 8 : 6)>();
        wait_l();
        FSTAMP(17 + 3 * s);
        bar();
        FSTAMP(18 + 3 * s);
      }
    });
    STAMP(2);
    asm volatile("s_nop 7" ::: "memory");
    SCHED();
    epi(2, 1);
    epi(3, 0);
    epi(3, 1);
  } else {
    const unsigned tq = w & 3, hd = w >> 2;
    unsigned xa[2], wa[2][2];
#pragma unroll
    for (int hs = 0; hs < 2; ++hs) {
      xa[hs] = opaque((xa0 ^ (hs * 64u)) + tq * 2u * REG);
#pragma unroll
      for (int c = 0; c < 2; ++c) {
        const unsigned bo = 56u * hd + 32u * c + 8u * p4;
        wa[hs][c] = opaque((4u * hs + g) * REG + q4 * 128u + (((bo >> 4) ^ (fsw << 1)) << 4) + (bo & 8u));
      }
    }
    const unsigned ga = opaque(GW_OFF + g * 16u);
    float mb[2][4];
    unsigned oa[2];
#pragma unroll
    for (int t = 0; t < 2; ++t) {
#pragma unroll
      for (int j = 0; j < 4; ++j) {
        const int kap = 16 * (j >> 1) + (int)(4 * g) + 2 * (j & 1) + (int)(li & 1) - 16;
        const int dl = (int)li - kap;
        const bool ok = (dl >= 0) && (dl <= 14) && (row0 + (int)(32 * tq) + 16 * t + kap >= 0);
        mb[t][j] = __uint_as_float(opaque(ok ? 0u : 0xff800000u));
      }
      const int lr = (int)(32 * tq + li) + 16 * t, tok = row0 + lr;
      oa[t] = opaque((lr >= 14 && tok < S) ? (unsigned)(tok >> 4) * A2_TT + (g * 16u + (unsigned)(tok & 15)) * 16u : 0x80000000u);
    }
    SCHED();
    bar();
    rep<0, 5>([&](auto I_) { st_m0(I_, IC<1>{}, IC<896>{}); NOP1(); st_go(I_, IC<1>{}, IC<896>{}); });
    wait_v<5>();
    bar();
    STAMP(1);
    f32x4 ac[7][2];
    bf16x8 xf[2][2], gf[2];
    s16x4 wl[2][6], wh[2][6];
    auto rd = [&](auto I_, auto HB_) {
      constexpr int I = decltype(I_)::value, HB = decltype(HB_)::value, hs = HB & 1;
      constexpr unsigned bo = ((HB >> 1) % 3) * STG;
      if constexpr (I == 2 || I == 3) xf[hs][I - 2] = lds128(L, xa[hs] + (I - 2) * REG + bo);
      else if constexpr (I < 14) {
        constexpr int c = I < 2 ? 0 : (I - 2) >> 1;
        if constexpr ((I & 1) == 0) wl[hs][c] = ldstr(L, wa[hs][c & 1] + ppos(2 + (c >> 1)) * 1024u + bo);
        else wh[hs][c] = ldstr(L, wa[hs][c & 1] + ppos(2 + (c >> 1)) * 1024u + bo + 512u);
      } else if constexpr (I == 14) gf[hs] = lds128(L, ga + HB * 64u);
    };
    rep<0, 15>([&](auto I_) { rd(I_, IC<0>{}); });
    rep<0, 14>([&](auto HB_) {
      constexpr int HB = decltype(HB_)::value, hs = HB & 1, s = HB >> 1;
      if constexpr (hs == 0) wait_l();
      rep<0, 14>([&](auto I_) {
        constexpr int I = decltype(I_)::value, c = I >> 1, t = I & 1;
        constexpr int K = HB == 0 ? ((I & 1) == 0 ? I >> 1 : -1) : (I % 3 == 1 ? I / 3 : -1);
        constexpr int E = K >= 0 ? sched(HB, K, 5) : -1;
        d_m0(IC<E>{}, IC<896>{});
        const bf16x8 wf = c < 6 ? cat(wl[hs][c < 6 ? c : 0], wh[hs][c < 6 ? c : 0]) : gf[hs];
        if constexpr (HB == 0) {
          if constexpr (c == 4 || c == 5) mma0(ac[c][t], xf[hs][t], wf);
          else mma0(ac[c][t], wf, xf[hs][t]);
        } else {
          if constexpr (c == 4 || c == 5) mma(ac[c][t], xf[hs][t], wf);
          else mma(ac[c][t], wf, xf[hs][t]);
        }
        SCHED();
        if constexpr (HB < 13) {
          if constexpr (I < 5) { rd(IC<2 * I>{}, IC<HB + 1>{}); rd(IC<2 * I + 1>{}, IC<HB + 1>{}); }
          else rd(IC<I + 5>{}, IC<HB + 1>{});
        } else if constexpr (I < 8) {
          b_rd(IC<(I >> 2)>{}, IC<(I & 3)>{}, IC<0>{});
          b_rd(IC<(I >> 2)>{}, IC<(I & 3)>{}, IC<1>{});
        }
        SCHED();
        d_go(IC<E>{}, IC<896>{});
      });
      if constexpr (hs == 0) {
        FSTAMP(16 + 3 * s);
        wait_v<(s == 5 ? 8 : (s == 6 ? 6 : 5))>();
        wait_l();
        FSTAMP(17 + 3 * s);
        bar();
        FSTAMP(18 + 3 * s);
      }
    });
    STAMP(2);
    asm volatile("s_nop 7" ::: "memory");
    SCHED();
    const unsigned pm = g == 3 ? 0u : 0xffffffffu;
    bf16x8 qf[2], kf[2];
    unsigned vh[2][2][2];
#pragma unroll
    for (int t = 1; t >= 0; --t) {
      kf[t] = asfrag(pk(ac[2][t][0], ac[2][t][1]), pk(ac[2][t][2], ac[2][t][3]), pm & pk(ac[3][t][0], ac[3][t][1]), pm & pk(ac[3][t][2], ac[3][t][3]));
#pragma unroll
      for (int c = 0; c < 2; ++c) {
        vh[t][c][0] = pk(ac[4 + c][t][0], ac[4 + c][t][1]);
        vh[t][c][1] = pk(ac[4 + c][t][2], ac[4 + c][t][3]);
      }
      if (t == 1) {
        const unsigned nw = ((w + 1) & 7) * REG;
        *(LAS bf16x8*)(L + nw + 4096u + lane * 16u) = kf[1];
        *(LAS v4u*)(L + nw + 5120u + lane * 16u) = v4u{vh[1][0][0], vh[1][0][1], vh[1][1][0], vh[1][1][1]};
      }
      qf[t] = asfrag(pk(ac[0][t][0], ac[0][t][1]), pk(ac[0][t][2], ac[0][t][3]), pm & pk(ac[1][t][0], ac[1][t][1]), pm & pk(ac[1][t][2], ac[1][t][3]));
    }
    wait_l();
    bar();
    const bf16x8 kp = lds128(L, wreg + 4096u + lane * 16u);
    const v4u vp = *(LAS v4u*)(L + wreg + 5120u + lane * 16u);
    const unsigned head = 2 * hp + hd;
    const bool odd = li & 1;
    const unsigned psh = odd ? 16u : 0u;
#pragma unroll
    for (int t = 1; t >= 0; --t) {
      const f32x4 z = {0.f, 0.f, 0.f, 0.f};
      const f32x4 s0 = mfma(t == 0 ? kp : kf[0], qf[t], z);
      const f32x4 s1 = mfma(kf[t], qf[t], z);
      const float gv = rbf(__builtin_amdgcn_rcpf(1.0f + __builtin_amdgcn_exp2f(rbf(ac[6][t][0]) * -1.4426950408889634f)));
      float sv[4];
      sv[0] = odd ? s0[1] : s0[0];
      sv[1] = odd ? s0[3] : s0[2];
      sv[2] = odd ? s1[1] : s1[0];
      sv[3] = odd ? s1[3] : s1[2];
      const float SC = 0.18898223650461362f * 1.4426950408889634f;
#pragma unroll
      for (int j = 0; j < 4; ++j) sv[j] = __builtin_fmaf(sv[j], SC, mb[t][j]);
      const float m = xmax(fmaxf(fmaxf(sv[0], sv[1]), fmaxf(sv[2], sv[3])));
#pragma unroll
      for (int j = 0; j < 4; ++j) sv[j] = __builtin_amdgcn_exp2f(sv[j] - m);
      const float inv = __builtin_amdgcn_rcpf(xsum((sv[0] + sv[1]) + (sv[2] + sv[3])));
      const bf16x8 pf = asfrag(pk(sv[0] * inv, 0.f) << psh, pk(sv[1] * inv, 0.f) << psh, pk(sv[2] * inv, 0.f) << psh, pk(sv[3] * inv, 0.f) << psh);
      f32x4 o[2];
#pragma unroll
      for (int c = 0; c < 2; ++c) {
        const bf16x8 vf = t == 0 ? asfrag(vp[2 * c], vp[2 * c + 1], vh[0][c][0], vh[0][c][1]) : asfrag(vh[0][c][0], vh[0][c][1], vh[1][c][0], vh[1][c][1]);
        o[c] = mfma(vf, pf, z) * gv;
      }
      const v4u ov = {pk(o[0][0], o[0][1]), pk(o[0][2], o[0][3]), pm & pk(o[1][0], o[1][1]), pm & pk(o[1][2], o[1][3])};
      st128(ov, rws, oa[t], head * 1024u);
      if (head == 0) {
        const unsigned tok = (unsigned)(row0 + (int)(32 * tq + li) + 16 * t);
        __builtin_amdgcn_raw_buffer_store_b32(__float_as_uint(gv), rws, (g == 0 && oa[t] < 0x80000000u) ? tok * 4u : 0x80000000u, WS_G, 16);
      }
    }
  }
  STAMP(3);
  const u64 tag = (gm_dispatch_id() + 1) * 0x9E3779B97F4A7C15ull ^ (u64)__builtin_amdgcn_queue_ptr();
  const unsigned fo_pub = ffn ? WS_FH + (2 * rb + (w >> 2)) * 256u + (4 * cs + (w & 3)) * 8u : WS_FO + (4 * rt + (w & 3)) * 128u + (2 * hp + (w >> 2)) * 8u;
  wait_v<0>();
  STAMP(4);
  if (lane == 0) __hip_atomic_store((GAS u64*)((char*)p.ws + fo_pub), tag, __ATOMIC_RELAXED, __HIP_MEMORY_SCOPE_AGENT);
  if (!has2) {
    STAMP_FLUSH();
    return;
  }
  rep<0, 16>([&](auto I_) { constexpr int I = decltype(I_)::value; b_rd(IC<2 + (I >> 3)>{}, IC<((I >> 1) & 3)>{}, IC<(I & 1)>{}); });
  SCHED();
  bar();
  wait_l();
  rep<16, 36>([&](auto N_) { b_m0(N_); NOP1(); b_go(N_); });
  STAMP(5);

  const unsigned T0 = 64 * tb;
  if (w == POLL_WAVE) {
    const unsigned g0 = 4 * (T0 / QROWS) + (T0 % QROWS + 14) / 32;
    const unsigned T1 = T0 + 63;
    const unsigned g1 = 4 * (T1 / QROWS) + (T1 % QROWS + 14) / 32;
    GAS u64* fa = (GAS u64*)((char*)p.ws + WS_FO + min(g0 + g, g1) * 128u + li * 8u);
    GAS u64* fh = (GAS u64*)((char*)p.ws + WS_FH + tb * 256u + min(lane, 27u) * 8u);
#pragma unroll 1
    for (unsigned spin = 0; spin < SPIN_CAP; ++spin) {
      const u64 v0 = __hip_atomic_load(fa, __ATOMIC_RELAXED, __HIP_MEMORY_SCOPE_AGENT);
      const u64 v1 = __hip_atomic_load(fh, __ATOMIC_RELAXED, __HIP_MEMORY_SCOPE_AGENT);
      if (__all(v0 == tag && v1 == tag)) break;
      __builtin_amdgcn_s_sleep(POLL_SLEEP);
    }
  }
  unsigned va[4];
#pragma unroll
  for (int t = 0; t < 4; ++t) va[t] = opaque(lane * 16u + t * A2_TT);
  const unsigned aol = 4 * tb * A2_TT + (w == 1 ? 16u : 9u * w) * 1024u, aoh = 4 * tb * A2_TT + (w == 1 ? 7u : 9u * w) * 1024u;
  SCHED();
  bar();
  STAMP(6);
  auto a_load = [&](int j, int t) {
    return j < 2 ? __builtin_amdgcn_raw_buffer_load_b128(rws, va[t] + j * 1024u, aol, 16)
                 : __builtin_amdgcn_raw_buffer_load_b128(rws, va[t] + ((j - 2) & 3) * 1024u, aoh + (j < 6 ? 2048u : 6144u), 16);
  };
  constexpr int WIN = WINDOW;
  v4u af[WIN][4];
  float gq[4] = {0.f, 0.f, 0.f, 0.f};
  if (PRIO) __builtin_amdgcn_s_setprio(3);
#pragma unroll
  for (int j = 0; j < WIN; ++j) {
#pragma unroll
    for (int t = 0; t < 4; ++t) af[j][t] = a_load(j, t);
    if (j == 1 && w == 1) {
#pragma unroll
      for (int t = 0; t < 4; ++t) gq[t] = __uint_as_float(__builtin_amdgcn_raw_buffer_load_b32(rws, (T0 + 16 * t + li) * 4u, WS_G, 16));
    }
    SCHED();
  }
  f32x4 acc[4][4];
  v4u b2v;
  rep<0, 9>([&](auto J_) {
    constexpr int j = decltype(J_)::value;
    if (PRIO && j + WIN == 4) __builtin_amdgcn_s_setprio(2);
    if (PRIO && j + WIN == 6) __builtin_amdgcn_s_setprio(1);
    if (PRIO && j + WIN == 8) __builtin_amdgcn_s_setprio(0);
    if (j == 2 && w == 1) {
      asm volatile("s_nop 7" ::: "memory");
      SCHED();
#pragma unroll
      for (int t = 0; t < 4; ++t) {
        const float og = rbf(1.0f - gq[t]);
#pragma unroll
        for (int c = 0; c < 4; ++c) acc[c][t] *= og;
      }
      SCHED();
      asm volatile("s_nop 1" ::: "memory");
    }
    if constexpr (j >= 4) wait_l();
    rep<0, 4>([&](auto T_) {
      constexpr int t = decltype(T_)::value;
      const bf16x8 a = __builtin_bit_cast(bf16x8, af[j % WIN][t]);
#pragma unroll
      for (int c = 0; c < 4; ++c) {
        if constexpr (j == 0) mma0(acc[c][t], cat(bl[0][c], bh[0][c]), a);
        else mma(acc[c][t], cat(bl[j & 3][c], bh[j & 3][c]), a);
      }
      SCHED();
      if constexpr (j + WIN < 9) af[j % WIN][t] = a_load(j + WIN, t);
      if constexpr (j >= 1 && j + 3 < 9) {
        b_rd(IC<j + 3>{}, T_, IC<0>{});
        b_rd(IC<j + 3>{}, T_, IC<1>{});
      }
      SCHED();
    });
    if constexpr (j == 6) b2v = *(const GAS v4u*)((const char*)p.b2 + ct * 128u + (tid & 7) * 16u);
  });
  STAMP(7);
  asm volatile("s_nop 7" ::: "memory");
  SCHED();
#pragma unroll
  for (int t = 0; t < 4; ++t)
#pragma unroll
    for (int c = 0; c < 4; ++c) {
      const unsigned lp = g * 16u + ((li + 4u * ((2u * c + (g >> 1)) & 3u)) & 15u);
      *(LAS f32x4*)(L + wreg + (t * 4 + c) * 1024u + lp * 16u) = acc[c][t];
    }
  if (w == 1 && g == 0) {
#pragma unroll
    for (int t = 0; t < 4; ++t) *(LAS float*)(L + REG + SPARE + (t * 16 + li) * 4u) = gq[t];
  }
  wait_l();
  bar();
  STAMP(8);
  {
    const unsigned tk = tid >> 3, q = tid & 7;
    const unsigned n = (tk >> 4) * 4u + (q >> 1), g0 = 2 * (q & 1), i = tk & 15;
    const unsigned ad = n * 1024u + (g0 * 16u + ((i + 4u * (q & 3u)) & 15u)) * 16u;
    f32x4 alo = *(LAS f32x4*)(L + ad) + *(LAS f32x4*)(L + ad + REG);
    f32x4 ahi = *(LAS f32x4*)(L + ad + 256u) + *(LAS f32x4*)(L + ad + REG + 256u);
    f32x4 lo = {bflo(b2v[0]), bfhi(b2v[0]), bflo(b2v[1]), bfhi(b2v[1])}, hi = {bflo(b2v[2]), bfhi(b2v[2]), bflo(b2v[3]), bfhi(b2v[3])};
#pragma unroll
    for (int k = 2; k < 8; ++k) {
      lo += *(LAS f32x4*)(L + k * REG + ad);
      hi += *(LAS f32x4*)(L + k * REG + ad + 256u);
    }
    const float og = rbf(1.0f - *(LAS float*)(L + REG + SPARE + tk * 4u));
    alo += og * lo;
    ahi += og * hi;
    const v4u o = {pk(alo[0], alo[1]), pk(alo[2], alo[3]), pk(ahi[0], ahi[1]), pk(ahi[2], ahi[3])};
    *(GAS v4u*)((char*)p.out + (64 * tb + tk) * 896u + ct * 128u + q * 16u) = o;
  }
  STAMP(9);
  STAMP_FLUSH();
}
}

extern "C" void kernel_launch(void* const* d_in, const int* in_sizes, int n_in, void* d_out, int out_size, void* d_ws, size_t ws_size,
                              hipStream_t stream) {
  fprintf(stderr, "PTRS:");
  for (int i = 0; i < n_in; ++i) fprintf(stderr, " in%d=%p", i, d_in[i]);
  fprintf(stderr, " out=%p ws=%p\n", d_out, d_ws);
  for (int i = 0; i < n_in; ++i) { hipPointerAttribute_t a; if (hipPointerGetAttributes(&a, d_in[i]) == hipSuccess) fprintf(stderr, " type%d=%d flags=%u", i, (int)a.type, a.allocationFlags); }
  fprintf(stderr, "\n");
  gm::Params p;
  p.x = d_in[0];
  p.wqkv = d_in[1];
  p.wo = d_in[2];
  p.w1 = d_in[4];
  p.b1 = d_in[5];
  p.w2 = d_in[6];
  p.b2 = d_in[7];
  p.gw = d_in[8];
  p.out = d_out;
  p.ws = d_ws;
  gm::gm_kernel<<<256, 512, 0, stream>>>(p);
}
```

```cpp
#include <hip/hip_runtime.h>
#include <stdint.h>

typedef short bf16x8 __attribute__((ext_vector_type(8)));
typedef short s16x4 __attribute__((ext_vector_type(4)));
typedef float f32x4 __attribute__((ext_vector_type(4)));
typedef unsigned v4u __attribute__((ext_vector_type(4)));
typedef unsigned v2u __attribute__((ext_vector_type(2)));
typedef __bf16 bf2_t __attribute__((ext_vector_type(2)));
typedef float f32x2 __attribute__((ext_vector_type(2)));
#define LAS __attribute__((address_space(3)))
#define GAS __attribute__((address_space(1)))
#define DI __device__ __forceinline__
typedef unsigned long long u64;
typedef __amdgpu_buffer_rsrc_t rsrc_t;
typedef u64 v8q __attribute__((ext_vector_type(8)));
typedef u64 v2q __attribute__((ext_vector_type(2)));

#define CODE_PF 16384
#define B_PF 0
#define WINDOW 2
#define PRIO 1
#define POLL_WAVE 0
#define POLL_SLEEP 4

extern "C" __device__ u64 gm_dispatch_id() __asm("llvm.amdgcn.dispatch.id");

namespace gm {
constexpr int S = 2048, D = 448, F = 1792, NQKV = 1344;
constexpr int KS_A = 16, KS = 72;
constexpr unsigned A2_TT = KS * 1024;
constexpr unsigned WS_G = 128 * A2_TT, WS_FH = WS_G + 8192, WS_FO = WS_FH + 8192, WS_END = WS_FO + 76 * 128;
constexpr unsigned REG = 20480, STG = 6144, SPARE = 3 * STG;
constexpr int LDS_BYTES = 163840;
constexpr unsigned GW_OFF = 5120;
constexpr int QROWS = 114;
constexpr unsigned SPIN_CAP = 1u << 16;

struct Params {
  const void *x, *wqkv, *wo, *w1, *b1, *w2, *b2, *gw;
  void *out, *ws;
};
template <int N> struct IC { static constexpr int value = N; };

DI rsrc_t mk(const void* p, unsigned n) { return __builtin_amdgcn_make_buffer_rsrc((void*)p, 0, n, 0x00020000); }
template <unsigned C> DI void set_m0(unsigned base) { asm volatile("s_add_u32 m0, %0, %1" ::"s"(base), "i"(C) : "scc", "memory"); }
template <unsigned IMM> DI void dma_go(unsigned voff, rsrc_t rs, unsigned soff) {
  asm volatile("buffer_load_dwordx4 %0, %1, %2 offen offset:%3 lds" ::"v"(voff), "s"(rs), "s"(soff), "i"(IMM) : "memory");
}
template <unsigned IMM> DI void dma_go0(unsigned voff, rsrc_t rs) {
  asm volatile("buffer_load_dwordx4 %0, %1, 0 offen offset:%2 lds" ::"v"(voff), "s"(rs), "i"(IMM) : "memory");
}
DI void dma_touch(unsigned voff, rsrc_t rs, unsigned soff) {
  asm volatile("buffer_load_dword %0, %1, %2 offen lds" ::"v"(voff), "s"(rs), "s"(soff) : "memory");
}
#define NOP1() asm volatile("s_nop 0")
DI f32x4 mfma(bf16x8 a, bf16x8 b, f32x4 c) { return __builtin_amdgcn_mfma_f32_16x16x32_bf16(a, b, c, 0, 0, 0); }
DI void mma(f32x4& c, bf16x8 a, bf16x8 b) { asm volatile("v_mfma_f32_16x16x32_bf16 %0, %1, %2, %0" : "+v"(c) : "v"(a), "v"(b)); }
DI void mma0(f32x4& c, bf16x8 a, bf16x8 b) { asm volatile("v_mfma_f32_16x16x32_bf16 %0, %1, %2, 0" : "=&v"(c) : "v"(a), "v"(b)); }
DI void mmac(f32x4& d, bf16x8 a, bf16x8 b, f32x4 c) { asm volatile("v_mfma_f32_16x16x32_bf16 %0, %1, %2, %3" : "=&v"(d) : "v"(a), "v"(b), "v"(c)); }
DI float relu(float v) {
  float r;
  asm("v_max_f32 %0, 0, %1" : "=v"(r) : "v"(v));
  return r;
}
DI float fmx(float a, float b) {
  float r;
  asm("v_max_f32 %0, %1, %2" : "=v"(r) : "v"(a), "v"(b));
  return r;
}
DI unsigned pk(float lo, float hi) {
  f32x2 v = {lo, hi};
  return __builtin_bit_cast(unsigned, __builtin_convertvector(v, bf2_t));
}
DI float rbf(float f) { return __uint_as_float(pk(f, 0.f) << 16); }
DI float bflo(unsigned u) { return __uint_as_float(u << 16); }
DI float bfhi(unsigned u) { return __uint_as_float(u & 0xffff0000u); }
DI bf16x8 asfrag(unsigned a, unsigned b, unsigned c, unsigned d) {
  v4u v = {a, b, c, d};
  return __builtin_bit_cast(bf16x8, v);
}
DI void st128(v4u v, rsrc_t rs, unsigned voff, unsigned soff) {
  __builtin_amdgcn_raw_buffer_store_b128(v, rs, voff, soff, 16);
  __builtin_amdgcn_sched_barrier(0);
  asm volatile("s_nop 1");
  __builtin_amdgcn_sched_barrier(0);
}
DI bf16x8 cat(s16x4 lo, s16x4 hi) { return __builtin_shufflevector(lo, hi, 0, 1, 2, 3, 4, 5, 6, 7); }
DI bf16x8 lds128(LAS char* L, unsigned a) { return *(LAS bf16x8*)(L + a); }
DI s16x4 ldstr(LAS char* L, unsigned a) { return __builtin_amdgcn_ds_read_tr16_b64_v4i16((LAS s16x4*)(L + a)); }
DI unsigned opaque(unsigned v) {
  asm volatile("" : "+v"(v));
  return v;
}
DI float xmax(float v) {
  auto a = __builtin_amdgcn_permlane32_swap(__float_as_uint(v), __float_as_uint(v), false, false);
  v = fmx(__uint_as_float(a[0]), __uint_as_float(a[1]));
  auto b = __builtin_amdgcn_permlane16_swap(__float_as_uint(v), __float_as_uint(v), false, false);
  return fmx(__uint_as_float(b[0]), __uint_as_float(b[1]));
}
DI float xsum(float v) {
  auto a = __builtin_amdgcn_permlane32_swap(__float_as_uint(v), __float_as_uint(v), false, false);
  v = __uint_as_float(a[0]) + __uint_as_float(a[1]);
  auto b = __builtin_amdgcn_permlane16_swap(__float_as_uint(v), __float_as_uint(v), false, false);
  return __uint_as_float(b[0]) + __uint_as_float(b[1]);
}
#define SCHED() __builtin_amdgcn_sched_barrier(0)
#define STAMP(k)
#define STAMP_FLUSH()
#define FSTAMP(k)
template <int N> DI void wait_v() { asm volatile("s_waitcnt vmcnt(%0)" ::"i"(N) : "memory"); }
DI void wait_l() {
  SCHED();
  __builtin_amdgcn_s_waitcnt(0xC07F);
  asm volatile("" ::: "memory");
  SCHED();
}
DI void bar() { asm volatile("s_barrier" ::: "memory"); }

constexpr unsigned hloc(int h) {
  h = h >= 10 ? h - 10 : h;
  return h == 0 ? SPARE : (h < 4 ? STG + (h - 1) * 2048u : (h < 7 ? 2 * STG + (h - 4) * 2048u : (h - 7) * 2048u));
}
constexpr unsigned ppos(int i) { return i < 2 ? i + 1 : (i == 2 ? 0 : i); }
template <int I, int N, class Fn> DI void rep(Fn&& f) {
  if constexpr (I < N) {
    f(IC<I>{});
    rep<I + 1, N>(f);
  }
}

__global__ __launch_bounds__(512, 2) void gm_kernel(Params pin) {
  __shared__ __attribute__((aligned(1024))) char smem[LDS_BYTES];
  LAS char* L = (LAS char*)smem;
  v8q ka;
  v2q kb2;
  asm volatile("s_load_dwordx16 %0, %2, 0x0\n\ts_load_dwordx4 %1, %2, 0x40" : "=&s"(ka), "=&s"(kb2) : "s"(__builtin_amdgcn_kernarg_segment_ptr()));
  const unsigned tid = threadIdx.x, lane = tid & 63;
  const unsigned b = blockIdx.x, xcd = b & 7, slot = b >> 3;
  if (tid < CODE_PF / 1024) {
    u64 pc;
    asm volatile("s_getpc_b64 %0" : "=s"(pc));
    const unsigned vo = ((slot & 7) * (CODE_PF / 1024) + tid) * 128u;
    asm volatile("s_mov_b32 m0, %0\n\ts_nop 0\n\tglobal_load_lds_dword %1, %2" ::"s"(SPARE + 1024u), "v"(vo), "s"(pc) : "memory");
  }
  const unsigned w = __builtin_amdgcn_readfirstlane(tid >> 6);
  const unsigned wreg = w * REG;
  const unsigned li = lane & 15, g = lane >> 4;
  STAMP(0);

  const unsigned rq = xcd >> 1, side = xcd & 1;
  const unsigned nffn = 16 - 4 * side;
  const bool ffn = slot < nffn;
  const unsigned rb = 4 * rq + (slot & 3), cs = side * 4 + (slot >> 2);
  const unsigned jq = slot - nffn;
  const unsigned hp = ((rq & 1) ^ side ^ 1) * 4 + (jq & 3);
  const unsigned rt = 9 * (rq >> 1) + (jq >> 2) + (rq & 1) * (5 - side);
  const bool has2 = slot < 28;

  const unsigned rr = lane >> 3, pos = lane & 7;
  const int row0 = ffn ? (int)(128 * rb) : (int)(QROWS * rt) - 14;
  unsigned vx[2];
#pragma unroll
  for (int h = 0; h < 2; ++h) {
    int r = row0 + (int)(16 * w + 8 * h + rr);
    r = r < 0 ? 0 : (r > S - 1 ? S - 1 : r);
    vx[h] = (unsigned)r * 896u + ((pos ^ (4u * h + (rr >> 1))) << 4);
  }
  const unsigned ldw = ffn ? 3584u : 2688u;
  unsigned wch = pos ^ ((((rr >> 1) & 1) | ((w & 1) << 1)) << 1);
  wch = min(wch, ffn ? 7u : 6u);
  const unsigned vw = (8 * w + rr) * ldw + (wch << 4) + (ffn ? cs * 512u : hp * 112u);
  const unsigned wstep = 64u * ldw;

  SCHED();
  asm volatile("s_waitcnt lgkmcnt(0)" : "+s"(ka), "+s"(kb2));
  Params p;
  p.x = (const void*)ka[0]; p.wqkv = (const void*)ka[1]; p.wo = (const void*)ka[2]; p.w1 = (const void*)ka[3];
  p.b1 = (const void*)ka[4]; p.w2 = (const void*)ka[5]; p.b2 = (const void*)ka[6]; p.gw = (const void*)ka[7];
  p.out = (void*)kb2[0]; p.ws = (void*)kb2[1];
  const rsrc_t rx = mk(p.x, S * D * 2);
  const rsrc_t rw = mk(ffn ? p.w1 : p.wqkv, ffn ? D * F * 2 : D * NQKV * 2);

  auto st_m0 = [&](auto I_, auto ST_, auto WUS_) {
    constexpr int I = decltype(I_)::value, ST = decltype(ST_)::value, WUS = decltype(WUS_)::value;
    constexpr unsigned imm = I < 2 ? ST * 128u : (I - 2) * WUS;
    set_m0<(ST % 3) * STG + ppos(I) * 1024u - imm>(wreg);
  };
  auto st_go = [&](auto I_, auto ST_, auto WUS_) {
    constexpr int I = decltype(I_)::value, ST = decltype(ST_)::value, WUS = decltype(WUS_)::value;
    if constexpr (I < 2) dma_go0<ST * 128u>(vx[I], rx);
    else if constexpr (ST == 0) dma_go0<(I - 2) * WUS>(vw, rw);
    else dma_go<(I - 2) * WUS>(vw, rw, ST * wstep);
  };
  auto st_all = [&](auto ST_) {
    if (ffn) rep<0, 6>([&](auto I_) { st_m0(I_, ST_, IC<128>{}); NOP1(); st_go(I_, ST_, IC<128>{}); });
    else rep<0, 5>([&](auto I_) { st_m0(I_, ST_, IC<896>{}); NOP1(); st_go(I_, ST_, IC<896>{}); });
  };
  st_all(IC<0>{});
  if (w == 0) {
    set_m0<0>(ffn ? SPARE : GW_OFF);
    NOP1();
    dma_go0<0>(lane * 16, mk(ffn ? (const char*)p.b1 + cs * 512u : (const char*)p.gw, ffn ? 512 : 896));
  }
  SCHED();

  const unsigned tb = 4 * xcd + slot / 7, ct = slot % 7;
  const rsrc_t rws = mk(p.ws, WS_END);
  const rsrc_t rbl = mk(w == 0 ? p.wo : p.w2, w == 0 ? D * D * 2 : F * D * 2);
  const rsrc_t rbh = mk(w <= 1 ? p.wo : p.w2, w <= 1 ? D * D * 2 : F * D * 2);
  const unsigned bsl = (w == 0 ? 28u : 32u) * 896u, bsh = (w <= 1 ? 28u : 32u) * 896u;
  const unsigned bol = w <= 1 ? 0u : (9 * w - 16) * 32u * 896u;
  const unsigned boh = w == 1 ? 7u * 28u * 896u : bol;
  unsigned vb[2];
  vb[0] = opaque(rr * 896u + ((pos ^ (((rr >> 1) & 3) << 1)) << 4) + ct * 128u);
  vb[1] = opaque(vb[0] + 8u * 896u);
  auto b_m0 = [&](auto N_) {
    constexpr int N = decltype(N_)::value;
    set_m0<hloc(N >> 1) + (N & 1) * 1024u>(wreg);
  };
  auto b_go = [&](auto N_) {
    constexpr int N = decltype(N_)::value, j = N >> 2;
    if constexpr (j < 2) dma_go<0>(vb[N & 1], rbl, bol + j * bsl + ((N >> 1) & 1) * (16u * 896u));
    else dma_go<0>(vb[N & 1], rbh, boh + j * bsh + ((N >> 1) & 1) * (16u * 896u));
  };
  const rsrc_t rpf = mk(w == 7 ? p.wo : p.w2, w == 7 ? D * D * 2 : F * D * 2);
  const unsigned vpf = opaque(lane < (w == 7 ? 28u : 16u) ? (4 * lane + (tb & 3) + (w == 7 ? 0u : w * 64u)) * 896u + ct * 128u : 0x80000000u);
  const unsigned pfs = (w == 7 ? 112u : 448u) * 896u;
  auto pf_go = [&](auto S_) {
    constexpr int SS = decltype(S_)::value;
    dma_touch(vpf, rpf, SS * pfs);
  };

  const unsigned q4 = (lane >> 2) & 3, p4 = lane & 3;
  const unsigned fsw = (q4 >> 1) | ((g & 1) << 1);
  const unsigned xa0 = 1024u + (li >> 3) * 1024u + (li & 7) * 128u + ((g ^ ((li >> 1) & 7)) << 4);
  unsigned ba[4];
#pragma unroll
  for (int c = 0; c < 4; ++c) ba[c] = opaque(wreg + (4 * g + q4) * 128u + ((2u * (c ^ fsw) + (p4 >> 1)) << 4) + (p4 & 1) * 8u);
  s16x4 bl[4][4], bh[4][4];
  auto b_rd = [&](auto J_, auto C_, auto E_) {
    constexpr int J = decltype(J_)::value, C = decltype(C_)::value, E = decltype(E_)::value;
    if constexpr (E == 0) bl[J & 3][C] = ldstr(L, ba[C] + hloc(2 * J));
    else bh[J & 3][C] = ldstr(L, ba[C] + hloc(2 * J + 1));
  };

  auto sched = [](int hb, int k, int np) -> int {
    const int s = hb >> 1;
    if (hb == 0) return k < np ? 8 * 2 + k : -1;
    if ((hb & 1) == 1) {
      if (s < 4) return k < 3 ? 8 * (s + 3) + k : (k == 3 && B_PF ? 200 + s : -1);
      if (s == 4) return k < 4 ? 100 + k : -1;
      if (s == 5) return k < 3 ? 108 + k : -1;
      return k < 2 ? 114 + k : -1;
    }
    if (s <= 4) return 3 + k < np ? 8 * (s + 2) + 3 + k : -1;
    if (s == 5) return k < 4 ? 104 + k : -1;
    return k < 3 ? 111 + k : -1;
  };
  auto d_m0 = [&](auto E_, auto WUS_) {
    constexpr int E = decltype(E_)::value;
    if constexpr (E >= 200) set_m0<SPARE + 1024u>(wreg);
    else if constexpr (E >= 100) b_m0(IC<E - 100>{});
    else if constexpr (E >= 0) st_m0(IC<(E & 7)>{}, IC<(E >> 3)>{}, WUS_);
  };
  auto d_go = [&](auto E_, auto WUS_) {
    constexpr int E = decltype(E_)::value;
    if constexpr (E >= 200) pf_go(IC<E - 200>{});
    else if constexpr (E >= 100) b_go(IC<E - 100>{});
    else if constexpr (E >= 0) st_go(IC<(E & 7)>{}, IC<(E >> 3)>{}, WUS_);
  };

  if (ffn) {
    const unsigned mh = w >> 2, nq = w & 3;
    unsigned xa[2][2], wa[2][4];
#pragma unroll
    for (int hs = 0; hs < 2; ++hs) {
#pragma unroll
      for (int t2 = 0; t2 < 2; ++t2) xa[hs][t2] = opaque((xa0 ^ (hs * 64u)) + (mh * 4u + t2 * 2u) * REG);
#pragma unroll
      for (int c = 0; c < 4; ++c)
        wa[hs][c] = opaque((4u * hs + g) * REG + (nq ? 2u + nq : 0u) * 1024u + q4 * 128u + ((2u * (c ^ fsw) + (p4 >> 1)) << 4) + (p4 & 1) * 8u);
    }
    const unsigned so = (8 * rb + 4 * mh) * A2_TT + (KS_A + 8 * cs + 2 * nq) * 1024u;
    SCHED();
    bar();
    rep<0, 6>([&](auto I_) { st_m0(I_, IC<1>{}, IC<128>{}); NOP1(); st_go(I_, IC<1>{}, IC<128>{}); });
    wait_v<6>();
    bar();
    STAMP(1);
    bf16x8 xf[2][4];
    s16x4 wl[2][4], wh[2][4];
    f32x4 acc[4][4];
    auto rd = [&](auto I_, auto HB_) {
      constexpr int I = decltype(I_)::value, HB = decltype(HB_)::value, hs = HB & 1;
      constexpr unsigned bo = ((HB >> 1) % 3) * STG;
      if constexpr (I >= 2 && I < 6) xf[hs][I - 2] = lds128(L, xa[hs][(I - 2) >> 1] + ((I - 2) & 1) * REG + bo);
      else if constexpr (I < 12) {
        constexpr int c = I < 2 ? 0 : (I - 4) >> 1;
        if constexpr ((I & 1) == 0) wl[hs][c] = ldstr(L, wa[hs][c] + bo);
        else wh[hs][c] = ldstr(L, wa[hs][c] + bo + 512u);
      }
    };
    rep<0, 12>([&](auto I_) { rd(I_, IC<0>{}); });
    f32x4 bv[4];
#pragma unroll
    for (int c = 0; c < 4; ++c) {
      const v2u bb = *(LAS v2u*)(L + SPARE + nq * 128u + c * 32u + g * 8u);
      bv[c] = f32x4{bflo(bb.x), bfhi(bb.x), bflo(bb.y), bfhi(bb.y)};
      asm volatile("" : "+v"(bv[c]));
    }
    auto epi = [&](int t, int u) {
      float hv[8];
#pragma unroll
      for (int j = 0; j < 8; ++j) {
        const float r = relu(acc[2 * u + (j >> 2)][t][j & 3]);
        hv[j] = r * r;
      }
      const v4u o = {pk(hv[0], hv[1]), pk(hv[2], hv[3]), pk(hv[4], hv[5]), pk(hv[6], hv[7])};
      st128(o, rws, lane * 16u + u * 1024u, so + t * A2_TT);
    };
    rep<0, 14>([&](auto HB_) {
      constexpr int HB = decltype(HB_)::value, hs = HB & 1, s = HB >> 1;
      if constexpr (hs == 0) {
        wait_l();
        if constexpr (HB == 0) asm volatile("s_nop 1");
      }
      rep<0, 16>([&](auto I_) {
        constexpr int I = decltype(I_)::value;
        constexpr int K = HB == 0 ? ((I & 1) == 0 ? I >> 1 : -1) : ((I & 3) == 1 ? I >> 2 : -1);
        constexpr int E = K >= 0 ? sched(HB, K, 6) : -1;
        d_m0(IC<E>{}, IC<128>{});
        if constexpr (HB < 13) {
          if constexpr (HB == 0) mmac(acc[I >> 2][I & 3], cat(wl[hs][I >> 2], wh[hs][I >> 2]), xf[hs][I & 3], bv[I >> 2]);
          else mma(acc[I >> 2][I & 3], cat(wl[hs][I >> 2], wh[hs][I >> 2]), xf[hs][I & 3]);
          SCHED();
          rd(I_, IC<HB + 1>{});
        } else {
          mma(acc[I & 3][I >> 2], cat(wl[hs][I & 3], wh[hs][I & 3]), xf[hs][I >> 2]);
          SCHED();
          if constexpr (I < 4) rep<0, 4>([&](auto Q_) { constexpr int Q = decltype(Q_)::value; b_rd(IC<(Q >> 1)>{}, I_, IC<(Q & 1)>{}); });
          if constexpr (I >= 6 && (I & 1) == 0) epi((I - 6) >> 2, ((I - 6) >> 1) & 1);
        }
        SCHED();
        d_go(IC<E>{}, IC<128>{});
      });
      if constexpr (hs == 0) {
        FSTAMP(16 + 3 * s);
        wait_v<(s == 5 ? 8 : 6)>();
        wait_l();
        FSTAMP(17 + 3 * s);
        bar();
        FSTAMP(18 + 3 * s);
      }
    });
    STAMP(2);
    asm volatile("s_nop 7" ::: "memory");
    SCHED();
    epi(2, 1);
    epi(3, 0);
    epi(3, 1);
  } else {
    const unsigned tq = w & 3, hd = w >> 2;
    unsigned xa[2], wa[2][2];
#pragma unroll
    for (int hs = 0; hs < 2; ++hs) {
      xa[hs] = opaque((xa0 ^ (hs * 64u)) + tq * 2u * REG);
#pragma unroll
      for (int c = 0; c < 2; ++c) {
        const unsigned bo = 56u * hd + 32u * c + 8u * p4;
        wa[hs][c] = opaque((4u * hs + g) * REG + q4 * 128u + (((bo >> 4) ^ (fsw << 1)) << 4) + (bo & 8u));
      }
    }
    const unsigned ga = opaque(GW_OFF + g * 16u);
    float mb[2][4];
    unsigned oa[2];
#pragma unroll
    for (int t = 0; t < 2; ++t) {
#pragma unroll
      for (int j = 0; j < 4; ++j) {
        const int kap = 16 * (j >> 1) + (int)(4 * g) + 2 * (j & 1) + (int)(li & 1) - 16;
        const int dl = (int)li - kap;
        const bool ok = (dl >= 0) && (dl <= 14) && (row0 + (int)(32 * tq) + 16 * t + kap >= 0);
        mb[t][j] = __uint_as_float(opaque(ok ? 0u : 0xff800000u));
      }
      const int lr = (int)(32 * tq + li) + 16 * t, tok = row0 + lr;
      oa[t] = opaque((lr >= 14 && tok < S) ? (unsigned)(tok >> 4) * A2_TT + (g * 16u + (unsigned)(tok & 15)) * 16u : 0x80000000u);
    }
    SCHED();
    bar();
    rep<0, 5>([&](auto I_) { st_m0(I_, IC<1>{}, IC<896>{}); NOP1(); st_go(I_, IC<1>{}, IC<896>{}); });
    wait_v<5>();
    bar();
    STAMP(1);
    f32x4 ac[7][2];
    bf16x8 xf[2][2], gf[2];
    s16x4 wl[2][6], wh[2][6];
    auto rd = [&](auto I_, auto HB_) {
      constexpr int I = decltype(I_)::value, HB = decltype(HB_)::value, hs = HB & 1;
      constexpr unsigned bo = ((HB >> 1) % 3) * STG;
      if constexpr (I == 2 || I == 3) xf[hs][I - 2] = lds128(L, xa[hs] + (I - 2) * REG + bo);
      else if constexpr (I < 14) {
        constexpr int c = I < 2 ? 0 : (I - 2) >> 1;
        if constexpr ((I & 1) == 0) wl[hs][c] = ldstr(L, wa[hs][c & 1] + ppos(2 + (c >> 1)) * 1024u + bo);
        else wh[hs][c] = ldstr(L, wa[hs][c & 1] + ppos(2 + (c >> 1)) * 1024u + bo + 512u);
      } else if constexpr (I == 14) gf[hs] = lds128(L, ga + HB * 64u);
    };
    rep<0, 15>([&](auto I_) { rd(I_, IC<0>{}); });
    rep<0, 14>([&](auto HB_) {
      constexpr int HB = decltype(HB_)::value, hs = HB & 1, s = HB >> 1;
      if constexpr (hs == 0) wait_l();
      rep<0, 14>([&](auto I_) {
        constexpr int I = decltype(I_)::value, c = I >> 1, t = I & 1;
        constexpr int K = HB == 0 ? ((I & 1) == 0 ? I >> 1 : -1) : (I % 3 == 1 ? I / 3 : -1);
        constexpr int E = K >= 0 ? sched(HB, K, 5) : -1;
        d_m0(IC<E>{}, IC<896>{});
        const bf16x8 wf = c < 6 ? cat(wl[hs][c < 6 ? c : 0], wh[hs][c < 6 ? c : 0]) : gf[hs];
        if constexpr (HB == 0) {
          if constexpr (c == 4 || c == 5) mma0(ac[c][t], xf[hs][t], wf);
          else mma0(ac[c][t], wf, xf[hs][t]);
        } else {
          if constexpr (c == 4 || c == 5) mma(ac[c][t], xf[hs][t], wf);
          else mma(ac[c][t], wf, xf[hs][t]);
        }
        SCHED();
        if constexpr (HB < 13) {
          if constexpr (I < 5) { rd(IC<2 * I>{}, IC<HB + 1>{}); rd(IC<2 * I + 1>{}, IC<HB + 1>{}); }
          else rd(IC<I + 5>{}, IC<HB + 1>{});
        } else if constexpr (I < 8) {
          b_rd(IC<(I >> 2)>{}, IC<(I & 3)>{}, IC<0>{});
          b_rd(IC<(I >> 2)>{}, IC<(I & 3)>{}, IC<1>{});
        }
        SCHED();
        d_go(IC<E>{}, IC<896>{});
      });
      if constexpr (hs == 0) {
        FSTAMP(16 + 3 * s);
        wait_v<(s == 5 ? 8 : (s == 6 ? 6 : 5))>();
        wait_l();
        FSTAMP(17 + 3 * s);
        bar();
        FSTAMP(18 + 3 * s);
      }
    });
    STAMP(2);
    asm volatile("s_nop 7" ::: "memory");
    SCHED();
    const unsigned pm = g == 3 ? 0u : 0xffffffffu;
    bf16x8 qf[2], kf[2];
    unsigned vh[2][2][2];
#pragma unroll
    for (int t = 1; t >= 0; --t) {
      kf[t] = asfrag(pk(ac[2][t][0], ac[2][t][1]), pk(ac[2][t][2], ac[2][t][3]), pm & pk(ac[3][t][0], ac[3][t][1]), pm & pk(ac[3][t][2], ac[3][t][3]));
#pragma unroll
      for (int c = 0; c < 2; ++c) {
        vh[t][c][0] = pk(ac[4 + c][t][0], ac[4 + c][t][1]);
        vh[t][c][1] = pk(ac[4 + c][t][2], ac[4 + c][t][3]);
      }
      if (t == 1) {
        const unsigned nw = ((w + 1) & 7) * REG;
        *(LAS bf16x8*)(L + nw + 4096u + lane * 16u) = kf[1];
        *(LAS v4u*)(L + nw + 5120u + lane * 16u) = v4u{vh[1][0][0], vh[1][0][1], vh[1][1][0], vh[1][1][1]};
      }
      qf[t] = asfrag(pk(ac[0][t][0], ac[0][t][1]), pk(ac[0][t][2], ac[0][t][3]), pm & pk(ac[1][t][0], ac[1][t][1]), pm & pk(ac[1][t][2], ac[1][t][3]));
    }
    wait_l();
    bar();
    const bf16x8 kp = lds128(L, wreg + 4096u + lane * 16u);
    const v4u vp = *(LAS v4u*)(L + wreg + 5120u + lane * 16u);
    const unsigned head = 2 * hp + hd;
    const bool odd = li & 1;
    const unsigned psh = odd ? 16u : 0u;
#pragma unroll
    for (int t = 1; t >= 0; --t) {
      const f32x4 z = {0.f, 0.f, 0.f, 0.f};
      const f32x4 s0 = mfma(t == 0 ? kp : kf[0], qf[t], z);
      const f32x4 s1 = mfma(kf[t], qf[t], z);
      const float gv = rbf(__builtin_amdgcn_rcpf(1.0f + __builtin_amdgcn_exp2f(rbf(ac[6][t][0]) * -1.4426950408889634f)));
      float sv[4];
      sv[0] = odd ? s0[1] : s0[0];
      sv[1] = odd ? s0[3] : s0[2];
      sv[2] = odd ? s1[1] : s1[0];
      sv[3] = odd ? s1[3] : s1[2];
      const float SC = 0.18898223650461362f * 1.4426950408889634f;
#pragma unroll
      for (int j = 0; j < 4; ++j) sv[j] = __builtin_fmaf(sv[j], SC, mb[t][j]);
      const float m = xmax(fmaxf(fmaxf(sv[0], sv[1]), fmaxf(sv[2], sv[3])));
#pragma unroll
      for (int j = 0; j < 4; ++j) sv[j] = __builtin_amdgcn_exp2f(sv[j] - m);
      const float inv = __builtin_amdgcn_rcpf(xsum((sv[0] + sv[1]) + (sv[2] + sv[3])));
      const bf16x8 pf = asfrag(pk(sv[0] * inv, 0.f) << psh, pk(sv[1] * inv, 0.f) << psh, pk(sv[2] * inv, 0.f) << psh, pk(sv[3] * inv, 0.f) << psh);
      f32x4 o[2];
#pragma unroll
      for (int c = 0; c < 2; ++c) {
        const bf16x8 vf = t == 0 ? asfrag(vp[2 * c], vp[2 * c + 1], vh[0][c][0], vh[0][c][1]) : asfrag(vh[0][c][0], vh[0][c][1], vh[1][c][0], vh[1][c][1]);
        o[c] = mfma(vf, pf, z) * gv;
      }
      const v4u ov = {pk(o[0][0], o[0][1]), pk(o[0][2], o[0][3]), pm & pk(o[1][0], o[1][1]), pm & pk(o[1][2], o[1][3])};
      st128(ov, rws, oa[t], head * 1024u);
      if (head == 0) {
        const unsigned tok = (unsigned)(row0 + (int)(32 * tq + li) + 16 * t);
        __builtin_amdgcn_raw_buffer_store_b32(__float_as_uint(gv), rws, (g == 0 && oa[t] < 0x80000000u) ? tok * 4u : 0x80000000u, WS_G, 16);
      }
    }
  }
  STAMP(3);
  const u64 tag = (gm_dispatch_id() + 1) * 0x9E3779B97F4A7C15ull;
  const unsigned fo_pub = ffn ? WS_FH + (2 * rb + (w >> 2)) * 256u + (4 * cs + (w & 3)) * 8u : WS_FO + (4 * rt + (w & 3)) * 128u + (2 * hp + (w >> 2)) * 8u;
  wait_v<0>();
  STAMP(4);
  if (lane == 0) __hip_atomic_store((GAS u64*)((char*)p.ws + fo_pub), tag, __ATOMIC_RELAXED, __HIP_MEMORY_SCOPE_AGENT);
  if (!has2) {
    STAMP_FLUSH();
    return;
  }
  rep<0, 16>([&](auto I_) { constexpr int I = decltype(I_)::value; b_rd(IC<2 + (I >> 3)>{}, IC<((I >> 1) & 3)>{}, IC<(I & 1)>{}); });
  SCHED();
  bar();
  wait_l();
  rep<16, 36>([&](auto N_) { b_m0(N_); NOP1(); b_go(N_); });
  STAMP(5);

  const unsigned T0 = 64 * tb;
  if (w == POLL_WAVE) {
    const unsigned g0 = 4 * (T0 / QROWS) + (T0 % QROWS + 14) / 32;
    const unsigned T1 = T0 + 63;
    const unsigned g1 = 4 * (T1 / QROWS) + (T1 % QROWS + 14) / 32;
    GAS u64* fa = (GAS u64*)((char*)p.ws + WS_FO + min(g0 + g, g1) * 128u + li * 8u);
    GAS u64* fh = (GAS u64*)((char*)p.ws + WS_FH + tb * 256u + min(lane, 27u) * 8u);
#pragma unroll 1
    for (unsigned spin = 0; spin < SPIN_CAP; ++spin) {
      const u64 v0 = __hip_atomic_load(fa, __ATOMIC_RELAXED, __HIP_MEMORY_SCOPE_AGENT);
      const u64 v1 = __hip_atomic_load(fh, __ATOMIC_RELAXED, __HIP_MEMORY_SCOPE_AGENT);
      if (__all(v0 == tag && v1 == tag)) break;
      __builtin_amdgcn_s_sleep(POLL_SLEEP);
    }
  }
  unsigned va[4];
#pragma unroll
  for (int t = 0; t < 4; ++t) va[t] = opaque(lane * 16u + t * A2_TT);
  const unsigned aol = 4 * tb * A2_TT + (w == 1 ? 16u : 9u * w) * 1024u, aoh = 4 * tb * A2_TT + (w == 1 ? 7u : 9u * w) * 1024u;
  SCHED();
  bar();
  STAMP(6);
  auto a_load = [&](int j, int t) {
    return j < 2 ? __builtin_amdgcn_raw_buffer_load_b128(rws, va[t] + j * 1024u, aol, 16)
                 : __builtin_amdgcn_raw_buffer_load_b128(rws, va[t] + ((j - 2) & 3) * 1024u, aoh + (j < 6 ? 2048u : 6144u), 16);
  };
  constexpr int WIN = WINDOW;
  v4u af[WIN][4];
  float gq[4] = {0.f, 0.f, 0.f, 0.f};
  if (PRIO) __builtin_amdgcn_s_setprio(3);
#pragma unroll
  for (int j = 0; j < WIN; ++j) {
#pragma unroll
    for (int t = 0; t < 4; ++t) af[j][t] = a_load(j, t);
    if (j == 1 && w == 1) {
#pragma unroll
      for (int t = 0; t < 4; ++t) gq[t] = __uint_as_float(__builtin_amdgcn_raw_buffer_load_b32(rws, (T0 + 16 * t + li) * 4u, WS_G, 16));
    }
    SCHED();
  }
  f32x4 acc[4][4];
  v4u b2v;
  rep<0, 9>([&](auto J_) {
    constexpr int j = decltype(J_)::value;
    if (PRIO && j + WIN == 4) __builtin_amdgcn_s_setprio(2);
    if (PRIO && j + WIN == 6) __builtin_amdgcn_s_setprio(1);
    if (PRIO && j + WIN == 8) __builtin_amdgcn_s_setprio(0);
    if (j == 2 && w == 1) {
      asm volatile("s_nop 7" ::: "memory");
      SCHED();
#pragma unroll
      for (int t = 0; t < 4; ++t) {
        const float og = rbf(1.0f - gq[t]);
#pragma unroll
        for (int c = 0; c < 4; ++c) acc[c][t] *= og;
      }
      SCHED();
      asm volatile("s_nop 1" ::: "memory");
    }
    if constexpr (j >= 4) wait_l();
    rep<0, 4>([&](auto T_) {
      constexpr int t = decltype(T_)::value;
      const bf16x8 a = __builtin_bit_cast(bf16x8, af[j % WIN][t]);
#pragma unroll
      for (int c = 0; c < 4; ++c) {
        if constexpr (j == 0) mma0(acc[c][t], cat(bl[0][c], bh[0][c]), a);
        else mma(acc[c][t], cat(bl[j & 3][c], bh[j & 3][c]), a);
      }
      SCHED();
      if constexpr (j + WIN < 9) af[j % WIN][t] = a_load(j + WIN, t);
      if constexpr (j >= 1 && j + 3 < 9) {
        b_rd(IC<j + 3>{}, T_, IC<0>{});
        b_rd(IC<j + 3>{}, T_, IC<1>{});
      }
      SCHED();
    });
    if constexpr (j == 6) b2v = *(const GAS v4u*)((const char*)p.b2 + ct * 128u + (tid & 7) * 16u);
  });
  STAMP(7);
  asm volatile("s_nop 7" ::: "memory");
  SCHED();
#pragma unroll
  for (int t = 0; t < 4; ++t)
#pragma unroll
    for (int c = 0; c < 4; ++c) {
      const unsigned lp = g * 16u + ((li + 4u * ((2u * c + (g >> 1)) & 3u)) & 15u);
      *(LAS f32x4*)(L + wreg + (t * 4 + c) * 1024u + lp * 16u) = acc[c][t];
    }
  if (w == 1 && g == 0) {
#pragma unroll
    for (int t = 0; t < 4; ++t) *(LAS float*)(L + REG + SPARE + (t * 16 + li) * 4u) = gq[t];
  }
  wait_l();
  bar();
  STAMP(8);
  {
    const unsigned tk = tid >> 3, q = tid & 7;
    const unsigned n = (tk >> 4) * 4u + (q >> 1), g0 = 2 * (q & 1), i = tk & 15;
    const unsigned ad = n * 1024u + (g0 * 16u + ((i + 4u * (q & 3u)) & 15u)) * 16u;
    f32x4 alo = *(LAS f32x4*)(L + ad) + *(LAS f32x4*)(L + ad + REG);
    f32x4 ahi = *(LAS f32x4*)(L + ad + 256u) + *(LAS f32x4*)(L + ad + REG + 256u);
    f32x4 lo = {bflo(b2v[0]), bfhi(b2v[0]), bflo(b2v[1]), bfhi(b2v[1])}, hi = {bflo(b2v[2]), bfhi(b2v[2]), bflo(b2v[3]), bfhi(b2v[3])};
#pragma unroll
    for (int k = 2; k < 8; ++k) {
      lo += *(LAS f32x4*)(L + k * REG + ad);
      hi += *(LAS f32x4*)(L + k * REG + ad + 256u);
    }
    const float og = rbf(1.0f - *(LAS float*)(L + REG + SPARE + tk * 4u));
    alo += og * lo;
    ahi += og * hi;
    const v4u o = {pk(alo[0], alo[1]), pk(alo[2], alo[3]), pk(ahi[0], ahi[1]), pk(ahi[2], ahi[3])};
    *(GAS v4u*)((char*)p.out + (64 * tb + tk) * 896u + ct * 128u + q * 16u) = o;
  }
  STAMP(9);
  STAMP_FLUSH();
}
}

extern "C" void kernel_launch(void* const* d_in, const int* in_sizes, int n_in, void* d_out, int out_size, void* d_ws, size_t ws_size,
                              hipStream_t stream) {
  gm::Params p;
  p.x = d_in[0];
  p.wqkv = d_in[1];
  p.wo = d_in[2];
  p.w1 = d_in[4];
  p.b1 = d_in[5];
  p.w2 = d_in[6];
  p.b2 = d_in[7];
  p.gw = d_in[8];
  p.out = d_out;
  p.ws = d_ws;
  gm::gm_kernel<<<256, 512, 0, stream>>>(p);
}
```

```cpp
#include <hip/hip_runtime.h>
#include <stdint.h>

typedef short bf16x8 __attribute__((ext_vector_type(8)));
typedef short s16x4 __attribute__((ext_vector_type(4)));
typedef float f32x4 __attribute__((ext_vector_type(4)));
typedef unsigned v4u __attribute__((ext_vector_type(4)));
typedef unsigned v2u __attribute__((ext_vector_type(2)));
typedef __bf16 bf2_t __attribute__((ext_vector_type(2)));
typedef float f32x2 __attribute__((ext_vector_type(2)));
#define LAS __attribute__((address_space(3)))
#define GAS __attribute__((address_space(1)))
#define DI __device__ __forceinline__
typedef unsigned long long u64;
typedef __amdgpu_buffer_rsrc_t rsrc_t;
typedef u64 v8q __attribute__((ext_vector_type(8)));
typedef u64 v2q __attribute__((ext_vector_type(2)));

#define CODE_PF 23552
#define TOUCH 1
#define TOUCH2_HB 3
#define WINDOW 2
#define PRIO 1
#define POLL_SLEEP 1
#define FLAG_STRIDE 256
#define POLL_AUX 16
#define FLAG_ST 2
#define TMODE 1
#define TCNT 0x0C31151C38101520ull
#define TFIRST 0x00003F5400000000ull
#define TSH 2u
#define POLL_WAVE 0
#define QPRE 6
#define QFAST 0
#define NA 0
#define NMF 5
#define NMQ 4
#define FBAR 1
#define DEFER 0
#define PBAR 1

extern "C" __device__ u64 gm_dispatch_id() __asm("llvm.amdgcn.dispatch.id");

namespace gm {
constexpr int S = 2048, D = 448, F = 1792, NQKV = 1344;
constexpr int KS_A = 16, KS = 72;
constexpr unsigned A2_TT = KS * 1024;
constexpr unsigned WS_G = 128 * A2_TT, WS_F = WS_G + 8 * 8192, WS_END = WS_F + 16 * 1024;
constexpr unsigned REG = 20480, STG = 6144, SPARE = 3 * STG;
constexpr int LDS_BYTES = 163840;
constexpr unsigned GW_OFF = 2 * REG + 5120;
constexpr unsigned SPIN_CAP = 1u << 16;

struct Params {
  const void *x, *wqkv, *wo, *w1, *b1, *w2, *b2, *gw;
  void *out, *ws;
};
template <int N> struct IC { static constexpr int value = N; };

DI rsrc_t mk(const void* p, unsigned n) { return __builtin_amdgcn_make_buffer_rsrc((void*)p, 0, n, 0x00020000); }
template <unsigned C> DI void set_m0(unsigned base) { asm volatile("s_add_u32 m0, %0, %1" ::"s"(base), "i"(C) : "scc", "memory"); }
template <unsigned IMM> DI void dma_go(unsigned voff, rsrc_t rs, unsigned soff) {
  asm volatile("buffer_load_dwordx4 %0, %1, %2 offen offset:%3 lds" ::"v"(voff), "s"(rs), "s"(soff), "i"(IMM) : "memory");
}
template <unsigned IMM> DI void dma_go0(unsigned voff, rsrc_t rs) {
  asm volatile("buffer_load_dwordx4 %0, %1, 0 offen offset:%2 lds" ::"v"(voff), "s"(rs), "i"(IMM) : "memory");
}
template <unsigned IMM> DI void ld128(v4u& d, unsigned voff, rsrc_t rs, unsigned soff) {
  asm volatile("buffer_load_dwordx4 %0, %1, %2, %3 offen offset:%4 sc1" : "=v"(d) : "v"(voff), "s"(rs), "s"(soff), "i"(IMM) : "memory");
}
template <unsigned IMM> DI void ld32(float& d, unsigned voff, rsrc_t rs, unsigned soff) {
  asm volatile("buffer_load_dword %0, %1, %2, %3 offen offset:%4 sc1" : "=v"(d) : "v"(voff), "s"(rs), "s"(soff), "i"(IMM) : "memory");
}
DI void ld64(v2u& d, unsigned voff, rsrc_t rs, unsigned soff) {
  asm volatile("buffer_load_dwordx2 %0, %1, %2, %3 offen sc1" : "+v"(d) : "v"(voff), "s"(rs), "s"(soff) : "memory");
}
template <int N, class T> DI void wait_for(T& v) { asm volatile("s_waitcnt vmcnt(%1)" : "+v"(v) : "i"(N) : "memory"); }
DI void touch(unsigned lds, unsigned voff, u64 base) {
  asm volatile("s_mov_b32 m0, %0\n\ts_nop 0\n\tglobal_load_lds_dword %1, %2" ::"s"(lds), "v"(voff), "s"(base) : "memory");
}
#define NOP1() asm volatile("s_nop 0")
DI f32x4 mfma(bf16x8 a, bf16x8 b, f32x4 c) { return __builtin_amdgcn_mfma_f32_16x16x32_bf16(a, b, c, 0, 0, 0); }
DI void mma(f32x4& c, bf16x8 a, bf16x8 b) { asm volatile("v_mfma_f32_16x16x32_bf16 %0, %1, %2, %0" : "+v"(c) : "v"(a), "v"(b)); }
DI void mma0(f32x4& c, bf16x8 a, bf16x8 b) { asm volatile("v_mfma_f32_16x16x32_bf16 %0, %1, %2, 0" : "=&v"(c) : "v"(a), "v"(b)); }
DI void mmac(f32x4& d, bf16x8 a, bf16x8 b, f32x4 c) { asm volatile("v_mfma_f32_16x16x32_bf16 %0, %1, %2, %3" : "=&v"(d) : "v"(a), "v"(b), "v"(c)); }
DI float relu(float v) {
  float r;
  asm("v_max_f32 %0, 0, %1" : "=v"(r) : "v"(v));
  return r;
}
DI float fmx(float a, float b) {
  float r;
  asm("v_max_f32 %0, %1, %2" : "=v"(r) : "v"(a), "v"(b));
  return r;
}
DI unsigned pk(float lo, float hi) {
  f32x2 v = {lo, hi};
  return __builtin_bit_cast(unsigned, __builtin_convertvector(v, bf2_t));
}
DI float rbf(float f) { return __uint_as_float(pk(f, 0.f) << 16); }
DI float bflo(unsigned u) { return __uint_as_float(u << 16); }
DI float bfhi(unsigned u) { return __uint_as_float(u & 0xffff0000u); }
DI bf16x8 asfrag(unsigned a, unsigned b, unsigned c, unsigned d) {
  v4u v = {a, b, c, d};
  return __builtin_bit_cast(bf16x8, v);
}
DI void st128(v4u v, rsrc_t rs, unsigned voff, unsigned soff) {
  __builtin_amdgcn_raw_buffer_store_b128(v, rs, voff, soff, 16);
  __builtin_amdgcn_sched_barrier(0);
  asm volatile("s_nop 1");
  __builtin_amdgcn_sched_barrier(0);
}
DI bf16x8 cat(s16x4 lo, s16x4 hi) { return __builtin_shufflevector(lo, hi, 0, 1, 2, 3, 4, 5, 6, 7); }
DI bf16x8 lds128(LAS char* L, unsigned a) { return *(LAS bf16x8*)(L + a); }
DI s16x4 ldstr(LAS char* L, unsigned a) { return __builtin_amdgcn_ds_read_tr16_b64_v4i16((LAS s16x4*)(L + a)); }
DI unsigned opaque(unsigned v) {
  asm volatile("" : "+v"(v));
  return v;
}
DI unsigned sopaque(unsigned v) {
  asm volatile("" : "+s"(v));
  return v;
}
DI float xmax(float v) {
  auto a = __builtin_amdgcn_permlane32_swap(__float_as_uint(v), __float_as_uint(v), false, false);
  v = fmx(__uint_as_float(a[0]), __uint_as_float(a[1]));
  auto b = __builtin_amdgcn_permlane16_swap(__float_as_uint(v), __float_as_uint(v), false, false);
  return fmx(__uint_as_float(b[0]), __uint_as_float(b[1]));
}
DI float xsum(float v) {
  auto a = __builtin_amdgcn_permlane32_swap(__float_as_uint(v), __float_as_uint(v), false, false);
  v = __uint_as_float(a[0]) + __uint_as_float(a[1]);
  auto b = __builtin_amdgcn_permlane16_swap(__float_as_uint(v), __float_as_uint(v), false, false);
  return __uint_as_float(b[0]) + __uint_as_float(b[1]);
}
#define SCHED() __builtin_amdgcn_sched_barrier(0)
#define STAMP(k)
#define STAMP_FLUSH()
#define FSTAMP(k)
template <int N> DI void wait_v() { asm volatile("s_waitcnt vmcnt(%0)" ::"i"(N) : "memory"); }
DI void wait_l() {
  SCHED();
  __builtin_amdgcn_s_waitcnt(0xC07F);
  asm volatile("" ::: "memory");
  SCHED();
}
DI void wait_args(v8q& a, v2q& b, u64& c) {
  asm volatile("s_waitcnt lgkmcnt(0)" ::: "memory");
  SCHED();
  asm volatile("" : "+s"(a), "+s"(b), "+s"(c)::"memory");
}
DI void keep(f32x4 v) { asm volatile("" ::"v"(v)); }
DI void bar() { asm volatile("s_barrier" ::: "memory"); }

constexpr unsigned hloc(int h) {
  h = h >= 10 ? h - 10 : h;
  return h == 0 ? SPARE : (h < 4 ? STG + (h - 1) * 2048u : (h < 7 ? 2 * STG + (h - 4) * 2048u : (h - 7) * 2048u));
}
constexpr unsigned ppos(int i) { return i < 2 ? i + 1 : (i == 2 ? 0 : i); }

constexpr int q_piece(int k, bool ffn) { return ffn ? k : (k + 5) % 6; }
constexpr int q_item(int i, bool ffn) { return i < 36 ? 8 * (1 + i / 6) + q_piece(i % 6, ffn) : 100 + (i - 36); }
constexpr int q_from(int item) {
  if (item < 100) return (item >> 3) <= 2 ? 0 : 2 * ((item >> 3) - 3) + 1;
  const int h = (item - 100) >> 1;
  return h == 0 ? 1 : (h < 4 ? 9 : (h < 7 ? 11 : 13));
}
constexpr int q_slots(int hb) { return hb < QFAST ? 7 : 4; }
constexpr int q_slot(int hb, int i) { return hb < QFAST ? ((i & 1) && i < 14 ? i >> 1 : -1) : ((i & 3) == 1 ? i >> 2 : -1); }
constexpr int q_start(int hb) {
  int i = QPRE;
  for (int h = 0; h < hb; ++h)
    for (int k = 0; k < q_slots(h); ++k)
      if (i < 52 && q_from(q_item(i, true)) <= h) ++i;
  return i;
}
constexpr int q_at(int hb, int i, bool ffn) {
  const int k = q_slot(hb, i);
  return k >= 0 && q_start(hb) + k < q_start(hb + 1) ? q_item(q_start(hb) + k, ffn) : -1;
}
constexpr int q_count(int from, int to, bool ffn) {
  int n = 0;
  for (int i = from; i < to; ++i) n += (ffn || i >= 36 || i % 6 != 0) ? 1 : 0;
  return n;
}
constexpr int q_wait(int s, bool ffn) { return q_count(s < 6 ? 6 * (s + 1) : 44, q_start(2 * s + 1), ffn); }
static_assert(q_start(14) == 52 && q_start(1) >= 6 && q_start(3) >= 12 && q_start(5) >= 18 && q_start(7) >= 24 && q_start(9) >= 30 && q_start(11) >= 36 && q_start(13) >= 44, "order");
constexpr int r_unit(int i) { return i < 2 ? 16 + i : (i < 6 ? 18 + i : 12 + i); }
constexpr int WIN = WINDOW;
static_assert(WIN == 2, "the landing of the in-loop weight units is derived for a window of 2");
constexpr int p2_unit(int j, int t, int nt, bool inl) { return inl && j * nt + t < 12 ? 24 + j * nt + t : -1; }
constexpr int p2_after(int j, int t, int nt, bool inl) {
  return (p2_unit(j, t, nt, inl) >= 0 ? 1 : 0) + (j + WIN < 9 ? 1 : 0)
         + (j + WIN == 7 && t == 0 ? 1 : 0)
         + (j + WIN == 5 && t == 0 ? 1 : 0);
}
constexpr int p2_before(int j, int t, int nt, bool inl) {
  int n = nt * WIN;
  for (int i = 0; i < nt * j + t; ++i) n += p2_after(i / nt, i % nt, nt, inl);
  return n;
}
constexpr int p2_pos(int j, int t, int nt, bool inl) {
  return j < WIN ? nt * j + t : p2_before(j - WIN, t, nt, inl) + (p2_unit(j - WIN, t, nt, inl) >= 0 ? 1 : 0);
}
template <int I, int N, class Fn> DI void rep(Fn&& f) {
  if constexpr (I < N) {
    f(IC<I>{});
    rep<I + 1, N>(f);
  }
}

__global__ __launch_bounds__(512, 2) void gm_kernel(Params pin) {
  __shared__ __attribute__((aligned(1024))) char smem[LDS_BYTES];
  LAS char* L = (LAS char*)smem;
  v8q ka;
  v2q kb2;
  asm volatile("s_load_dwordx16 %0, %2, 0x0\n\ts_load_dwordx4 %1, %2, 0x40" : "=&s"(ka), "=&s"(kb2) : "s"(__builtin_amdgcn_kernarg_segment_ptr()));
  const unsigned tid = threadIdx.x, lane = tid & 63;
  const unsigned b = blockIdx.x, xcd = b & 7, slot = b >> 3;
  if (tid < CODE_PF / 1024) {
    u64 pc;
    asm volatile("s_getpc_b64 %0" : "=s"(pc));
    touch(SPARE + 1024u, ((slot & 7) * (CODE_PF / 1024) + tid) * 128u, pc);
  }
  const unsigned w = __builtin_amdgcn_readfirstlane(tid >> 6);
  u64 tptr;
  asm volatile("s_load_dwordx2 %0, %1, %2" : "=&s"(tptr) : "s"(__builtin_amdgcn_kernarg_segment_ptr()), "s"(((0x25130130u >> (4 * w)) & 7u) * 8u));
  const unsigned wreg = w * REG;
  const unsigned li = lane & 15, g = lane >> 4, rr = lane >> 3, pos = lane & 7;
  const unsigned rq = xcd >> 1, side = xcd & 1;
  const unsigned nffn = 16 - 4 * side;
  const bool ffn = slot < nffn, qkv = slot >= 16;
  const unsigned tcnt = (unsigned)(TCNT >> (8 * w)) & 0xffu;
  const unsigned tfirst = ((unsigned)(TFIRST >> (8 * w)) & 0xffu) * 64u;
  const unsigned tsh = w ? 6u : TSH;
  const unsigned tln = opaque((sopaque((32 * xcd + slot) * (w ? tcnt : 56u) + tfirst) + (lane >> tsh) * 7u + (lane & ((1u << tsh) - 1u))) * 128u);
  auto do_touch = [&]() {
    if (lane < tcnt) touch(wreg + SPARE + 1024u, tln, tptr);
  };

  Params p;
  rsrc_t rws, rw2, rwo;
  unsigned vb[2], ba[4], fo_pub;
  const unsigned bo2 = w * (224u * 896u), boo = w * (56u * 896u);
  const unsigned ct = slot < 12 ? slot >> 2 : (slot < 16 ? slot - 9 : (slot >> 2) - 1);
  const unsigned tt0 = 16 * xcd + (slot < 12 ? 4 * (slot & 3) : (slot < 16 ? 0u : 4 + 3 * (slot & 3)));
  const unsigned q4 = (lane >> 2) & 3, p4 = lane & 3;
  const unsigned fsw = (q4 >> 1) | ((g & 1) << 1);
  s16x4 bl[4][4], bh[4][4];
  auto b_m0 = [&](auto N_) {
    constexpr int N = decltype(N_)::value;
    set_m0<hloc(N >> 1) + (N & 1) * 1024u>(wreg);
  };
  auto b_go = [&](auto N_) {
    constexpr int N = decltype(N_)::value, j = N >> 2;
    if constexpr (j < 7) dma_go<0>(vb[N & 1], rw2, bo2 + (2 * j + ((N >> 1) & 1)) * (16u * 896u));
    else dma_go<0>(vb[N & 1], rwo, boo + ((j - 7) * 28u + ((N >> 1) & 1) * 16u) * 896u);
  };
  auto b_dma = [&](auto N_) { b_m0(N_); NOP1(); b_go(N_); };
  auto r_dma = [&](auto I_) { b_dma(IC<r_unit(decltype(I_)::value)>{}); };
  auto b_rd = [&](auto J_, auto C_, auto E_) {
    constexpr int J = decltype(J_)::value, C = decltype(C_)::value, E = decltype(E_)::value;
    if constexpr (E == 0) bl[J & 3][C] = ldstr(L, ba[C] + hloc(2 * J));
    else bh[J & 3][C] = ldstr(L, ba[C] + hloc(2 * J + 1));
  };
  auto setup2 = [&](auto K_) {
    constexpr int K = decltype(K_)::value;
    if constexpr (K == 0) {
      rw2 = mk(p.w2, F * D * 2);
      rwo = mk(p.wo, D * D * 2);
    } else if constexpr (K == 1) {
    } else if constexpr (K == 2) {
      vb[0] = opaque(rr * 896u + ((pos ^ (((rr >> 1) & 3) << 1)) << 4) + ct * 128u);
      vb[1] = opaque(vb[0] + 8u * 896u);
    } else if constexpr (K < 7) {
      ba[K - 3] = opaque(wreg + (4 * g + q4) * 128u + ((2u * ((K - 3) ^ fsw) + (p4 >> 1)) << 4) + (p4 & 1) * 8u);
    }
  };

  auto set_params = [&]() {
    p.x = (const void*)ka[0]; p.wqkv = (const void*)ka[1]; p.wo = (const void*)ka[2]; p.w1 = (const void*)ka[3];
    p.b1 = (const void*)ka[4]; p.w2 = (const void*)ka[5]; p.b2 = (const void*)ka[6]; p.gw = (const void*)ka[7];
    p.out = (void*)kb2[0]; p.ws = (void*)kb2[1];
    rws = mk(p.ws, WS_END);
  };
  auto phase1 = [&](auto FFN_) {
    constexpr bool FFN = decltype(FFN_)::value;
    constexpr int NP = FFN ? 6 : 5;
    constexpr unsigned LDW = FFN ? 3584u : 2688u, WUS = FFN ? 128u : 896u;
    const unsigned rb = 4 * rq + (slot & 3), cs = side * 4 + (slot >> 2), hp = side * 4 + ((slot >> 2) & 3);
    unsigned vx[3];
#pragma unroll
    for (int h = 0; h < 2; ++h) vx[h] = opaque((128 * rb + 16 * w + 8 * h + rr) * 896u + ((pos ^ (4u * h + (rr >> 1))) << 4));
    if (!FFN) vx[2] = opaque(((rb ? 128 * rb - 16 : 0u) + 8 * (w & 1) + rr) * 896u + ((pos ^ (4u * (w & 1) + (rr >> 1))) << 4));
    unsigned wch = pos ^ ((((rr >> 1) & 1) | ((w & 1) << 1)) << 1);
    if (!FFN) wch = min(wch, 6u);
    const unsigned vw = opaque((8 * w + rr) * LDW + (wch << 4) + (FFN ? cs * 512u : hp * 112u));
    SCHED();
    wait_args(ka, kb2, tptr);
    SCHED();
    set_params();
    STAMP(10);
    const rsrc_t rx = mk(p.x, S * D * 2);
    const rsrc_t rw = mk(FFN ? p.w1 : p.wqkv, FFN ? D * F * 2 : D * NQKV * 2);
    auto d_m0 = [&](auto E_) {
      constexpr int E = decltype(E_)::value, I = E & 7, ST = E >> 3;
      if constexpr (E >= 100) b_m0(IC<E - 100>{});
      else if constexpr (E >= 0) set_m0<(ST % 3) * STG + ppos(I) * 1024u - ((I < 2 || (!FFN && I == 5)) ? ST * 128u : (I - 2) * WUS)>(wreg);
    };
    auto d_go = [&](auto E_) {
      constexpr int E = decltype(E_)::value, I = E & 7, ST = E >> 3;
      if constexpr (E >= 100) b_go(IC<E - 100>{});
      else if constexpr (E >= 0) {
        if constexpr (I < 2) dma_go0<ST * 128u>(vx[I], rx);
        else if constexpr (!FFN && I == 5) { if (w < 2) dma_go0<ST * 128u>(vx[2], rx); }
        else if constexpr (ST == 0) dma_go0<(I - 2) * WUS>(vw, rw);
        else dma_go<(I - 2) * WUS>(vw, rw, ST * 64u * LDW);
      }
    };
    if (TOUCH && w < 3) do_touch();
    rep<0, 6>([&](auto I_) { d_m0(I_); NOP1(); d_go(I_); });
    if (w == 2) {
      set_m0<FFN ? SPARE : GW_OFF>(0);
      NOP1();
      dma_go0<0>(lane * 16, mk(FFN ? (const char*)p.b1 + cs * 512u : (const char*)p.gw, FFN ? 512 : 896));
    }
    SCHED();
    STAMP(11);
    const unsigned xa0 = 1024u + (li >> 3) * 1024u + (li & 7) * 128u + ((g ^ ((li >> 1) & 7)) << 4);
    auto second_stage = [&]() {
      SCHED();
      STAMP(12);
      if (PBAR != 0 && QPRE != 0) bar();
      rep<0, QPRE>([&](auto I_) { constexpr int E = q_item(decltype(I_)::value, FFN); d_m0(IC<E>{}); NOP1(); d_go(IC<E>{}); });
      STAMP(14);
      if (TOUCH && w >= 3 && w < 6) {
        do_touch();
        wait_v<q_count(0, QPRE, FFN) + 1>();
      } else {
        wait_v<q_count(0, QPRE, FFN)>();
      }
      bar();
      STAMP(1);
    };
    auto mid = [&](auto S_) {
      constexpr int s = decltype(S_)::value;
      FSTAMP(16 + 3 * s);
      wait_v<q_wait(s, FFN)>();
      wait_l();
      FSTAMP(17 + 3 * s);
      bar();
      FSTAMP(18 + 3 * s);
    };

    if constexpr (FFN) {
      const unsigned mh = w >> 2, nq = w & 3;
      unsigned xa[2][2], wa[2][4];
#pragma unroll
      for (int hs = 0; hs < 2; ++hs) {
#pragma unroll
        for (int t2 = 0; t2 < 2; ++t2) xa[hs][t2] = opaque((xa0 ^ (hs * 64u)) + (mh * 4u + t2 * 2u) * REG);
#pragma unroll
        for (int c = 0; c < 4; ++c)
          wa[hs][c] = opaque((4u * hs + g) * REG + (nq ? 2u + nq : 0u) * 1024u + q4 * 128u + ((2u * (c ^ fsw) + (p4 >> 1)) << 4) + (p4 & 1) * 8u);
      }
      second_stage();
      bf16x8 xf[2][4];
      s16x4 wl[2][4], wh[2][4];
      f32x4 acc[4][4];
      auto rd = [&](auto I_, auto HB_) {
        constexpr int I = decltype(I_)::value, HB = decltype(HB_)::value, hs = HB & 1;
        constexpr unsigned bo = ((HB >> 1) % 3) * STG;
        if constexpr (I >= 2 && I < 6) xf[hs][I - 2] = lds128(L, xa[hs][(I - 2) >> 1] + ((I - 2) & 1) * REG + bo);
        else if constexpr (I < 12) {
          constexpr int c = I < 2 ? 0 : (I - 4) >> 1;
          if constexpr ((I & 1) == 0) wl[hs][c] = ldstr(L, wa[hs][c] + bo);
          else wh[hs][c] = ldstr(L, wa[hs][c] + bo + 512u);
        }
      };
      f32x4 bv[4];
#pragma unroll
      for (int c = 0; c < 4; ++c) {
        const v2u bb = *(LAS v2u*)(L + SPARE + nq * 128u + c * 32u + g * 8u);
        bv[c] = f32x4{bflo(bb.x), bfhi(bb.x), bflo(bb.y), bfhi(bb.y)};
      }
      rep<0, 12>([&](auto I_) { rd(I_, IC<0>{}); });
#pragma unroll
      for (int c = 0; c < 4; ++c) asm volatile("" : "+v"(bv[c]));
      unsigned so = 0;
      auto epi = [&](int t, int u) {
        float hv[8];
#pragma unroll
        for (int j = 0; j < 8; ++j) {
          const float r = relu(acc[2 * u + (j >> 2)][t][j & 3]);
          hv[j] = r * r;
        }
        const v4u o = {pk(hv[0], hv[1]), pk(hv[2], hv[3]), pk(hv[4], hv[5]), pk(hv[6], hv[7])};
        st128(o, rws, lane * 16u + u * 1024u, so + t * A2_TT);
      };
      rep<0, 14>([&](auto HB_) {
        constexpr int HB = decltype(HB_)::value, hs = HB & 1;
        if constexpr (hs == 0) {
          wait_l();
          if constexpr (HB == 0) asm volatile("s_nop 1");
        }
        rep<0, 16>([&](auto I_) {
          constexpr int I = decltype(I_)::value;
          constexpr int E = q_at(HB, I, FFN);
          d_m0(IC<E>{});
          if constexpr (HB < 13) {
            if constexpr (HB == 0) mmac(acc[I >> 2][I & 3], cat(wl[hs][I >> 2], wh[hs][I >> 2]), xf[hs][I & 3], bv[I >> 2]);
            else mma(acc[I >> 2][I & 3], cat(wl[hs][I >> 2], wh[hs][I >> 2]), xf[hs][I & 3]);
            SCHED();
            rd(I_, IC<HB + 1>{});
            if constexpr (I == 14 && HB < 7) setup2(HB_);
            if constexpr (I == 14 && HB == 7) {
              so = sopaque((8 * rb + 4 * mh) * A2_TT + (KS_A + 8 * cs + 2 * nq) * 1024u);
              fo_pub = sopaque(WS_F + rb * 1024u + cs * 64u);
            }
            if constexpr (I == 15 && HB == TOUCH2_HB && TOUCH) { if (w >= 6) do_touch(); }
          } else {
            mma(acc[I & 3][I >> 2], cat(wl[hs][I & 3], wh[hs][I & 3]), xf[hs][I >> 2]);
            SCHED();
            if constexpr (I < 4) rep<0, 4>([&](auto Q_) { constexpr int Q = decltype(Q_)::value; b_rd(IC<(Q >> 1)>{}, I_, IC<(Q & 1)>{}); });
            if constexpr (I >= 6 && (I & 1) == 0) epi((I - 6) >> 2, ((I - 6) >> 1) & 1);
          }
          SCHED();
          d_go(IC<E>{});
        });
        if constexpr (hs == 0) mid(IC<(HB >> 1)>{});
      });
      STAMP(2);
      asm volatile("s_nop 7" ::: "memory");
      SCHED();
      epi(2, 1);
      epi(3, 0);
      epi(3, 1);
    } else {
      const unsigned tq = w & 3, hd = w >> 2;
      unsigned xa[2], wa[2][2];
#pragma unroll
      for (int hs = 0; hs < 2; ++hs) {
        xa[hs] = opaque((xa0 ^ (hs * 64u)) + tq * 2u * REG);
#pragma unroll
        for (int c = 0; c < 2; ++c) {
          const unsigned bo = 56u * hd + 32u * c + 8u * p4;
          wa[hs][c] = opaque((4u * hs + g) * REG + q4 * 128u + (((bo >> 4) ^ (fsw << 1)) << 4) + (bo & 8u));
        }
      }
      const unsigned ga = opaque(GW_OFF + g * 16u);
      unsigned xh[2], wq[2];
#pragma unroll
      for (int hs = 0; hs < 2; ++hs) {
        xh[hs] = opaque((li >> 3) * REG + 5120u + (li & 7) * 128u + (((4u * hs + g) ^ ((li >> 1) & 7)) << 4));
        const unsigned bo = 56u * hd + 32u * (tq & 1) + 8u * p4;
        wq[hs] = opaque((4u * hs + g) * REG + q4 * 128u + (((bo >> 4) ^ (fsw << 1)) << 4) + (bo & 8u) + (3u + (tq >> 1)) * 1024u);
      }
      second_stage();
      float mb[2][4];
      const unsigned head = 2 * hp + hd;
      unsigned oa = 0, hwa = 0, nwa = 0, pm = 0;
      auto setupq = [&](auto K_) {
        constexpr int K = decltype(K_)::value, t = K & 1;
        if constexpr (K < 2) {
#pragma unroll
          for (int j = 0; j < 4; ++j) {
            const int kap = 16 * (j >> 1) + (int)(4 * g) + 2 * (j & 1) + (int)(li & 1) - 16;
            const int dl = (int)li - kap;
            const bool ok = (dl >= 0) && (dl <= 14) && (t == 1 || j >= 2 || (rb | tq) != 0);
            mb[t][j] = __uint_as_float(opaque(ok ? 0u : 0xff800000u));
          }
        } else if constexpr (K == 2) {
          oa = sopaque((8 * rb + 2 * tq) * A2_TT + head * 1024u);
          fo_pub = sopaque(WS_F + rb * 1024u + (7 + hp) * 64u);
        } else if constexpr (K == 3) {
          hwa = opaque(hd * 4u * REG + (tq < 2 ? 4096u + lane * 16u + tq * 8u : 4096u + tq * 512u + li * 32u + g * 8u));
          nwa = opaque(wreg + (tq < 3 ? REG + 4096u : 2048u) + lane * 16u);
          pm = opaque(g == 3 ? 0u : 0xffffffffu);
        }
      };
      f32x4 ah;
      bf16x8 hf[2];
      s16x4 hl[2], hh[2];
      f32x4 ac[7][2];
      bf16x8 xf[2][2], gf[2];
      s16x4 wl[2][6], wh[2][6];
      auto rd = [&](auto I_, auto HB_) {
        constexpr int I = decltype(I_)::value, HB = decltype(HB_)::value, hs = HB & 1;
        constexpr unsigned bo = ((HB >> 1) % 3) * STG;
        if constexpr (I == 2 || I == 3) xf[hs][I - 2] = lds128(L, xa[hs] + (I - 2) * REG + bo);
        else if constexpr (I < 14) {
          constexpr int c = I < 2 ? 0 : (I - 2) >> 1;
          if constexpr ((I & 1) == 0) wl[hs][c] = ldstr(L, wa[hs][c & 1] + ppos(2 + (c >> 1)) * 1024u + bo);
          else wh[hs][c] = ldstr(L, wa[hs][c & 1] + ppos(2 + (c >> 1)) * 1024u + bo + 512u);
        } else if constexpr (I == 14) gf[hs] = lds128(L, ga + HB * 64u);
        else if constexpr (I == 15) hf[hs] = lds128(L, xh[hs] + bo);
        else if constexpr (I == 16) hl[hs] = ldstr(L, wq[hs] + bo);
        else if constexpr (I == 17) hh[hs] = ldstr(L, wq[hs] + bo + 512u);
      };
      bf16x8 qf[2], kf[2];
      unsigned vh[2][2][2];
      auto pack_kv = [&](int t) {
        kf[t] = asfrag(pk(ac[2][t][0], ac[2][t][1]), pk(ac[2][t][2], ac[2][t][3]), pk(ac[3][t][0], ac[3][t][1]), pk(ac[3][t][2], ac[3][t][3]));
#pragma unroll
        for (int c = 0; c < 2; ++c) {
          vh[t][c][0] = pk(ac[4 + c][t][0], ac[4 + c][t][1]);
          vh[t][c][1] = pk(ac[4 + c][t][2], ac[4 + c][t][3]);
        }
      };
      auto pack_q = [&](int t) {
        qf[t] = asfrag(pk(ac[0][t][0], ac[0][t][1]), pk(ac[0][t][2], ac[0][t][3]), pm & pk(ac[1][t][0], ac[1][t][1]), pm & pk(ac[1][t][2], ac[1][t][3]));
        asm volatile("" : "+v"(qf[t]));
      };
      float gv[2];
      bf16x8 kp;
      v4u vp;
      rep<0, 18>([&](auto I_) { rd(I_, IC<0>{}); });
      rep<0, 14>([&](auto HB_) {
        constexpr int HB = decltype(HB_)::value, hs = HB & 1;
        if constexpr (hs == 0) wait_l();
        rep<0, 15>([&](auto I_) {
          constexpr int I = decltype(I_)::value;
          constexpr int c = HB < 13 ? I >> 1 : (I == 0 ? 7 : (I < 5 ? 1 + I : (I < 7 ? 6 : (I < 11 ? (I - 7) & 1 : I - 9))));
          constexpr int t = HB < 13 ? I & 1 : (I < 5 ? 1 : (I < 7 ? I - 5 : (I < 11 ? 1 - ((I - 7) >> 1) : 0)));
          constexpr int E = q_at(HB, I, FFN);
          d_m0(IC<E>{});
          if constexpr (c == 7) {
            if constexpr (HB == 0) mma0(ah, cat(hl[hs], hh[hs]), hf[hs]);
            else mma(ah, cat(hl[hs], hh[hs]), hf[hs]);
          } else {
            const bf16x8 wf = c < 6 ? cat(wl[hs][c < 6 ? c : 0], wh[hs][c < 6 ? c : 0]) : gf[hs];
            if constexpr (HB == 0) {
              if constexpr (c == 4 || c == 5) mma0(ac[c][t], xf[hs][t], wf);
              else mma0(ac[c][t], wf, xf[hs][t]);
            } else {
              if constexpr (c == 4 || c == 5) mma(ac[c][t], xf[hs][t], wf);
              else mma(ac[c][t], wf, xf[hs][t]);
            }
          }
          SCHED();
          if constexpr (HB < 13) {
            if constexpr (I < 7) { rd(IC<2 * I>{}, IC<HB + 1>{}); rd(IC<2 * I + 1>{}, IC<HB + 1>{}); }
            else rd(IC<I + 7>{}, IC<HB + 1>{});
            if constexpr (I == 12 && HB < 7) setup2(HB_);
            if constexpr (I == 12 && HB >= 7 && HB < 11) setupq(IC<HB - 7>{});
            if constexpr (I == 14 && HB == TOUCH2_HB && TOUCH) { if (w >= 6) do_touch(); }
          } else {
            if constexpr (I < 8) {
              b_rd(IC<(I >> 2)>{}, IC<(I & 3)>{}, IC<0>{});
              b_rd(IC<(I >> 2)>{}, IC<(I & 3)>{}, IC<1>{});
            }
            if constexpr (I == 5) {
              *(LAS v2u*)(L + hwa) = v2u{pk(ah[0], ah[1]), pk(ah[2], ah[3])};
            }
            if constexpr (I == 8) {
              pack_kv(1);
              *(LAS bf16x8*)(L + nwa) = kf[1];
              *(LAS v4u*)(L + nwa + 1024u) = v4u{vh[1][0][0], vh[1][0][1], vh[1][1][0], vh[1][1][1]};
            }
            if constexpr (I == 10) {
#pragma unroll
              for (int t2 = 0; t2 < 2; ++t2) {
                gv[t2] = rbf(__builtin_amdgcn_rcpf(1.0f + __builtin_amdgcn_exp2f(rbf(ac[6][t2][0]) * -1.4426950408889634f)));
                keep(ac[6][t2]);
                asm volatile("" : "+v"(gv[t2]));
              }
            }
            if constexpr (I == 11) {
              wait_l();
              bar();
              kp = lds128(L, wreg + 4096u + lane * 16u);
              if (tq == 0) vp = __builtin_bit_cast(v4u, cat(ldstr(L, wreg + 5120u + lane * 8u), ldstr(L, wreg + 5632u + lane * 8u)));
              else vp = *(LAS v4u*)(L + wreg + 5120u + lane * 16u);
              pack_q(1);
            }
            if constexpr (I == 13) pack_q(0);
          }
          SCHED();
          d_go(IC<E>{});
        });
        if constexpr (hs == 0) mid(IC<(HB >> 1)>{});
      });
      STAMP(2);
      auto slip = [&](auto I_) {
        if constexpr (decltype(I_)::value < NA) {
          SCHED();
          r_dma(I_);
          SCHED();
        }
      };
      const f32x4 z = {0.f, 0.f, 0.f, 0.f};
      f32x4 s0[2], s1[2];
      s1[1] = mfma(kf[1], qf[1], z);
      s0[0] = mfma(kp, qf[0], z);
      SCHED();
      asm volatile("s_nop 4");
      SCHED();
      pack_kv(0);
      s0[1] = mfma(kf[0], qf[1], z);
      s1[0] = mfma(kf[0], qf[0], z);
      slip(IC<0>{});
      const bool odd = li & 1;
      const unsigned psh = odd ? 16u : 0u;
      float sv[2][4];
      auto pick = [&](f32x4 v, int u) {
        float lo = v[2 * u], hi = v[2 * u + 1];
        asm("" : "+v"(hi));
        return odd ? hi : lo;
      };
      const float SC = 0.18898223650461362f * 1.4426950408889634f;
#pragma unroll
      for (int u = 0; u < 2; ++u) {
        sv[1][2 + u] = __builtin_fmaf(pick(s1[1], u), SC, mb[1][2 + u]);
        sv[0][u] = __builtin_fmaf(pick(s0[0], u), SC, mb[0][u]);
      }
#pragma unroll
      for (int u = 0; u < 2; ++u) {
        sv[1][u] = __builtin_fmaf(pick(s0[1], u), SC, mb[1][u]);
        sv[0][2 + u] = __builtin_fmaf(pick(s1[0], u), SC, mb[0][2 + u]);
      }
      slip(IC<1>{});
      float m[2];
      {
        const float m0 = fmaxf(fmaxf(sv[0][0], sv[0][1]), fmaxf(sv[0][2], sv[0][3])), m1 = fmaxf(fmaxf(sv[1][0], sv[1][1]), fmaxf(sv[1][2], sv[1][3]));
        auto a = __builtin_amdgcn_permlane32_swap(__float_as_uint(m0), __float_as_uint(m1), false, false);
        const float x = fmx(__uint_as_float(a[0]), __uint_as_float(a[1]));
        auto b2 = __builtin_amdgcn_permlane16_swap(__float_as_uint(x), __float_as_uint(x), false, false);
        const float y = fmx(__uint_as_float(b2[0]), __uint_as_float(b2[1]));
        auto c2 = __builtin_amdgcn_permlane32_swap(__float_as_uint(y), __float_as_uint(y), false, false);
        m[0] = __uint_as_float(c2[0]);
        m[1] = __uint_as_float(c2[1]);
      }
      slip(IC<2>{});
      const bf16x8 ones = asfrag(0x3f803f80u, 0x3f803f80u, 0x3f803f80u, 0x3f803f80u);
      f32x4 o[2][2], sm[2];
#pragma unroll
      for (int t = 0; t < 2; ++t) {
        unsigned pw[4];
#pragma unroll
        for (int j = 0; j < 4; ++j) pw[j] = pk(__builtin_amdgcn_exp2f(sv[t][j] - m[t]), 0.f) << psh;
        const bf16x8 pf = asfrag(pw[0], pw[1], pw[2], pw[3]);
        sm[t] = mfma(ones, pf, z);
#pragma unroll
        for (int c = 0; c < 2; ++c) {
          const bf16x8 vf = t == 0 ? asfrag(vp[2 * c], vp[2 * c + 1], vh[0][c][0], vh[0][c][1]) : asfrag(vh[0][c][0], vh[0][c][1], vh[1][c][0], vh[1][c][1]);
          o[t][c] = mfma(vf, pf, z);
        }
      }
      slip(IC<3>{});
      v4u ov[2];
#pragma unroll
      for (int t = 0; t < 2; ++t) {
        const float sc = gv[t] * __builtin_amdgcn_rcpf(sm[t][0]);
        const f32x4 o0 = o[t][0] * sc, o1 = o[t][1] * sc;
        ov[t] = v4u{pk(o0[0], o0[1]), pk(o0[2], o0[3]), pm & pk(o1[0], o1[1]), pm & pk(o1[2], o1[3])};
      }
      SCHED();
      st128(ov[0], rws, lane * 16u, oa);
      st128(ov[1], rws, lane * 16u, oa + A2_TT);
      if (hd == 0 && g < 2) __builtin_amdgcn_raw_buffer_store_b32(__float_as_uint(g ? gv[1] : gv[0]), rws, lane * 4u, WS_G + hp * 8192u + (128 * rb + 32 * tq) * 4u, 16);
    }
  };
  if (ffn) phase1(IC<1>{});
  else if (qkv) phase1(IC<0>{});
  else {
    SCHED();
    wait_args(ka, kb2, tptr);
    SCHED();
    set_params();
    if (TOUCH) do_touch();
    rep<0, 7>(setup2);
    rep<0, 16>(b_dma);
    wait_v<0>();
    rep<0, 16>([&](auto I_) { constexpr int I = decltype(I_)::value; b_rd(IC<(I >> 3)>{}, IC<((I >> 1) & 3)>{}, IC<(I & 1)>{}); });
    fo_pub = WS_F + 15 * 64u;
  }
  STAMP(3);
  const u64 tag = (gm_dispatch_id() + 1) * 0x9E3779B97F4A7C15ull ^ (u64)__builtin_amdgcn_queue_ptr();
  unsigned va[4];
#pragma unroll
  for (int t = 0; t < 4; ++t) va[t] = opaque(lane * 16u + t * A2_TT);
  const unsigned aob = tt0 * A2_TT + (16u + 7u * w) * 1024u, aoa = tt0 * A2_TT + 2u * w * 1024u;
  const unsigned frow = WS_F + (tt0 >> 3) * 1024u, frows = (tt0 & 7) == 7 ? 1u : 0u;
  v4u b2v;
  SCHED();
  if (!qkv) {
    wait_l();
    if (NMF) bar();
    rep<0, NMF>(r_dma);
    wait_v<NMF>();
  } else {
    rep<NA, NA + NMQ>(r_dma);
    wait_v<NMQ>();
  }
  rep<0, 16>([&](auto I_) { constexpr int I = decltype(I_)::value; b_rd(IC<2 + (I >> 3)>{}, IC<((I >> 1) & 3)>{}, IC<(I & 1)>{}); });
  SCHED();
  STAMP(4);
  bar();
  if (tid < 4) {
    const v2q tv = {tag, tag};
    st128(__builtin_bit_cast(v4u, tv), rws, lane * 16u, fo_pub);
  }
  ld128<0>(b2v, (tid & 7) * 16u, mk(p.b2, D * 2), ct * 128u);
  wait_l();
  if (!qkv) {
    rep<NMF, 8>(r_dma);
    rep<24, 36>(b_dma);
  } else {
    rep<NA + NMQ, 8>(r_dma);
  }
  STAMP(5);

  auto poll = [&](bool todo) {
#pragma unroll 1
    for (unsigned spin = 0; spin < SPIN_CAP; ++spin) {
      if (todo) {
        const v2u v = __builtin_amdgcn_raw_buffer_load_b64(rws, lane * 64u, frow, POLL_AUX);
        todo = __builtin_bit_cast(u64, v) != tag;
      }
      asm volatile("" ::: "memory");
      if (!__any(todo)) break;
      __builtin_amdgcn_s_sleep(POLL_SLEEP);
    }
  };
  poll(li >= ((7 * w) >> 3) && li <= ((7 * w + 6) >> 3) && g <= frows);
  asm volatile("" : "+v"(b2v));
  STAMP(6);
  auto phase2 = [&](auto NT_) {
    constexpr int NT = decltype(NT_)::value;
    constexpr bool INL = NT == 3;
    v4u af[WIN][NT];
    float gl, og[4];
    v2u fl = {(unsigned)tag, (unsigned)(tag >> 32)};
    const bool fme = li == 7 + w && g <= frows;
    auto a_load = [&](auto J_, auto T_) {
      constexpr int j = decltype(J_)::value, t = decltype(T_)::value;
      if constexpr (j < 4) ld128<j * 1024u>(af[j % WIN][t], va[t], rws, aob);
      else if constexpr (j < 7) ld128<(j - 4) * 1024u>(af[j % WIN][t], va[t], rws, aob + 4096u);
      else ld128<(j - 7) * 1024u>(af[j % WIN][t], va[t], rws, aoa);
    };
    if (PRIO) __builtin_amdgcn_s_setprio(3);
    rep<0, NT * WIN>([&](auto I_) { constexpr int I = decltype(I_)::value; a_load(IC<I / NT>{}, IC<I % NT>{}); });
    f32x4 acc[4][NT];
    unsigned pa[2];
    auto put = [&](int t) {
#pragma unroll
      for (int c = 0; c < 4; ++c) *(LAS f32x4*)(L + pa[c & 1] + (t * 4 + c) * 1024u) = acc[c][t];
    };
    rep<0, 9>([&](auto J_) {
      constexpr int j = decltype(J_)::value;
      if (PRIO && j + WIN == 4) __builtin_amdgcn_s_setprio(2);
      if (PRIO && j + WIN == 6) __builtin_amdgcn_s_setprio(1);
      if (PRIO && j + WIN == 8) __builtin_amdgcn_s_setprio(0);
      if constexpr (j + WIN == 7) {
        wait_for<p2_before(j, 0, NT, INL) - p2_before(5 - WIN, 1, NT, INL)>(fl);
        const bool todo = __builtin_bit_cast(u64, fl) != tag;
        if (__any(todo)) poll(todo);
        SCHED();
      }
      if constexpr (j == 6) {
#pragma unroll
        for (int c = 0; c < 2; ++c) pa[c] = opaque(wreg + (g * 16u + ((li + 4u * ((2u * c + (g >> 1)) & 3u)) & 15u)) * 16u);
      }
      if constexpr (j == 7) {
        wait_for<p2_before(7, 0, NT, INL) - p2_before(7 - WIN, 1, NT, INL)>(gl);
        gl = rbf(1.0f - gl);
        auto h2 = __builtin_amdgcn_permlane32_swap(__float_as_uint(gl), __float_as_uint(gl), false, false);
        auto lo2 = __builtin_amdgcn_permlane16_swap(h2[0], h2[0], false, false);
        auto hi2 = __builtin_amdgcn_permlane16_swap(h2[1], h2[1], false, false);
        og[0] = __uint_as_float(lo2[0]); og[1] = __uint_as_float(lo2[1]); og[2] = __uint_as_float(hi2[0]); og[3] = __uint_as_float(hi2[1]);
        SCHED();
      }
      if constexpr (j >= 4) wait_l();
      if constexpr (j == 8) { if (w == 0) *(LAS float*)(L + SPARE + lane * 4u) = gl; }
      rep<0, NT>([&](auto T_) {
        constexpr int t = decltype(T_)::value, U = p2_unit(j, t, NT, INL);
        if constexpr (U >= 0) b_m0(IC<U>{});
        if constexpr (j == 7) {
#pragma unroll
          for (int c = 0; c < 4; ++c) acc[c][t] *= og[t];
          SCHED();
          asm volatile("s_nop 0");
        }
        wait_v<p2_before(j, t, NT, INL) - p2_pos(j, t, NT, INL) - 1>();
        const bf16x8 a = __builtin_bit_cast(bf16x8, af[j % WIN][t]);
#pragma unroll
        for (int c = 0; c < 4; ++c) {
          if constexpr (j == 0) mma0(acc[c][t], cat(bl[0][c], bh[0][c]), a);
          else mma(acc[c][t], cat(bl[j & 3][c], bh[j & 3][c]), a);
        }
        SCHED();
        if constexpr (U >= 0) b_go(IC<U>{});
        if constexpr (j + WIN < 9) a_load(IC<j + WIN>{}, T_);
        if constexpr (j + WIN == 7 && t == 0) ld32<0>(gl, lane * 4u, rws, WS_G + w * 8192u + tt0 * 64u);
        if constexpr (j + WIN == 5 && t == 0) { if (fme) ld64(fl, lane * 64u, rws, frow); }
        if constexpr (j >= 2 && j + 2 < 9) {
          b_rd(IC<j + 2>{}, T_, IC<0>{});
          b_rd(IC<j + 2>{}, T_, IC<1>{});
          if constexpr (NT == 3 && t == 2) {
            b_rd(IC<j + 2>{}, IC<3>{}, IC<0>{});
            b_rd(IC<j + 2>{}, IC<3>{}, IC<1>{});
          }
        }
        if constexpr (j == 8 && t >= 1) {
          asm volatile("s_nop 2");
          SCHED();
          put(t - 1);
        }
        SCHED();
      });
      if constexpr (j == 0) STAMP(16);
      if constexpr (j == 2) STAMP(17);
      if constexpr (j == 4) STAMP(18);
      if constexpr (j == 6) STAMP(19);
    });
    STAMP(7);
    asm volatile("s_nop 3" ::: "memory");
    SCHED();
    put(NT - 1);
  };
  if (qkv) phase2(IC<3>{});
  else phase2(IC<4>{});
  wait_l();
  bar();
  STAMP(8);
  if (!qkv || w < 6) {
    const unsigned tk = tid >> 3, q = tid & 7;
    const unsigned n = (tk >> 4) * 4u + (q >> 1), gg = 2 * (q & 1), i = tk & 15;
    const unsigned ad = n * 1024u + (gg * 16u + ((i + 4u * (q & 3u)) & 15u)) * 16u;
    const float og = *(LAS float*)(L + SPARE + tk * 4u);
    f32x4 alo = f32x4{bflo(b2v[0]), bfhi(b2v[0]), bflo(b2v[1]), bfhi(b2v[1])} * og, ahi = f32x4{bflo(b2v[2]), bfhi(b2v[2]), bflo(b2v[3]), bfhi(b2v[3])} * og;
#pragma unroll
    for (int k = 0; k < 8; ++k) {
      alo += *(LAS f32x4*)(L + k * REG + ad);
      ahi += *(LAS f32x4*)(L + k * REG + ad + 256u);
    }
    const v4u o = {pk(alo[0], alo[1]), pk(alo[2], alo[3]), pk(ahi[0], ahi[1]), pk(ahi[2], ahi[3])};
    __builtin_amdgcn_raw_buffer_store_b128(o, mk(p.out, S * D * 2), (16 * tt0 + tk) * 896u + ct * 128u + q * 16u, 0, 16);
  }
  STAMP(9);
  STAMP_FLUSH();
}
}

extern "C" void kernel_launch(void* const* d_in, const int* in_sizes, int n_in, void* d_out, int out_size, void* d_ws, size_t ws_size,
                              hipStream_t stream) {
  gm::Params p;
  p.x = d_in[0];
  p.wqkv = d_in[1];
  p.wo = d_in[2];
  p.w1 = d_in[4];
  p.b1 = d_in[5];
  p.w2 = d_in[6];
  p.b2 = d_in[7];
  p.gw = d_in[8];
  p.out = d_out;
  p.ws = d_ws;
  gm::gm_kernel<<<256, 512, 0, stream>>>(p);
}
```

```cpp
#include <hip/hip_runtime.h>
#include <stdint.h>

typedef short bf16x8 __attribute__((ext_vector_type(8)));
typedef short s16x4 __attribute__((ext_vector_type(4)));
typedef float f32x4 __attribute__((ext_vector_type(4)));
typedef unsigned v4u __attribute__((ext_vector_type(4)));
typedef unsigned v2u __attribute__((ext_vector_type(2)));
typedef __bf16 bf2_t __attribute__((ext_vector_type(2)));
typedef float f32x2 __attribute__((ext_vector_type(2)));
#define LAS __attribute__((address_space(3)))
#define GAS __attribute__((address_space(1)))
#define DI __device__ __forceinline__
typedef unsigned long long u64;
typedef __amdgpu_buffer_rsrc_t rsrc_t;
typedef u64 v8q __attribute__((ext_vector_type(8)));
typedef u64 v2q __attribute__((ext_vector_type(2)));

#define CODE_PF 23552
#define TOUCH 1
#define TOUCH2_HB 3
#define WINDOW 2
#define PRIO 1
#define POLL_SLEEP 1
#define FLAG_STRIDE 256
#define POLL_AUX 16
#define FLAG_ST 2
#define TMODE 1
#define TCNT 0x0C31151C38101520ull
#define TFIRST 0x00003F5400000000ull
#define TSH 2u
#define POLL_WAVE 0
#define QPRE 6
#define QSLOW 0
#define TLATE 0
#define PEEK 0
#define PKBAR 0
#define QPEEK 0
#define L2W 0
#define L2W_SLEEP 3
#define ROT 0u
#define L2W_HB 6
#define FSLEEP 0
#define FREE_SLEEP 0
#define NA 0
#define NMF 5
#define NMQ 4
#define FBAR 1
#define DEFER 0
#define PBAR 1

extern "C" __device__ u64 gm_dispatch_id() __asm("llvm.amdgcn.dispatch.id");

namespace gm {
constexpr int S = 2048, D = 448, F = 1792, NQKV = 1344;
constexpr int KS_A = 16, KS = 72;
constexpr unsigned A2_TT = KS * 1024;
constexpr unsigned WS_G = 128 * A2_TT, WS_F = WS_G + 8 * 8192, WS_END = WS_F + 16 * 1024;
constexpr unsigned REG = 20480, STG = 6144, SPARE = 3 * STG;
constexpr int LDS_BYTES = 163840;
constexpr unsigned GW_OFF = 2 * REG + 5120;
constexpr unsigned SPIN_CAP = 1u << 16;

struct Params {
  const void *x, *wqkv, *wo, *w1, *b1, *w2, *b2, *gw;
  void *out, *ws;
};
template <int N> struct IC { static constexpr int value = N; };

DI rsrc_t mk(const void* p, unsigned n) { return __builtin_amdgcn_make_buffer_rsrc((void*)p, 0, n, 0x00020000); }
template <unsigned C> DI void set_m0(unsigned base) { asm volatile("s_add_u32 m0, %0, %1" ::"s"(base), "i"(C) : "scc", "memory"); }
template <unsigned IMM> DI void dma_go(unsigned voff, rsrc_t rs, unsigned soff) {
  asm volatile("buffer_load_dwordx4 %0, %1, %2 offen offset:%3 lds" ::"v"(voff), "s"(rs), "s"(soff), "i"(IMM) : "memory");
}
template <unsigned IMM> DI void dma_go0(unsigned voff, rsrc_t rs) {
  asm volatile("buffer_load_dwordx4 %0, %1, 0 offen offset:%2 lds" ::"v"(voff), "s"(rs), "i"(IMM) : "memory");
}
template <unsigned IMM> DI void ld128(v4u& d, unsigned voff, rsrc_t rs, unsigned soff) {
  asm volatile("buffer_load_dwordx4 %0, %1, %2, %3 offen offset:%4 sc1" : "=v"(d) : "v"(voff), "s"(rs), "s"(soff), "i"(IMM) : "memory");
}
template <unsigned IMM> DI void ld32(float& d, unsigned voff, rsrc_t rs, unsigned soff) {
  asm volatile("buffer_load_dword %0, %1, %2, %3 offen offset:%4 sc1" : "=v"(d) : "v"(voff), "s"(rs), "s"(soff), "i"(IMM) : "memory");
}
DI void ld64(v2u& d, unsigned voff, rsrc_t rs, unsigned soff) {
  asm volatile("buffer_load_dwordx2 %0, %1, %2, %3 offen sc1" : "+v"(d) : "v"(voff), "s"(rs), "s"(soff) : "memory");
}
template <int N, class T> DI void wait_for(T& v) { asm volatile("s_waitcnt vmcnt(%1)" : "+v"(v) : "i"(N) : "memory"); }
DI void touch(unsigned lds, unsigned voff, u64 base) {
  asm volatile("s_mov_b32 m0, %0\n\ts_nop 0\n\tglobal_load_lds_dword %1, %2" ::"s"(lds), "v"(voff), "s"(base) : "memory");
}
#define NOP1() asm volatile("s_nop 0")
DI f32x4 mfma(bf16x8 a, bf16x8 b, f32x4 c) { return __builtin_amdgcn_mfma_f32_16x16x32_bf16(a, b, c, 0, 0, 0); }
DI void mma(f32x4& c, bf16x8 a, bf16x8 b) { asm volatile("v_mfma_f32_16x16x32_bf16 %0, %1, %2, %0" : "+v"(c) : "v"(a), "v"(b)); }
DI void mma0(f32x4& c, bf16x8 a, bf16x8 b) { asm volatile("v_mfma_f32_16x16x32_bf16 %0, %1, %2, 0" : "=&v"(c) : "v"(a), "v"(b)); }
DI void mmac(f32x4& d, bf16x8 a, bf16x8 b, f32x4 c) { asm volatile("v_mfma_f32_16x16x32_bf16 %0, %1, %2, %3" : "=&v"(d) : "v"(a), "v"(b), "v"(c)); }
DI float relu(float v) {
  float r;
  asm("v_max_f32 %0, 0, %1" : "=v"(r) : "v"(v));
  return r;
}
DI float fmx(float a, float b) {
  float r;
  asm("v_max_f32 %0, %1, %2" : "=v"(r) : "v"(a), "v"(b));
  return r;
}
DI unsigned pk(float lo, float hi) {
  f32x2 v = {lo, hi};
  return __builtin_bit_cast(unsigned, __builtin_convertvector(v, bf2_t));
}
DI float rbf(float f) { return __uint_as_float(pk(f, 0.f) << 16); }
DI float bflo(unsigned u) { return __uint_as_float(u << 16); }
DI float bfhi(unsigned u) { return __uint_as_float(u & 0xffff0000u); }
DI bf16x8 asfrag(unsigned a, unsigned b, unsigned c, unsigned d) {
  v4u v = {a, b, c, d};
  return __builtin_bit_cast(bf16x8, v);
}
DI void st128(v4u v, rsrc_t rs, unsigned voff, unsigned soff) {
  __builtin_amdgcn_raw_buffer_store_b128(v, rs, voff, soff, 16);
  __builtin_amdgcn_sched_barrier(0);
  asm volatile("s_nop 1");
  __builtin_amdgcn_sched_barrier(0);
}
DI bf16x8 cat(s16x4 lo, s16x4 hi) { return __builtin_shufflevector(lo, hi, 0, 1, 2, 3, 4, 5, 6, 7); }
DI bf16x8 lds128(LAS char* L, unsigned a) { return *(LAS bf16x8*)(L + a); }
DI s16x4 ldstr(LAS char* L, unsigned a) { return __builtin_amdgcn_ds_read_tr16_b64_v4i16((LAS s16x4*)(L + a)); }
DI unsigned opaque(unsigned v) {
  asm volatile("" : "+v"(v));
  return v;
}
DI unsigned sopaque(unsigned v) {
  asm volatile("" : "+s"(v));
  return v;
}
DI float xmax(float v) {
  auto a = __builtin_amdgcn_permlane32_swap(__float_as_uint(v), __float_as_uint(v), false, false);
  v = fmx(__uint_as_float(a[0]), __uint_as_float(a[1]));
  auto b = __builtin_amdgcn_permlane16_swap(__float_as_uint(v), __float_as_uint(v), false, false);
  return fmx(__uint_as_float(b[0]), __uint_as_float(b[1]));
}
DI float xsum(float v) {
  auto a = __builtin_amdgcn_permlane32_swap(__float_as_uint(v), __float_as_uint(v), false, false);
  v = __uint_as_float(a[0]) + __uint_as_float(a[1]);
  auto b = __builtin_amdgcn_permlane16_swap(__float_as_uint(v), __float_as_uint(v), false, false);
  return __uint_as_float(b[0]) + __uint_as_float(b[1]);
}
#define SCHED() __builtin_amdgcn_sched_barrier(0)
#define STAMP(k)
#define STAMP_FLUSH()
#define FSTAMP(k)
template <int N> DI void wait_v() { asm volatile("s_waitcnt vmcnt(%0)" ::"i"(N) : "memory"); }
DI void wait_l() {
  SCHED();
  __builtin_amdgcn_s_waitcnt(0xC07F);
  asm volatile("" ::: "memory");
  SCHED();
}
DI void wait_args(u64& a, u64& b, u64& c, u64& d) {
  asm volatile("s_waitcnt lgkmcnt(0)" ::: "memory");
  SCHED();
  asm volatile("" : "+s"(a), "+s"(b), "+s"(c), "+s"(d)::"memory");
}
DI void keep(f32x4 v) { asm volatile("" ::"v"(v)); }
DI void bar() { asm volatile("s_barrier" ::: "memory"); }

constexpr unsigned hloc(int h) {
  h = h >= 10 ? h - 10 : h;
  return h == 0 ? SPARE : (h < 4 ? STG + (h - 1) * 2048u : (h < 7 ? 2 * STG + (h - 4) * 2048u : (h - 7) * 2048u));
}
constexpr unsigned ppos(int i) { return i < 2 ? i + 1 : (i == 2 ? 0 : i); }

constexpr int q_piece(int k, bool ffn) { return ffn ? k : (k + 5) % 6; }
constexpr int q_item(int i, bool ffn) { return i < 36 ? 8 * (1 + i / 6) + q_piece(i % 6, ffn) : 100 + (i - 36); }
constexpr int q_from(int item) {
  if (item < 100) return (item >> 3) <= 2 ? 0 : 2 * ((item >> 3) - 3) + 1;
  const int h = (item - 100) >> 1;
  return h == 0 ? 1 : (h < 4 ? 9 : (h < 7 ? 11 : 13));
}
constexpr int q_slots(int hb) { return hb < QSLOW ? 3 : 4; }
constexpr int q_slot(int hb, int i) { return hb < QSLOW ? (i % 5 == 1 ? i / 5 : -1) : ((i & 3) == 1 ? i >> 2 : -1); }
constexpr int q_start(int hb) {
  int i = QPRE;
  for (int h = 0; h < hb; ++h)
    for (int k = 0; k < q_slots(h); ++k)
      if (i < 52 && q_from(q_item(i, true)) <= h) ++i;
  return i;
}
constexpr int q_at(int hb, int i, bool ffn) {
  const int k = q_slot(hb, i);
  return k >= 0 && q_start(hb) + k < q_start(hb + 1) ? q_item(q_start(hb) + k, ffn) : -1;
}
constexpr int q_count(int from, int to, bool ffn) {
  int n = 0;
  for (int i = from; i < to; ++i) n += (ffn || i >= 36 || i % 6 != 0) ? 1 : 0;
  return n;
}
constexpr int q_wait(int s, bool ffn) { return q_count(s < 6 ? 6 * (s + 1) : 44, q_start(2 * s + 1), ffn); }
static_assert(q_start(14) == 52 && q_start(1) >= 6 && q_start(3) >= 12 && q_start(5) >= 18 && q_start(7) >= 24 && q_start(9) >= 30 && q_start(11) >= 36 && q_start(13) >= 44, "order");
constexpr int r_unit(int i) { return i < 2 ? 16 + i : (i < 6 ? 18 + i : 12 + i); }
constexpr int WIN = WINDOW;
static_assert(WIN == 2, "the landing of the in-loop weight units is derived for a window of 2");
constexpr int p2_unit(int j, int t, int nt, bool inl) { return inl && j * nt + t < 12 ? 24 + j * nt + t : -1; }
constexpr int p2_after(int j, int t, int nt, bool inl) {
  return (p2_unit(j, t, nt, inl) >= 0 ? 1 : 0) + (j < 5 ? 1 : (j == 5 ? 2 : 0))
         + (j == 5 && t == 0 ? 1 : 0)
         + (j == 3 && t == 0 ? 1 : 0);
}
constexpr int p2_before(int j, int t, int nt, bool inl) {
  int n = nt * WIN;
  for (int i = 0; i < nt * j + t; ++i) n += p2_after(i / nt, i % nt, nt, inl);
  return n;
}
constexpr int p2_pos(int j, int t, int nt, bool inl) {
  return j < WIN ? nt * j + t : (j == 8 ? p2_before(5, t, nt, inl) + 1 : p2_before(j - WIN, t, nt, inl) + (p2_unit(j - WIN, t, nt, inl) >= 0 ? 1 : 0));
}
template <int I, int N, class Fn> DI void rep(Fn&& f) {
  if constexpr (I < N) {
    f(IC<I>{});
    rep<I + 1, N>(f);
  }
}

__global__ __launch_bounds__(512, 2) void gm_kernel(Params p) {
  __shared__ __attribute__((aligned(1024))) char smem[LDS_BYTES];
  LAS char* L = (LAS char*)smem;
  const unsigned tid = threadIdx.x, lane = tid & 63;
  const unsigned b = blockIdx.x, xcd = b & 7, slot = b >> 3;
  const bool ffn = b < 112, qkv = b >= 128;
  u64 px, pw, pv, tptr;
  asm volatile("s_load_dwordx2 %0, %3, 0x0\n\ts_load_dwordx2 %1, %3, %4\n\ts_load_dwordx2 %2, %3, %5"
               : "=&s"(px), "=&s"(pw), "=&s"(pv)
               : "s"(__builtin_amdgcn_kernarg_segment_ptr()), "s"(ffn ? 24u : 8u), "s"(ffn ? 32u : 56u));
  if (tid < CODE_PF / 1024) {
    u64 pc;
    asm volatile("s_getpc_b64 %0" : "=s"(pc));
    touch(SPARE + 1024u, ((slot & 7) * (CODE_PF / 1024) + tid) * 128u, pc);
  }
  const unsigned w = __builtin_amdgcn_readfirstlane(tid >> 6);
  asm volatile("s_load_dwordx2 %0, %1, %2" : "=&s"(tptr) : "s"(__builtin_amdgcn_kernarg_segment_ptr()), "s"(((0x25130130u >> (4 * w)) & 7u) * 8u));
  const unsigned wreg = w * REG;
  const unsigned li = lane & 15, g = lane >> 4, rr = lane >> 3, pos = lane & 7;
  const unsigned rq = xcd >> 1, side = xcd & 1;
  unsigned tcnt = 0, tln = 0;
  auto prep_touch = [&]() {
    tcnt = (unsigned)(TCNT >> (8 * w)) & 0xffu;
    const unsigned tfirst = ((unsigned)(TFIRST >> (8 * w)) & 0xffu) * 64u;
    const unsigned tsh = w ? 6u : TSH;
    tln = opaque(((32 * xcd + slot) * (w ? tcnt : 56u) + tfirst + (lane >> tsh) * 7u + (lane & ((1u << tsh) - 1u))) * 128u);
  };
  auto do_touch = [&]() {
    if (lane < tcnt) touch(wreg + SPARE + 1024u, tln, tptr);
  };

  rsrc_t rws, rw2, rwo;
  unsigned vb[2], ba[4], fo_pub;
  const unsigned bo2 = w * (224u * 896u), boo = w * (56u * 896u);
  unsigned ct = 0, tt0 = 0;
  const unsigned q4 = (lane >> 2) & 3, p4 = lane & 3;
  const unsigned fsw = (q4 >> 1) | ((g & 1) << 1);
  s16x4 bl[4][4], bh[4][4];
  auto b_m0 = [&](auto N_) {
    constexpr int N = decltype(N_)::value;
    set_m0<hloc(N >> 1) + (N & 1) * 1024u>(wreg);
  };
  auto jr = [&](int j) -> unsigned {
    const unsigned a = (unsigned)j + (ROT);
    return a >= 7 ? a - 7 : a;
  };
  auto b_go = [&](auto N_) {
    constexpr int N = decltype(N_)::value, j = N >> 2;
    if constexpr (j < 7) dma_go<0>(vb[N & 1], rw2, bo2 + (2 * jr(j) + ((N >> 1) & 1)) * (16u * 896u));
    else dma_go<0>(vb[N & 1], rwo, boo + ((j - 7) * 28u + ((N >> 1) & 1) * 16u) * 896u);
  };
  auto b_dma = [&](auto N_) { b_m0(N_); NOP1(); b_go(N_); };
  auto r_dma = [&](auto I_) { b_dma(IC<r_unit(decltype(I_)::value)>{}); };
  auto b_rd = [&](auto J_, auto C_, auto E_) {
    constexpr int J = decltype(J_)::value, C = decltype(C_)::value, E = decltype(E_)::value;
    if constexpr (E == 0) bl[J & 3][C] = ldstr(L, ba[C] + hloc(2 * J));
    else bh[J & 3][C] = ldstr(L, ba[C] + hloc(2 * J + 1));
  };
  auto setup2 = [&](auto K_, auto Q_) {
    constexpr int K = decltype(K_)::value;
    constexpr bool Q = decltype(Q_)::value;
    if constexpr (K == 0) {
      ct = Q ? (slot >> 2) - 1 : (unsigned)max((int)(slot >> 2), (int)slot - 9);
      tt0 = 16 * xcd + (Q ? 4 + 3 * (slot & 3) : (slot < 12 ? 4 * (slot & 3) : 0u));
      rws = mk(p.ws, WS_END);
      rw2 = mk(p.w2, F * D * 2);
      rwo = mk(p.wo, D * D * 2);
    } else if constexpr (K == 1) {
    } else if constexpr (K == 2) {
      vb[0] = opaque(rr * 896u + ((pos ^ (((rr >> 1) & 3) << 1)) << 4) + ct * 128u);
      vb[1] = opaque(vb[0] + 8u * 896u);
    } else if constexpr (K < 7) {
      ba[K - 3] = opaque(wreg + (4 * g + q4) * 128u + ((2u * ((K - 3) ^ fsw) + (p4 >> 1)) << 4) + (p4 & 1) * 8u);
    }
  };

  auto phase1 = [&](auto FFN_) {
    constexpr bool FFN = decltype(FFN_)::value;
    constexpr int NP = FFN ? 6 : 5;
    constexpr unsigned LDW = FFN ? 3584u : 2688u, WUS = FFN ? 128u : 896u;
    const bool mid3 = FFN && slot >= 12;
    const unsigned rb = 4 * rq + (mid3 ? 2 * side + (slot & 1) : slot & 3), cs = mid3 ? 3u : side * 4 + (slot >> 2), hp = side * 4 + ((slot >> 2) & 3);
    unsigned vx[3];
#pragma unroll
    for (int h = 0; h < 2; ++h) vx[h] = opaque((128 * rb + 16 * w + 8 * h + rr) * 896u + ((pos ^ (4u * h + (rr >> 1))) << 4));
    if (!FFN) vx[2] = opaque(((rb ? 128 * rb - 16 : 0u) + 8 * (w & 1) + rr) * 896u + ((pos ^ (4u * (w & 1) + (rr >> 1))) << 4));
    unsigned wch = pos ^ ((((rr >> 1) & 1) | ((w & 1) << 1)) << 1);
    if (!FFN) wch = min(wch, 6u);
    const unsigned vw = opaque((8 * w + rr) * LDW + (wch << 4) + (FFN ? cs * 512u : hp * 112u));
    if (!TLATE) prep_touch();
    SCHED();
    wait_args(px, pw, pv, tptr);
    SCHED();
    STAMP(10);
    const rsrc_t rx = mk((const void*)px, S * D * 2);
    const rsrc_t rw = mk((const void*)pw, FFN ? D * F * 2 : D * NQKV * 2);
    auto d_m0 = [&](auto E_) {
      constexpr int E = decltype(E_)::value, I = E & 7, ST = E >> 3;
      if constexpr (E >= 100) b_m0(IC<E - 100>{});
      else if constexpr (E >= 0) set_m0<(ST % 3) * STG + ppos(I) * 1024u - ((I < 2 || (!FFN && I == 5)) ? ST * 128u : (I - 2) * WUS)>(wreg);
    };
    auto d_go = [&](auto E_) {
      constexpr int E = decltype(E_)::value, I = E & 7, ST = E >> 3;
      if constexpr (E >= 100) b_go(IC<E - 100>{});
      else if constexpr (E >= 0) {
        if constexpr (I < 2) dma_go0<ST * 128u>(vx[I], rx);
        else if constexpr (!FFN && I == 5) { if (w < 2) dma_go0<ST * 128u>(vx[2], rx); }
        else if constexpr (ST == 0) dma_go0<(I - 2) * WUS>(vw, rw);
        else dma_go<(I - 2) * WUS>(vw, rw, ST * 64u * LDW);
      }
    };
    if (TOUCH && !TLATE && w < 3) do_touch();
    rep<0, 6>([&](auto I_) { d_m0(I_); NOP1(); d_go(I_); });
    if (w == 2) {
      set_m0<FFN ? SPARE : GW_OFF>(0);
      NOP1();
      dma_go0<0>(lane * 16, mk((const char*)pv + (FFN ? cs * 512u : 0u), FFN ? 512 : 896));
    }
    SCHED();
    if (TLATE) {
      prep_touch();
      if (TOUCH && w < 3) do_touch();
    }
    STAMP(11);
    const unsigned xa0 = 1024u + (li >> 3) * 1024u + (li & 7) * 128u + ((g ^ ((li >> 1) & 7)) << 4);
    auto second_stage = [&]() {
      SCHED();
      STAMP(12);
      if (PBAR != 0 && QPRE != 0) bar();
      rep<0, QPRE>([&](auto I_) { constexpr int E = q_item(decltype(I_)::value, FFN); d_m0(IC<E>{}); NOP1(); d_go(IC<E>{}); });
      STAMP(14);
      if (TOUCH && w < 6 && (TLATE || w >= 3)) {
        if (w >= 3) do_touch();
        wait_v<q_count(0, QPRE, FFN) + 1>();
      } else {
        wait_v<q_count(0, QPRE, FFN)>();
      }
      bar();
      STAMP(1);
    };
    auto mid = [&](auto S_) {
      constexpr int s = decltype(S_)::value;
      wait_v<q_wait(s, FFN)>();
      wait_l();
      bar();
      FSTAMP(20 + s);
    };

    if constexpr (FFN) {
      const unsigned mh = w >> 2, nq = w & 3;
      unsigned xa[2][2], wa[2][4];
#pragma unroll
      for (int hs = 0; hs < 2; ++hs) {
#pragma unroll
        for (int t2 = 0; t2 < 2; ++t2) xa[hs][t2] = opaque((xa0 ^ (hs * 64u)) + (mh * 4u + t2 * 2u) * REG);
#pragma unroll
        for (int c = 0; c < 4; ++c)
          wa[hs][c] = opaque((4u * hs + g) * REG + (nq ? 2u + nq : 0u) * 1024u + q4 * 128u + ((2u * (c ^ fsw) + (p4 >> 1)) << 4) + (p4 & 1) * 8u);
      }
      second_stage();
      bf16x8 xf[2][4];
      s16x4 wl[2][4], wh[2][4];
      f32x4 acc[4][4];
      auto rd = [&](auto I_, auto HB_) {
        constexpr int I = decltype(I_)::value, HB = decltype(HB_)::value, hs = HB & 1;
        constexpr unsigned bo = ((HB >> 1) % 3) * STG;
        if constexpr (I >= 2 && I < 6) xf[hs][I - 2] = lds128(L, xa[hs][(I - 2) >> 1] + ((I - 2) & 1) * REG + bo);
        else if constexpr (I < 12) {
          constexpr int c = I < 2 ? 0 : (I - 4) >> 1;
          if constexpr ((I & 1) == 0) wl[hs][c] = ldstr(L, wa[hs][c] + bo);
          else wh[hs][c] = ldstr(L, wa[hs][c] + bo + 512u);
        }
      };
      f32x4 bv[4];
#pragma unroll
      for (int c = 0; c < 4; ++c) {
        const v2u bb = *(LAS v2u*)(L + SPARE + nq * 128u + c * 32u + g * 8u);
        bv[c] = f32x4{bflo(bb.x), bfhi(bb.x), bflo(bb.y), bfhi(bb.y)};
      }
      rep<0, 12>([&](auto I_) { rd(I_, IC<0>{}); });
#pragma unroll
      for (int c = 0; c < 4; ++c) asm volatile("" : "+v"(bv[c]));
      unsigned so = 0;
      auto epi = [&](int t, int u) {
        float hv[8];
#pragma unroll
        for (int j = 0; j < 8; ++j) {
          const float r = relu(acc[2 * u + (j >> 2)][t][j & 3]);
          hv[j] = r * r;
        }
        const v4u o = {pk(hv[0], hv[1]), pk(hv[2], hv[3]), pk(hv[4], hv[5]), pk(hv[6], hv[7])};
        st128(o, rws, lane * 16u + u * 1024u, so + t * A2_TT);
      };
      rep<0, 12>([&](auto HB_) {
        constexpr int HB = decltype(HB_)::value, hs = HB & 1;
        if constexpr (hs == 0) {
          wait_l();
          if constexpr (HB == 0) asm volatile("s_nop 1");
        }
        rep<0, 16>([&](auto I_) {
          constexpr int I = decltype(I_)::value;
          constexpr int E = q_at(HB, I, FFN);
          d_m0(IC<E>{});
          if constexpr (HB == 0) mmac(acc[I >> 2][I & 3], cat(wl[hs][I >> 2], wh[hs][I >> 2]), xf[hs][I & 3], bv[I >> 2]);
          else mma(acc[I >> 2][I & 3], cat(wl[hs][I >> 2], wh[hs][I >> 2]), xf[hs][I & 3]);
          SCHED();
          rd(I_, IC<HB + 1>{});
          if constexpr (I == 14 && HB < 7) setup2(HB_, IC<0>{});
          if constexpr (I == 14 && HB == 7) {
            so = sopaque((8 * rb + 4 * mh) * A2_TT + (KS_A + 8 * cs + 2 * nq) * 1024u);
            fo_pub = sopaque(WS_F + rb * 1024u + cs * 64u);
          }
          if constexpr (I == 15 && HB == TOUCH2_HB && TOUCH) { if (w >= 6) do_touch(); }
          if constexpr (I == 15 && HB == L2W_HB && L2W) {
            if (lane < 35) touch(wreg + SPARE + 1024u, ((224u * w + 128u + min(7u * slot + lane / 5u, 95u)) * 7u + lane % 5u) * 128u, (u64)p.w2);
          }
          SCHED();
          d_go(IC<E>{});
        });
        if constexpr (hs == 0) mid(IC<(HB >> 1)>{});
      });

      wait_l();
      rep<0, 32>([&](auto M_) {
        constexpr int M = decltype(M_)::value, G = M >> 2, c = M & 3;
        constexpr int hs = G < 4 ? G >> 1 : G & 1, t = G < 4 ? G & 1 : G >> 1;
        constexpr int E = M < 8 ? ((M & 1) == 0 ? q_at(12, 2 * M + 1, FFN) : -1) : (M < 24 ? q_at(13, M - 8, FFN) : -1);
        d_m0(IC<E>{});
        mma(acc[c][t], cat(wl[hs][c], wh[hs][c]), xf[hs][t]);
        SCHED();
        if constexpr (M < 6) { rd(IC<2 * M>{}, IC<13>{}); rd(IC<2 * M + 1>{}, IC<13>{}); }
        if constexpr (M >= 8 && M < 14) {
          rep<0, (M < 12 ? 3 : 2)>([&](auto Q_) {
            constexpr int Q = (M < 12 ? 3 * (M - 8) : 12 + 2 * (M - 12)) + decltype(Q_)::value;
            b_rd(IC<(Q >> 3)>{}, IC<((Q >> 1) & 3)>{}, IC<(Q & 1)>{});
          });
        }
        if constexpr (M == 14) epi(0, 0);
        if constexpr (M == 17) epi(0, 1);
        if constexpr (M == 20) epi(1, 0);
        if constexpr (M == 23) epi(1, 1);
        if constexpr (M == 26) epi(2, 0);
        if constexpr (M == 29) epi(2, 1);
        SCHED();
        d_go(IC<E>{});
        if constexpr (M == 7) mid(IC<6>{});
      });
      STAMP(2);
      asm volatile("s_nop 5" ::: "memory");
      SCHED();
      epi(3, 0);
      epi(3, 1);
    } else {
      const unsigned tq = w & 3, hd = w >> 2;
      unsigned xa[2], wa[2][2];
#pragma unroll
      for (int hs = 0; hs < 2; ++hs) {
        xa[hs] = opaque((xa0 ^ (hs * 64u)) + tq * 2u * REG);
#pragma unroll
        for (int c = 0; c < 2; ++c) {
          const unsigned bo = 56u * hd + 32u * c + 8u * p4;
          wa[hs][c] = opaque((4u * hs + g) * REG + q4 * 128u + (((bo >> 4) ^ (fsw << 1)) << 4) + (bo & 8u));
        }
      }
      const unsigned ga = opaque(GW_OFF + g * 16u);
      unsigned xh[2], wq[2];
#pragma unroll
      for (int hs = 0; hs < 2; ++hs) {
        xh[hs] = opaque((li >> 3) * REG + 5120u + (li & 7) * 128u + (((4u * hs + g) ^ ((li >> 1) & 7)) << 4));
        const unsigned bo = 56u * hd + 32u * (tq & 1) + 8u * p4;
        wq[hs] = opaque((4u * hs + g) * REG + q4 * 128u + (((bo >> 4) ^ (fsw << 1)) << 4) + (bo & 8u) + (3u + (tq >> 1)) * 1024u);
      }
      second_stage();
      float mb[2][4];
      const unsigned head = 2 * hp + hd;
      unsigned oa = 0, hwa = 0, nwa = 0, pm = 0;
      auto setupq = [&](auto K_) {
        constexpr int K = decltype(K_)::value, t = K & 1;
        if constexpr (K < 2) {
#pragma unroll
          for (int j = 0; j < 4; ++j) {
            const int kap = 16 * (j >> 1) + (int)(4 * g) + 2 * (j & 1) + (int)(li & 1) - 16;
            const int dl = (int)li - kap;
            const bool ok = (dl >= 0) && (dl <= 14) && (t == 1 || j >= 2 || (rb | tq) != 0);
            mb[t][j] = __uint_as_float(opaque(ok ? 0u : 0xff800000u));
          }
        } else if constexpr (K == 2) {
          oa = sopaque((8 * rb + 2 * tq) * A2_TT + head * 1024u);
          fo_pub = sopaque(WS_F + rb * 1024u + (7 + hp) * 64u);
        } else if constexpr (K == 3) {
          hwa = opaque(hd * 4u * REG + (tq < 2 ? 4096u + lane * 16u + tq * 8u : 4096u + tq * 512u + li * 32u + g * 8u));
          nwa = opaque(wreg + (tq < 3 ? REG + 4096u : 2048u) + lane * 16u);
          pm = opaque(g == 3 ? 0u : 0xffffffffu);
        }
      };
      f32x4 ah;
      bf16x8 hf[2];
      s16x4 hl[2], hh[2];
      f32x4 ac[7][2];
      bf16x8 xf[2][2], gf[2];
      s16x4 wl[2][6], wh[2][6];
      auto rd = [&](auto I_, auto HB_) {
        constexpr int I = decltype(I_)::value, HB = decltype(HB_)::value, hs = HB & 1;
        constexpr unsigned bo = ((HB >> 1) % 3) * STG;
        if constexpr (I == 2 || I == 3) xf[hs][I - 2] = lds128(L, xa[hs] + (I - 2) * REG + bo);
        else if constexpr (I < 14) {
          constexpr int c = I < 2 ? 0 : (I - 2) >> 1;
          if constexpr ((I & 1) == 0) wl[hs][c] = ldstr(L, wa[hs][c & 1] + ppos(2 + (c >> 1)) * 1024u + bo);
          else wh[hs][c] = ldstr(L, wa[hs][c & 1] + ppos(2 + (c >> 1)) * 1024u + bo + 512u);
        } else if constexpr (I == 14) gf[hs] = lds128(L, ga + HB * 64u);
        else if constexpr (I == 15) hf[hs] = lds128(L, xh[hs] + bo);
        else if constexpr (I == 16) hl[hs] = ldstr(L, wq[hs] + bo);
        else if constexpr (I == 17) hh[hs] = ldstr(L, wq[hs] + bo + 512u);
      };
      bf16x8 qf[2], kf[2];
      unsigned vh[2][2][2];
      auto pack_kv = [&](int t) {
        kf[t] = asfrag(pk(ac[2][t][0], ac[2][t][1]), pk(ac[2][t][2], ac[2][t][3]), pk(ac[3][t][0], ac[3][t][1]), pk(ac[3][t][2], ac[3][t][3]));
#pragma unroll
        for (int c = 0; c < 2; ++c) {
          vh[t][c][0] = pk(ac[4 + c][t][0], ac[4 + c][t][1]);
          vh[t][c][1] = pk(ac[4 + c][t][2], ac[4 + c][t][3]);
        }
      };
      auto pack_q = [&](int t) {
        qf[t] = asfrag(pk(ac[0][t][0], ac[0][t][1]), pk(ac[0][t][2], ac[0][t][3]), pm & pk(ac[1][t][0], ac[1][t][1]), pm & pk(ac[1][t][2], ac[1][t][3]));
        asm volatile("" : "+v"(qf[t]));
      };
      float gv[2];
      bf16x8 kp;
      v4u vp;
      rep<0, 18>([&](auto I_) { rd(I_, IC<0>{}); });
      rep<0, 14>([&](auto HB_) {
        constexpr int HB = decltype(HB_)::value, hs = HB & 1;
        if constexpr (hs == 0) wait_l();
        rep<0, 15>([&](auto I_) {
          constexpr int I = decltype(I_)::value;
          constexpr int c = HB < 13 ? I >> 1 : (I == 0 ? 7 : (I < 5 ? 1 + I : (I < 7 ? 6 : (I < 11 ? (I - 7) & 1 : I - 9))));
          constexpr int t = HB < 13 ? I & 1 : (I < 5 ? 1 : (I < 7 ? I - 5 : (I < 11 ? 1 - ((I - 7) >> 1) : 0)));
          constexpr int E = q_at(HB, I, FFN);
          d_m0(IC<E>{});
          if constexpr (c == 7) {
            if constexpr (HB == 0) mma0(ah, cat(hl[hs], hh[hs]), hf[hs]);
            else mma(ah, cat(hl[hs], hh[hs]), hf[hs]);
          } else {
            const bf16x8 wf = c < 6 ? cat(wl[hs][c < 6 ? c : 0], wh[hs][c < 6 ? c : 0]) : gf[hs];
            if constexpr (HB == 0) {
              if constexpr (c == 4 || c == 5) mma0(ac[c][t], xf[hs][t], wf);
              else mma0(ac[c][t], wf, xf[hs][t]);
            } else {
              if constexpr (c == 4 || c == 5) mma(ac[c][t], xf[hs][t], wf);
              else mma(ac[c][t], wf, xf[hs][t]);
            }
          }
          SCHED();
          if constexpr (HB < 13) {
            if constexpr (I < 7) { rd(IC<2 * I>{}, IC<HB + 1>{}); rd(IC<2 * I + 1>{}, IC<HB + 1>{}); }
            else rd(IC<I + 7>{}, IC<HB + 1>{});
            if constexpr (I == 12 && HB < 7) setup2(HB_, IC<1>{});
            if constexpr (I == 12 && HB >= 7 && HB < 11) setupq(IC<HB - 7>{});
            if constexpr (I == 14 && HB == TOUCH2_HB && TOUCH) { if (w >= 6) do_touch(); }
          } else {
            if constexpr (I < 8) {
              b_rd(IC<(I >> 2)>{}, IC<(I & 3)>{}, IC<0>{});
              b_rd(IC<(I >> 2)>{}, IC<(I & 3)>{}, IC<1>{});
            }
            if constexpr (I == 5) {
              *(LAS v2u*)(L + hwa) = v2u{pk(ah[0], ah[1]), pk(ah[2], ah[3])};
            }
            if constexpr (I == 8) {
              pack_kv(1);
              *(LAS bf16x8*)(L + nwa) = kf[1];
              *(LAS v4u*)(L + nwa + 1024u) = v4u{vh[1][0][0], vh[1][0][1], vh[1][1][0], vh[1][1][1]};
            }
            if constexpr (I == 10) {
#pragma unroll
              for (int t2 = 0; t2 < 2; ++t2) {
                gv[t2] = rbf(__builtin_amdgcn_rcpf(1.0f + __builtin_amdgcn_exp2f(rbf(ac[6][t2][0]) * -1.4426950408889634f)));
                keep(ac[6][t2]);
                asm volatile("" : "+v"(gv[t2]));
              }
            }
            if constexpr (I == 11) {
              wait_l();
              bar();
              kp = lds128(L, wreg + 4096u + lane * 16u);
              if (tq == 0) vp = __builtin_bit_cast(v4u, cat(ldstr(L, wreg + 5120u + lane * 8u), ldstr(L, wreg + 5632u + lane * 8u)));
              else vp = *(LAS v4u*)(L + wreg + 5120u + lane * 16u);
              pack_q(1);
            }
            if constexpr (I == 13) pack_q(0);
          }
          SCHED();
          d_go(IC<E>{});
        });
        if constexpr (hs == 0) mid(IC<(HB >> 1)>{});
      });
      STAMP(2);
      auto slip = [&](auto I_) {
        if constexpr (decltype(I_)::value < NA) {
          SCHED();
          r_dma(I_);
          SCHED();
        }
      };
      const f32x4 z = {0.f, 0.f, 0.f, 0.f};
      f32x4 s0[2], s1[2];
      s1[1] = mfma(kf[1], qf[1], z);
      s0[0] = mfma(kp, qf[0], z);
      SCHED();
      asm volatile("s_nop 4");
      SCHED();
      pack_kv(0);
      s0[1] = mfma(kf[0], qf[1], z);
      s1[0] = mfma(kf[0], qf[0], z);
      slip(IC<0>{});
      const bool odd = li & 1;
      const unsigned psh = odd ? 16u : 0u;
      float sv[2][4];
      auto pick = [&](f32x4 v, int u) {
        float lo = v[2 * u], hi = v[2 * u + 1];
        asm("" : "+v"(hi));
        return odd ? hi : lo;
      };
      const float SC = 0.18898223650461362f * 1.4426950408889634f;
#pragma unroll
      for (int u = 0; u < 2; ++u) {
        sv[1][2 + u] = __builtin_fmaf(pick(s1[1], u), SC, mb[1][2 + u]);
        sv[0][u] = __builtin_fmaf(pick(s0[0], u), SC, mb[0][u]);
      }
#pragma unroll
      for (int u = 0; u < 2; ++u) {
        sv[1][u] = __builtin_fmaf(pick(s0[1], u), SC, mb[1][u]);
        sv[0][2 + u] = __builtin_fmaf(pick(s1[0], u), SC, mb[0][2 + u]);
      }
      slip(IC<1>{});
      float m[2];
      {
        const float m0 = fmaxf(fmaxf(sv[0][0], sv[0][1]), fmaxf(sv[0][2], sv[0][3])), m1 = fmaxf(fmaxf(sv[1][0], sv[1][1]), fmaxf(sv[1][2], sv[1][3]));
        auto a = __builtin_amdgcn_permlane32_swap(__float_as_uint(m0), __float_as_uint(m1), false, false);
        const float x = fmx(__uint_as_float(a[0]), __uint_as_float(a[1]));
        auto b2 = __builtin_amdgcn_permlane16_swap(__float_as_uint(x), __float_as_uint(x), false, false);
        const float y = fmx(__uint_as_float(b2[0]), __uint_as_float(b2[1]));
        auto c2 = __builtin_amdgcn_permlane32_swap(__float_as_uint(y), __float_as_uint(y), false, false);
        m[0] = __uint_as_float(c2[0]);
        m[1] = __uint_as_float(c2[1]);
      }
      slip(IC<2>{});
      const bf16x8 ones = asfrag(0x3f803f80u, 0x3f803f80u, 0x3f803f80u, 0x3f803f80u);
      f32x4 o[2][2], sm[2];
#pragma unroll
      for (int t = 0; t < 2; ++t) {
        unsigned pw[4];
#pragma unroll
        for (int j = 0; j < 4; ++j) pw[j] = pk(__builtin_amdgcn_exp2f(sv[t][j] - m[t]), 0.f) << psh;
        const bf16x8 pf = asfrag(pw[0], pw[1], pw[2], pw[3]);
        sm[t] = mfma(ones, pf, z);
#pragma unroll
        for (int c = 0; c < 2; ++c) {
          const bf16x8 vf = t == 0 ? asfrag(vp[2 * c], vp[2 * c + 1], vh[0][c][0], vh[0][c][1]) : asfrag(vh[0][c][0], vh[0][c][1], vh[1][c][0], vh[1][c][1]);
          o[t][c] = mfma(vf, pf, z);
        }
      }
      slip(IC<3>{});
      v4u ov[2];
#pragma unroll
      for (int t = 0; t < 2; ++t) {
        const float sc = gv[t] * __builtin_amdgcn_rcpf(sm[t][0]);
        const f32x4 o0 = o[t][0] * sc, o1 = o[t][1] * sc;
        ov[t] = v4u{pk(o0[0], o0[1]), pk(o0[2], o0[3]), pm & pk(o1[0], o1[1]), pm & pk(o1[2], o1[3])};
      }
      SCHED();
      st128(ov[0], rws, lane * 16u, oa);
      st128(ov[1], rws, lane * 16u, oa + A2_TT);
      if (hd == 0 && g < 2) __builtin_amdgcn_raw_buffer_store_b32(__float_as_uint(g ? gv[1] : gv[0]), rws, lane * 4u, WS_G + hp * 8192u + (128 * rb + 32 * tq) * 4u, 16);
    }
  };
  if (ffn) phase1(IC<1>{});
  else if (qkv) phase1(IC<0>{});
  else {
    SCHED();
    wait_args(px, pw, pv, tptr);
    SCHED();
    prep_touch();
    if (TOUCH) do_touch();
    rep<0, 7>([&](auto K_) { setup2(K_, IC<0>{}); });
    for (int i = 0; i < FREE_SLEEP; ++i) __builtin_amdgcn_s_sleep(16);
    rep<0, 16>(b_dma);
    wait_v<0>();
    rep<0, 16>([&](auto I_) { constexpr int I = decltype(I_)::value; b_rd(IC<(I >> 3)>{}, IC<((I >> 1) & 3)>{}, IC<(I & 1)>{}); });
    fo_pub = WS_F + 15 * 64u;
  }
  STAMP(3);
  ct = __builtin_amdgcn_readfirstlane(ct);
  tt0 = __builtin_amdgcn_readfirstlane(tt0);
  const u64 tag = (gm_dispatch_id() + 1) * 0x9E3779B97F4A7C15ull ^ (u64)__builtin_amdgcn_queue_ptr();
  unsigned va[4];
#pragma unroll
  for (int t = 0; t < 4; ++t) va[t] = opaque(lane * 16u + t * A2_TT);
  const unsigned aob = tt0 * A2_TT + (16u + 7u * w) * 1024u, aoa = tt0 * A2_TT + 2u * w * 1024u;
  const unsigned frow = WS_F + (tt0 >> 3) * 1024u, frows = (tt0 & 7) == 7 ? 1u : 0u;
  v4u b2v;
  v2u fh = {(unsigned)tag, (unsigned)(tag >> 32)};
  const bool fhme = li >= ((7 * w) >> 3) && li <= ((7 * w + 6) >> 3) && g <= frows;
  SCHED();
  if (!qkv) {
    wait_l();
    if (NMF) bar();
    rep<0, NMF>(r_dma);
    wait_v<NMF>();
  } else {
    if (QPEEK) { if (fhme) ld64(fh, lane * 64u, rws, frow); }
    rep<NA, NA + NMQ>(r_dma);
    wait_v<NMQ>();
  }
  rep<0, 16>([&](auto I_) { constexpr int I = decltype(I_)::value; b_rd(IC<2 + (I >> 3)>{}, IC<((I >> 1) & 3)>{}, IC<(I & 1)>{}); });
  SCHED();
  STAMP(4);
  bar();
  if (tid < 4) {
    const v2q tv = {tag, tag};
    st128(__builtin_bit_cast(v4u, tv), rws, lane * 16u, fo_pub);
  }
  ld128<0>(b2v, (tid & 7) * 16u, mk(p.b2, D * 2), ct * 128u);
  wait_l();
  if (!qkv) {
    rep<NMF, 8>(r_dma);
    rep<24, 36 - PEEK>(b_dma);
    if (PKBAR) bar();
    if (fhme) ld64(fh, lane * 64u, rws, frow);
    rep<36 - PEEK, 36>(b_dma);
    STAMP(5);
    wait_v<PEEK>();
  } else {
    rep<NA + NMQ, 8>(r_dma);
    STAMP(5);
    if (!QPEEK) {
      if (fhme) ld64(fh, lane * 64u, rws, frow);
      wait_v<0>();
    }
  }

  auto poll = [&](bool todo) {
#pragma unroll 1
    for (unsigned spin = 0; spin < SPIN_CAP; ++spin) {
      if (todo) {
        const v2u v = __builtin_amdgcn_raw_buffer_load_b64(rws, lane * 64u, frow, POLL_AUX);
        todo = __builtin_bit_cast(u64, v) != tag;
      }
      asm volatile("" ::: "memory");
      if (!__any(todo)) break;
      __builtin_amdgcn_s_sleep(POLL_SLEEP);
      if (!ffn && !qkv) __builtin_amdgcn_s_sleep(12);
    }
  };
  asm volatile("" : "+v"(fh));
  if (FSLEEP && !ffn && !qkv) { for (int i = 0; i < FSLEEP; ++i) __builtin_amdgcn_s_sleep(16); }
  {
    const bool todo = __builtin_bit_cast(u64, fh) != tag;
    if (__any(todo)) poll(todo);
  }
  STAMP(6);
  auto phase2 = [&](auto NT_) {
    constexpr int NT = decltype(NT_)::value;
    constexpr bool INL = NT == 3;
    v4u af[WIN][NT], aa[2][NT];
    float gl, og[4];
    v2u fl = {(unsigned)tag, (unsigned)(tag >> 32)};
    const bool fme = li == 7 + w && g <= frows;
    auto a_load = [&](auto J_, auto T_) {
      constexpr int j = decltype(J_)::value, t = decltype(T_)::value;
      if constexpr (j < 7) ld128<0>(af[j % WIN][t], va[t], rws, aob + jr(j) * 1024u);
      else ld128<(j - 7) * 1024u>(aa[j - 7][t], va[t], rws, aoa);
    };
    if (PRIO) __builtin_amdgcn_s_setprio(3);
    rep<0, NT * WIN>([&](auto I_) { constexpr int I = decltype(I_)::value; a_load(IC<I / NT>{}, IC<I % NT>{}); });
    f32x4 acc[4][NT];
    unsigned pa[2];
    auto put = [&](int t) {
#pragma unroll
      for (int c = 0; c < 4; ++c) *(LAS f32x4*)(L + pa[c & 1] + (t * 4 + c) * 1024u) = acc[c][t];
    };
    rep<0, 7>([&](auto J_) {
      constexpr int j = decltype(J_)::value;
      if (PRIO && j == 2) __builtin_amdgcn_s_setprio(2);
      if (PRIO && j == 4) __builtin_amdgcn_s_setprio(1);
      if (PRIO && j == 6) __builtin_amdgcn_s_setprio(0);
      if constexpr (j == 5) {
        wait_for<p2_before(5, 0, NT, INL) - p2_before(3, 1, NT, INL)>(fl);
        const bool todo = __builtin_bit_cast(u64, fl) != tag;
        if (__any(todo)) poll(todo);
        SCHED();
      }
      if constexpr (j == 6) {
#pragma unroll
        for (int c = 0; c < 2; ++c) pa[c] = opaque(wreg + (g * 16u + ((li + 4u * ((2u * c + (g >> 1)) & 3u)) & 15u)) * 16u);
      }
      if constexpr (j >= 4) wait_l();
      rep<0, NT>([&](auto T_) {
        constexpr int t = decltype(T_)::value, U = p2_unit(j, t, NT, INL);
        if constexpr (U >= 0) b_m0(IC<U>{});
        wait_v<p2_before(j, t, NT, INL) - p2_pos(j, t, NT, INL) - 1>();
        const bf16x8 a = __builtin_bit_cast(bf16x8, af[j % WIN][t]);
#pragma unroll
        for (int c = 0; c < 4; ++c) {
          if constexpr (j == 0) mma0(acc[c][t], cat(bl[0][c], bh[0][c]), a);
          else mma(acc[c][t], cat(bl[j & 3][c], bh[j & 3][c]), a);
        }
        SCHED();
        if constexpr (U >= 0) b_go(IC<U>{});
        if constexpr (j < 6) a_load(IC<j + 2>{}, T_);
        if constexpr (j == 5) a_load(IC<8>{}, T_);
        if constexpr (j == 5 && t == 0) ld32<0>(gl, lane * 4u, rws, WS_G + w * 8192u + tt0 * 64u);
        if constexpr (j == 3 && t == 0) { if (fme) ld64(fl, lane * 64u, rws, frow); }
        if constexpr (j >= 2) {
          b_rd(IC<j + 2>{}, T_, IC<0>{});
          b_rd(IC<j + 2>{}, T_, IC<1>{});
          if constexpr (NT == 3 && t == 2) {
            b_rd(IC<j + 2>{}, IC<3>{}, IC<0>{});
            b_rd(IC<j + 2>{}, IC<3>{}, IC<1>{});
          }
        }
        SCHED();
      });
      if constexpr (j == 0) STAMP(16);
      if constexpr (j == 2) STAMP(17);
      if constexpr (j == 4) STAMP(18);
      if constexpr (j == 6) STAMP(19);
    });
    constexpr int NALL = p2_before(6, 0, NT, INL);
    wait_for<NALL - p2_before(5, 1, NT, INL)>(gl);
    gl = rbf(1.0f - gl);
    {
      auto h2 = __builtin_amdgcn_permlane32_swap(__float_as_uint(gl), __float_as_uint(gl), false, false);
      auto lo2 = __builtin_amdgcn_permlane16_swap(h2[0], h2[0], false, false);
      auto hi2 = __builtin_amdgcn_permlane16_swap(h2[1], h2[1], false, false);
      og[0] = __uint_as_float(lo2[0]); og[1] = __uint_as_float(lo2[1]); og[2] = __uint_as_float(hi2[0]); og[3] = __uint_as_float(hi2[1]);
    }
    wait_l();
    if (w == 0) *(LAS float*)(L + SPARE + lane * 4u) = gl;
    rep<0, NT>([&](auto T_) {
      constexpr int t = decltype(T_)::value;
#pragma unroll
      for (int c = 0; c < 4; ++c) acc[c][t] *= og[t];
      SCHED();
      if constexpr (t >= 1) put(t - 1);
      SCHED();
      wait_v<NALL - p2_pos(8, t, NT, INL) - 1>();
      asm volatile("s_nop 0");
#pragma unroll
      for (int c = 0; c < 4; ++c) mma(acc[c][t], cat(bl[3][c], bh[3][c]), __builtin_bit_cast(bf16x8, aa[0][t]));
#pragma unroll
      for (int c = 0; c < 4; ++c) mma(acc[c][t], cat(bl[0][c], bh[0][c]), __builtin_bit_cast(bf16x8, aa[1][t]));
      SCHED();
    });
    STAMP(7);
    asm volatile("s_nop 4" ::: "memory");
    SCHED();
    put(NT - 1);
  };
  if (qkv) phase2(IC<3>{});
  else phase2(IC<4>{});
  asm volatile("" : "+v"(b2v));
  wait_l();
  bar();
  STAMP(8);
  if (!qkv || w < 6) {
    const unsigned tk = tid >> 3, q = tid & 7;
    const unsigned n = (tk >> 4) * 4u + (q >> 1), gg = 2 * (q & 1), i = tk & 15;
    const unsigned ad = n * 1024u + (gg * 16u + ((i + 4u * (q & 3u)) & 15u)) * 16u;
    const float og = *(LAS float*)(L + SPARE + tk * 4u);
    f32x4 alo = f32x4{bflo(b2v[0]), bfhi(b2v[0]), bflo(b2v[1]), bfhi(b2v[1])} * og, ahi = f32x4{bflo(b2v[2]), bfhi(b2v[2]), bflo(b2v[3]), bfhi(b2v[3])} * og;
#pragma unroll
    for (int k = 0; k < 8; ++k) {
      alo += *(LAS f32x4*)(L + k * REG + ad);
      ahi += *(LAS f32x4*)(L + k * REG + ad + 256u);
    }
    const v4u o = {pk(alo[0], alo[1]), pk(alo[2], alo[3]), pk(ahi[0], ahi[1]), pk(ahi[2], ahi[3])};
    *(GAS v4u*)((char*)p.out + (16 * tt0 + tk) * 896u + ct * 128u + q * 16u) = o;
  }
  STAMP(9);
  STAMP_FLUSH();
}
}

extern "C" void kernel_launch(void* const* d_in, const int* in_sizes, int n_in, void* d_out, int out_size, void* d_ws, size_t ws_size,
                              hipStream_t stream) {
  gm::Params p;
  p.x = d_in[0];
  p.wqkv = d_in[1];
  p.wo = d_in[2];
  p.w1 = d_in[4];
  p.b1 = d_in[5];
  p.w2 = d_in[6];
  p.b2 = d_in[7];
  p.gw = d_in[8];
  p.out = d_out;
  p.ws = d_ws;
  gm::gm_kernel<<<256, 512, 0, stream>>>(p);
}
```

```cpp
#include <hip/hip_runtime.h>
#include <stdint.h>

typedef short bf16x8 __attribute__((ext_vector_type(8)));
typedef short s16x4 __attribute__((ext_vector_type(4)));
typedef float f32x4 __attribute__((ext_vector_type(4)));
typedef unsigned v4u __attribute__((ext_vector_type(4)));
typedef unsigned v2u __attribute__((ext_vector_type(2)));
typedef __bf16 bf2_t __attribute__((ext_vector_type(2)));
typedef float f32x2 __attribute__((ext_vector_type(2)));
#define LAS __attribute__((address_space(3)))
#define GAS __attribute__((address_space(1)))
#define DI __device__ __forceinline__
typedef unsigned long long u64;
typedef __amdgpu_buffer_rsrc_t rsrc_t;
typedef u64 v8q __attribute__((ext_vector_type(8)));
typedef u64 v2q __attribute__((ext_vector_type(2)));

#define CODE_PF 23552
#define TOUCH 1
#define TOUCH2_HB 3
#define WINDOW 2
#define PRIO 1
#define POLL_SLEEP 1
#define FLAG_STRIDE 256
#define POLL_AUX 16
#define FLAG_ST 2
#define TMODE 1
#define TCNT 0x0C31151C38101520ull
#define TFIRST 0x00003F5400000000ull
#define TSH 2u
#define POLL_WAVE 0
#define QPRE 6
#define QSLOW 0
#define TLATE 0
#define PEEK 0
#define PKBAR 0
#define QPEEK 0
#define L2W 0
#define L2W_SLEEP 3
#define ROT 0u
#define L2W_HB 6
#define FSLEEP 0
#define FREE_SLEEP 0
#define TABL 0
#define FINE0 1
#define RUNA 0
#define VROT 0
#define NE 0
#define INL4 0
#define INL4_AT 0
#define RATIO 1
#define NA 0
#define NMF 4
#define NMQ 4
#define FBAR 1
#define DEFER 0
#define PBAR 1

extern "C" __device__ u64 gm_dispatch_id() __asm("llvm.amdgcn.dispatch.id");

namespace gm {
constexpr int S = 2048, D = 448, F = 1792, NQKV = 1344;
constexpr int KS_A = 16, KS = 72;
constexpr unsigned A2_TT = KS * 1024;
constexpr unsigned WS_G = 128 * A2_TT, WS_F = WS_G + 8 * 8192, WS_END = WS_F + 16 * 1024;
constexpr unsigned REG = 20480, STG = 6144, SPARE = 3 * STG;
constexpr int LDS_BYTES = 163840;
constexpr unsigned GW_OFF = 2 * REG + 5120;
constexpr unsigned SPIN_CAP = 1u << 16;

struct Params {
  const void *x, *wqkv, *wo, *w1, *b1, *w2, *b2, *gw;
  void *out, *ws;
};
template <int N> struct IC { static constexpr int value = N; };

DI rsrc_t mk(const void* p, unsigned n) { return __builtin_amdgcn_make_buffer_rsrc((void*)p, 0, n, 0x00020000); }
template <unsigned C> DI void set_m0(unsigned base) { asm volatile("s_add_u32 m0, %0, %1" ::"s"(base), "i"(C) : "scc", "memory"); }
template <unsigned IMM> DI void dma_go(unsigned voff, rsrc_t rs, unsigned soff) {
  asm volatile("buffer_load_dwordx4 %0, %1, %2 offen offset:%3 lds" ::"v"(voff), "s"(rs), "s"(soff), "i"(IMM) : "memory");
}
template <unsigned IMM> DI void dma_go0(unsigned voff, rsrc_t rs) {
  asm volatile("buffer_load_dwordx4 %0, %1, 0 offen offset:%2 lds" ::"v"(voff), "s"(rs), "i"(IMM) : "memory");
}
template <unsigned IMM> DI void ld128(v4u& d, unsigned voff, rsrc_t rs, unsigned soff) {
  asm volatile("buffer_load_dwordx4 %0, %1, %2, %3 offen offset:%4 sc1" : "=v"(d) : "v"(voff), "s"(rs), "s"(soff), "i"(IMM) : "memory");
}
template <unsigned IMM> DI void ld32(float& d, unsigned voff, rsrc_t rs, unsigned soff) {
  asm volatile("buffer_load_dword %0, %1, %2, %3 offen offset:%4 sc1" : "=v"(d) : "v"(voff), "s"(rs), "s"(soff), "i"(IMM) : "memory");
}
DI void ld64(v2u& d, unsigned voff, rsrc_t rs, unsigned soff) {
  asm volatile("buffer_load_dwordx2 %0, %1, %2, %3 offen sc1" : "+v"(d) : "v"(voff), "s"(rs), "s"(soff) : "memory");
}
template <int N, class T> DI void wait_for(T& v) { asm volatile("s_waitcnt vmcnt(%1)" : "+v"(v) : "i"(N) : "memory"); }
DI void touch(unsigned lds, unsigned voff, u64 base) {
  asm volatile("s_mov_b32 m0, %0\n\ts_nop 0\n\tglobal_load_lds_dword %1, %2" ::"s"(lds), "v"(voff), "s"(base) : "memory");
}
#define NOP1() asm volatile("s_nop 0")
DI f32x4 mfma(bf16x8 a, bf16x8 b, f32x4 c) { return __builtin_amdgcn_mfma_f32_16x16x32_bf16(a, b, c, 0, 0, 0); }
DI void mma(f32x4& c, bf16x8 a, bf16x8 b) { asm volatile("v_mfma_f32_16x16x32_bf16 %0, %1, %2, %0" : "+v"(c) : "v"(a), "v"(b)); }
DI void mma0(f32x4& c, bf16x8 a, bf16x8 b) { asm volatile("v_mfma_f32_16x16x32_bf16 %0, %1, %2, 0" : "=&v"(c) : "v"(a), "v"(b)); }
DI void mmac(f32x4& d, bf16x8 a, bf16x8 b, f32x4 c) { asm volatile("v_mfma_f32_16x16x32_bf16 %0, %1, %2, %3" : "=&v"(d) : "v"(a), "v"(b), "v"(c)); }
DI float relu(float v) {
  float r;
  asm("v_max_f32 %0, 0, %1" : "=v"(r) : "v"(v));
  return r;
}
DI float fmx(float a, float b) {
  float r;
  asm("v_max_f32 %0, %1, %2" : "=v"(r) : "v"(a), "v"(b));
  return r;
}
DI unsigned pk(float lo, float hi) {
  f32x2 v = {lo, hi};
  return __builtin_bit_cast(unsigned, __builtin_convertvector(v, bf2_t));
}
DI float rbf(float f) { return __uint_as_float(pk(f, 0.f) << 16); }
DI float bflo(unsigned u) { return __uint_as_float(u << 16); }
DI float bfhi(unsigned u) { return __uint_as_float(u & 0xffff0000u); }
DI bf16x8 asfrag(unsigned a, unsigned b, unsigned c, unsigned d) {
  v4u v = {a, b, c, d};
  return __builtin_bit_cast(bf16x8, v);
}
DI void st128(v4u v, rsrc_t rs, unsigned voff, unsigned soff) {
  __builtin_amdgcn_raw_buffer_store_b128(v, rs, voff, soff, 16);
  __builtin_amdgcn_sched_barrier(0);
  asm volatile("s_nop 1");
  __builtin_amdgcn_sched_barrier(0);
}
DI bf16x8 cat(s16x4 lo, s16x4 hi) { return __builtin_shufflevector(lo, hi, 0, 1, 2, 3, 4, 5, 6, 7); }
DI bf16x8 lds128(LAS char* L, unsigned a) { return *(LAS bf16x8*)(L + a); }
DI s16x4 ldstr(LAS char* L, unsigned a) { return __builtin_amdgcn_ds_read_tr16_b64_v4i16((LAS s16x4*)(L + a)); }
DI unsigned opaque(unsigned v) {
  asm volatile("" : "+v"(v));
  return v;
}
DI unsigned sopaque(unsigned v) {
  asm volatile("" : "+s"(v));
  return v;
}
DI float xmax(float v) {
  auto a = __builtin_amdgcn_permlane32_swap(__float_as_uint(v), __float_as_uint(v), false, false);
  v = fmx(__uint_as_float(a[0]), __uint_as_float(a[1]));
  auto b = __builtin_amdgcn_permlane16_swap(__float_as_uint(v), __float_as_uint(v), false, false);
  return fmx(__uint_as_float(b[0]), __uint_as_float(b[1]));
}
DI float xsum(float v) {
  auto a = __builtin_amdgcn_permlane32_swap(__float_as_uint(v), __float_as_uint(v), false, false);
  v = __uint_as_float(a[0]) + __uint_as_float(a[1]);
  auto b = __builtin_amdgcn_permlane16_swap(__float_as_uint(v), __float_as_uint(v), false, false);
  return __uint_as_float(b[0]) + __uint_as_float(b[1]);
}
#define SCHED() __builtin_amdgcn_sched_barrier(0)
#define STAMP(k)
#define STAMP_FLUSH()
#define FSTAMP(k)
template <int N> DI void wait_v() { asm volatile("s_waitcnt vmcnt(%0)" ::"i"(N) : "memory"); }
DI void wait_l() {
  SCHED();
  __builtin_amdgcn_s_waitcnt(0xC07F);
  asm volatile("" ::: "memory");
  SCHED();
}
DI void wait_args(u64& a, u64& b, u64& c, u64& d) {
  asm volatile("s_waitcnt lgkmcnt(0)" ::: "memory");
  SCHED();
  asm volatile("" : "+s"(a), "+s"(b), "+s"(c), "+s"(d)::"memory");
}
DI void keep(f32x4 v) { asm volatile("" ::"v"(v)); }
DI void bar() { asm volatile("s_barrier" ::: "memory"); }

constexpr unsigned hloc(int h) {
  h = h >= 10 ? h - 10 : h;
  return h == 0 ? SPARE : (h < 4 ? STG + (h - 1) * 2048u : (h < 7 ? 2 * STG + (h - 4) * 2048u : (h - 7) * 2048u));
}
constexpr unsigned ppos(int i) { return i < 2 ? i + 1 : (i == 2 ? 0 : i); }

constexpr int q_piece(int k, bool ffn) { return ffn ? k : (k + 5) % 6; }
constexpr int q_item(int i, bool ffn) { return i < 36 ? 8 * (1 + i / 6) + q_piece(i % 6, ffn) : 100 + (i - 36); }
constexpr int q_from(int item) {
  if (item < 100) return (item >> 3) <= 2 ? 0 : 2 * ((item >> 3) - 3) + 1;
  const int h = (item - 100) >> 1;
  return h == 0 ? 1 : (h < 4 ? 9 : (h < 7 ? 11 : 13));
}
constexpr int q_slots(int hb) { return hb < QSLOW ? 3 : 4; }
constexpr int q_slot(int hb, int i) { return hb < QSLOW ? (i % 5 == 1 ? i / 5 : -1) : ((i & 3) == 1 ? i >> 2 : -1); }
constexpr int q_start(int hb) {
  int i = QPRE;
  for (int h = 0; h < hb; ++h)
    for (int k = 0; k < q_slots(h); ++k)
      if (i < 52 && q_from(q_item(i, true)) <= h) ++i;
  return i;
}
constexpr int q_at(int hb, int i, bool ffn) {
  const int k = q_slot(hb, i);
  return k >= 0 && q_start(hb) + k < q_start(hb + 1) ? q_item(q_start(hb) + k, ffn) : -1;
}
constexpr int q_count(int from, int to, bool ffn) {
  int n = 0;
  for (int i = from; i < to; ++i) n += (ffn || i >= 36 || i % 6 != 0) ? 1 : 0;
  return n;
}
constexpr int q_wait(int s, bool ffn) { return q_count(s < 6 ? 6 * (s + 1) : 44, q_start(2 * s + 1), ffn); }
static_assert(q_start(14) == 52 && q_start(1) >= 6 && q_start(3) >= 12 && q_start(5) >= 18 && q_start(7) >= 24 && q_start(9) >= 30 && q_start(11) >= 36 && q_start(13) >= 44, "order");
constexpr int r_unit(int i) { return i < 2 ? 16 + i : (i < 6 ? 18 + i : 12 + i); }
constexpr int v_unit(int v, int i) {
  constexpr int ord[4][5] = {{4, 5, 6, 7, 8}, {5, 7, 8, 4, 6}, {6, 8, 4, 5, 7}, {4, 7, 5, 8, 6}};
  return 4 * ord[v][i >> 2] + (v == 3 ? 3 - (i & 3) : (i & 3));
}
static_assert(NE + NMF <= 12 && 2 * NE / 7 <= 4, "homes of the early weight units");
static_assert(INL4 <= 12 && INL4_AT + INL4 <= 17, "landing of the in-loop weight units");
constexpr int WIN = WINDOW;
static_assert(WIN == 2, "the landing of the in-loop weight units is derived for a window of 2");
constexpr int p2_unit(int j, int t, int nt, int inl) {
  const int i = j * nt + t - (nt == 4 ? INL4_AT : 0);
  return i >= 0 && i < inl ? 36 - inl + i : -1;
}
constexpr int p2_after(int j, int t, int nt, int inl) {
  return (p2_unit(j, t, nt, inl) >= 0 ? 1 : 0) + (j < 5 ? 1 : (j == 5 ? 2 : 0))
         + (j == 5 && t == 0 ? 1 : 0)
         + (j == 3 && t == 0 ? 1 : 0);
}
constexpr int p2_before(int j, int t, int nt, int inl) {
  int n = nt * WIN;
  for (int i = 0; i < nt * j + t; ++i) n += p2_after(i / nt, i % nt, nt, inl);
  return n;
}
constexpr int p2_pos(int j, int t, int nt, int inl) {
  return j < WIN ? nt * j + t : (j == 8 ? p2_before(5, t, nt, inl) + 1 : p2_before(j - WIN, t, nt, inl) + (p2_unit(j - WIN, t, nt, inl) >= 0 ? 1 : 0));
}
template <int I, int N, class Fn> DI void rep(Fn&& f) {
  if constexpr (I < N) {
    f(IC<I>{});
    rep<I + 1, N>(f);
  }
}

__global__ __launch_bounds__(512, 2) void gm_kernel(Params p) {
  __shared__ __attribute__((aligned(1024))) char smem[LDS_BYTES];
  LAS char* L = (LAS char*)smem;
  const unsigned tid = threadIdx.x, lane = tid & 63;
  const unsigned b = blockIdx.x, xcd = b & 7, slot = b >> 3;
  const bool ffn = b < 112, qkv = b >= 128;
  u64 px, pw, pv, tptr;
  asm volatile("s_load_dwordx2 %0, %3, 0x0\n\ts_load_dwordx2 %1, %3, %4\n\ts_load_dwordx2 %2, %3, %5"
               : "=&s"(px), "=&s"(pw), "=&s"(pv)
               : "s"(__builtin_amdgcn_kernarg_segment_ptr()), "s"(ffn ? 24u : 8u), "s"(ffn ? 32u : 56u));
  if (tid < CODE_PF / 1024) {
    u64 pc;
    asm volatile("s_getpc_b64 %0" : "=s"(pc));
    touch(SPARE + 1024u, ((slot & 7) * (CODE_PF / 1024) + tid) * 128u, pc);
  }
  const unsigned w = __builtin_amdgcn_readfirstlane(tid >> 6);
  asm volatile("s_load_dwordx2 %0, %1, %2" : "=&s"(tptr) : "s"(__builtin_amdgcn_kernarg_segment_ptr()), "s"(((0x25130130u >> (4 * w)) & 7u) * 8u));
  const unsigned wreg = w * REG;
  const unsigned li = lane & 15, g = lane >> 4, rr = lane >> 3, pos = lane & 7;
  const unsigned rq = xcd >> 1, side = xcd & 1;
  unsigned tcnt = 0, tln = 0;
  auto prep_touch = [&]() {
    tcnt = (unsigned)(TCNT >> (8 * w)) & 0xffu;
    const unsigned tfirst = ((unsigned)(TFIRST >> (8 * w)) & 0xffu) * 64u;
    const unsigned tsh = w ? 6u : TSH;
    tln = opaque(((32 * xcd + slot) * (w ? tcnt : 56u) + tfirst + (lane >> tsh) * 7u + (lane & ((1u << tsh) - 1u))) * 128u);
  };
  auto do_touch = [&]() {
    if (lane < tcnt) touch(wreg + SPARE + 1024u, tln, tptr);
  };

  rsrc_t rws, rw2, rwo;
  unsigned vb[2], ba[4], fo_pub;
  const unsigned bo2 = w * (224u * 896u), boo = w * (56u * 896u);
  unsigned ct = 0, tt0 = 0;
  const unsigned q4 = (lane >> 2) & 3, p4 = lane & 3;
  const unsigned fsw = (q4 >> 1) | ((g & 1) << 1);
  s16x4 bl[4][4], bh[4][4];
  auto b_m0 = [&](auto N_) {
    constexpr int N = decltype(N_)::value;
    set_m0<hloc(N >> 1) + (N & 1) * 1024u>(wreg);
  };
  auto jr = [&](int j) -> unsigned {
    const unsigned a = (unsigned)j + (ROT);
    return a >= 7 ? a - 7 : a;
  };
  auto b_go = [&](auto N_) {
    constexpr int N = decltype(N_)::value, j = N >> 2;
    if constexpr (j < 7) dma_go<0>(vb[N & 1], rw2, bo2 + (2 * jr(j) + ((N >> 1) & 1)) * (16u * 896u));
    else dma_go<0>(vb[N & 1], rwo, boo + ((j - 7) * 28u + ((N >> 1) & 1) * 16u) * 896u);
  };
  auto b_dma = [&](auto N_) { b_m0(N_); NOP1(); b_go(N_); };
  auto r_dma = [&](auto I_) { b_dma(IC<r_unit(decltype(I_)::value)>{}); };
  auto b_rd = [&](auto J_, auto C_, auto E_) {
    constexpr int J = decltype(J_)::value, C = decltype(C_)::value, E = decltype(E_)::value;
    if constexpr (E == 0) bl[J & 3][C] = ldstr(L, ba[C] + hloc(2 * J));
    else bh[J & 3][C] = ldstr(L, ba[C] + hloc(2 * J + 1));
  };
  auto setup2 = [&](auto K_, auto Q_) {
    constexpr int K = decltype(K_)::value;
    constexpr bool Q = decltype(Q_)::value;
    if constexpr (K == 0) {
      ct = Q ? (slot >> 2) - 1 : (unsigned)max((int)(slot >> 2), (int)slot - 9);
      tt0 = 16 * xcd + (Q ? 4 + 3 * (slot & 3) : (slot < 12 ? 4 * (slot & 3) : 0u));
      rws = mk(p.ws, WS_END);
      rw2 = mk(p.w2, F * D * 2);
      rwo = mk(p.wo, D * D * 2);
    } else if constexpr (K == 1) {
    } else if constexpr (K == 2) {
      vb[0] = opaque(rr * 896u + ((pos ^ (((rr >> 1) & 3) << 1)) << 4) + ct * 128u);
      vb[1] = opaque(vb[0] + 8u * 896u);
    } else if constexpr (K < 7) {
      ba[K - 3] = opaque(wreg + (4 * g + q4) * 128u + ((2u * ((K - 3) ^ fsw) + (p4 >> 1)) << 4) + (p4 & 1) * 8u);
    }
  };

  auto phase1 = [&](auto FFN_) {
    constexpr bool FFN = decltype(FFN_)::value;
    constexpr int NP = FFN ? 6 : 5;
    constexpr unsigned LDW = FFN ? 3584u : 2688u, WUS = FFN ? 128u : 896u;
    const bool mid3 = FFN && slot >= 12;
    const unsigned rb = 4 * rq + (mid3 ? 2 * side + (slot & 1) : slot & 3), cs = mid3 ? 3u : side * 4 + (slot >> 2), hp = side * 4 + ((slot >> 2) & 3);
    unsigned vx[3];
#pragma unroll
    for (int h = 0; h < 2; ++h) vx[h] = opaque((128 * rb + 16 * w + 8 * h + rr) * 896u + ((pos ^ (4u * h + (rr >> 1))) << 4));
    if (!FFN) vx[2] = opaque(((rb ? 128 * rb - 16 : 0u) + 8 * (w & 1) + rr) * 896u + ((pos ^ (4u * (w & 1) + (rr >> 1))) << 4));
    unsigned wch = pos ^ ((((rr >> 1) & 1) | ((w & 1) << 1)) << 1);
    if (!FFN) wch = min(wch, 6u);
    const unsigned vw = opaque((8 * w + rr) * LDW + (wch << 4) + (FFN ? cs * 512u : hp * 112u));
    if (!TLATE) prep_touch();
    SCHED();
    wait_args(px, pw, pv, tptr);
    SCHED();
    STAMP(10);
    const rsrc_t rx = mk((const void*)px, S * D * 2);
    const rsrc_t rw = mk((const void*)pw, FFN ? D * F * 2 : D * NQKV * 2);
    auto d_m0 = [&](auto E_) {
      constexpr int E = decltype(E_)::value, I = E & 7, ST = E >> 3;
      if constexpr (E >= 100) b_m0(IC<E - 100>{});
      else if constexpr (E >= 0) set_m0<(ST % 3) * STG + ppos(I) * 1024u - ((I < 2 || (!FFN && I == 5)) ? ST * 128u : (I - 2) * WUS)>(wreg);
    };
    auto d_go = [&](auto E_) {
      constexpr int E = decltype(E_)::value, I = E & 7, ST = E >> 3;
      if constexpr (E >= 100) b_go(IC<E - 100>{});
      else if constexpr (E >= 0) {
        if constexpr (I < 2) dma_go0<ST * 128u>(vx[I], rx);
        else if constexpr (!FFN && I == 5) { if (w < 2) dma_go0<ST * 128u>(vx[2], rx); }
        else if constexpr (ST == 0) dma_go0<(I - 2) * WUS>(vw, rw);
        else dma_go<(I - 2) * WUS>(vw, rw, ST * 64u * LDW);
      }
    };
    if (TOUCH && !TLATE && w < 3) do_touch();
    rep<0, 6>([&](auto I_) { d_m0(I_); NOP1(); d_go(I_); });
    if (w == 2) {
      set_m0<FFN ? SPARE : GW_OFF>(0);
      NOP1();
      dma_go0<0>(lane * 16, mk((const char*)pv + (FFN ? cs * 512u : 0u), FFN ? 512 : 896));
    }
    SCHED();
    if (TLATE) {
      prep_touch();
      if (TOUCH && w < 3) do_touch();
    }
    STAMP(11);
    const unsigned xa0 = 1024u + (li >> 3) * 1024u + (li & 7) * 128u + ((g ^ ((li >> 1) & 7)) << 4);
    auto second_stage = [&]() {
      SCHED();
      STAMP(12);
      if (PBAR != 0 && QPRE != 0) bar();
      rep<0, QPRE>([&](auto I_) { constexpr int E = q_item(decltype(I_)::value, FFN); d_m0(IC<E>{}); NOP1(); d_go(IC<E>{}); });
      STAMP(14);
      if (TOUCH && w < 6 && (TLATE || w >= 3)) {
        if (w >= 3) do_touch();
        wait_v<q_count(0, QPRE, FFN) + 1>();
      } else {
        wait_v<q_count(0, QPRE, FFN)>();
      }
      bar();
      STAMP(1);
    };
    auto mid = [&](auto S_) {
      constexpr int s = decltype(S_)::value;
      wait_v<q_wait(s, FFN)>();
      wait_l();
      bar();
      FSTAMP(20 + s);
    };

    if constexpr (FFN) {
      const unsigned mh = w >> 2, nq = w & 3;
      unsigned xa[2][2], wa[2][4];
#pragma unroll
      for (int hs = 0; hs < 2; ++hs) {
#pragma unroll
        for (int t2 = 0; t2 < 2; ++t2) xa[hs][t2] = opaque((xa0 ^ (hs * 64u)) + (mh * 4u + t2 * 2u) * REG);
#pragma unroll
        for (int c = 0; c < 4; ++c)
          wa[hs][c] = opaque((4u * hs + g) * REG + (nq ? 2u + nq : 0u) * 1024u + q4 * 128u + ((2u * (c ^ fsw) + (p4 >> 1)) << 4) + (p4 & 1) * 8u);
      }
      second_stage();
      bf16x8 xf[2][4];
      s16x4 wl[2][4], wh[2][4];
      f32x4 acc[4][4];
      auto rd = [&](auto I_, auto HB_) {
        constexpr int I = decltype(I_)::value, HB = decltype(HB_)::value, hs = HB & 1;
        constexpr unsigned bo = ((HB >> 1) % 3) * STG;
        if constexpr (I >= 2 && I < 6) xf[hs][I - 2] = lds128(L, xa[hs][(I - 2) >> 1] + ((I - 2) & 1) * REG + bo);
        else if constexpr (I < 12) {
          constexpr int c = I < 2 ? 0 : (I - 4) >> 1;
          if constexpr ((I & 1) == 0) wl[hs][c] = ldstr(L, wa[hs][c] + bo);
          else wh[hs][c] = ldstr(L, wa[hs][c] + bo + 512u);
        }
      };
      f32x4 bv[4];
#pragma unroll
      for (int c = 0; c < 4; ++c) {
        const v2u bb = *(LAS v2u*)(L + SPARE + nq * 128u + c * 32u + g * 8u);
        bv[c] = f32x4{bflo(bb.x), bfhi(bb.x), bflo(bb.y), bfhi(bb.y)};
      }
      rep<0, 12>([&](auto I_) { rd(I_, IC<0>{}); });
#pragma unroll
      for (int c = 0; c < 4; ++c) asm volatile("" : "+v"(bv[c]));
      unsigned so = 0;
      auto epi = [&](int t, int u) {
        float hv[8];
#pragma unroll
        for (int j = 0; j < 8; ++j) {
          const float r = relu(acc[2 * u + (j >> 2)][t][j & 3]);
          hv[j] = r * r;
        }
        const v4u o = {pk(hv[0], hv[1]), pk(hv[2], hv[3]), pk(hv[4], hv[5]), pk(hv[6], hv[7])};
        st128(o, rws, lane * 16u + u * 1024u, so + t * A2_TT);
      };
      rep<0, 12>([&](auto HB_) {
        constexpr int HB = decltype(HB_)::value, hs = HB & 1;
        if constexpr (hs == 0) {
          if constexpr (HB > 0 || !FINE0) wait_l();
          if constexpr (HB == 0) asm volatile("s_nop 1");
        }
        rep<0, 16>([&](auto I_) {
          constexpr int I = decltype(I_)::value;
          constexpr int E = q_at(HB, I, FFN);
          d_m0(IC<E>{});
          if constexpr (HB == 0) mmac(acc[I >> 2][I & 3], cat(wl[hs][I >> 2], wh[hs][I >> 2]), xf[hs][I & 3], bv[I >> 2]);
          else mma(acc[I >> 2][I & 3], cat(wl[hs][I >> 2], wh[hs][I >> 2]), xf[hs][I & 3]);
          SCHED();
          rd(I_, IC<HB + 1>{});
          if constexpr (I == 14 && HB < 7) setup2(HB_, IC<0>{});
          if constexpr (I == 14 && HB == 7) {
            so = sopaque((8 * rb + 4 * mh) * A2_TT + (KS_A + 8 * cs + 2 * nq) * 1024u);
            fo_pub = sopaque(WS_F + rb * 1024u + cs * 64u);
          }
          if constexpr (I == 15 && HB == TOUCH2_HB && TOUCH) { if (w >= 6) do_touch(); }
          if constexpr (I == 15 && HB == L2W_HB && L2W) {
            if (lane < 35) touch(wreg + SPARE + 1024u, ((224u * w + 128u + min(7u * slot + lane / 5u, 95u)) * 7u + lane % 5u) * 128u, (u64)p.w2);
          }
          SCHED();
          d_go(IC<E>{});
        });
        if constexpr (hs == 0) mid(IC<(HB >> 1)>{});
        if constexpr (HB == 10 && RUNA == 2) { if (w < 4) __builtin_amdgcn_s_setprio(2); }
      });
      auto eslip = [&](auto K_) {
        constexpr int K = decltype(K_)::value;
        if constexpr (K == 2) wait_l();
        rep<K * NE / 7, (K + 1) * NE / 7>([&](auto I_) { b_dma(IC<16 + decltype(I_)::value>{}); });
      };

      wait_l();
      rep<0, 32>([&](auto M_) {
        constexpr int M = decltype(M_)::value, G = M >> 2, c = M & 3;
        constexpr int hs = G < 4 ? G >> 1 : G & 1, t = G < 4 ? G & 1 : G >> 1;
        constexpr int E = M < 8 ? ((M & 1) == 0 ? q_at(12, 2 * M + 1, FFN) : -1) : (M < 24 && !RUNA ? q_at(13, M - 8, FFN) : -1);
        d_m0(IC<E>{});
        mma(acc[c][t], cat(wl[hs][c], wh[hs][c]), xf[hs][t]);
        SCHED();
        if constexpr (M < 6) { rd(IC<2 * M>{}, IC<13>{}); rd(IC<2 * M + 1>{}, IC<13>{}); }
        if constexpr (M >= 8 && M < 14) {
          rep<0, (M < 12 ? 3 : 2)>([&](auto Q_) {
            constexpr int Q = (M < 12 ? 3 * (M - 8) : 12 + 2 * (M - 12)) + decltype(Q_)::value;
            b_rd(IC<(Q >> 3)>{}, IC<((Q >> 1) & 3)>{}, IC<(Q & 1)>{});
          });
        }
        if constexpr (M == 14) { epi(0, 0); eslip(IC<0>{}); }
        if constexpr (M == 17) { epi(0, 1); eslip(IC<1>{}); }
        if constexpr (M == 20) { epi(1, 0); eslip(IC<2>{}); }
        if constexpr (M == 23) { epi(1, 1); eslip(IC<3>{}); }
        if constexpr (M == 26) { epi(2, 0); eslip(IC<4>{}); }
        if constexpr (M == 29) { epi(2, 1); eslip(IC<5>{}); }
        SCHED();
        d_go(IC<E>{});
        if constexpr (M == 7) {
          if constexpr (RUNA) {
            wait_v<q_wait(6, FFN)>();
            wait_l();
          } else mid(IC<6>{});
        }
      });
      STAMP(2);
      asm volatile("s_nop 5" ::: "memory");
      SCHED();
      epi(3, 0);
      eslip(IC<6>{});
      epi(3, 1);
    } else {
      const unsigned tq = w & 3, hd = w >> 2;
      unsigned xa[2], wa[2][2];
#pragma unroll
      for (int hs = 0; hs < 2; ++hs) {
        xa[hs] = opaque((xa0 ^ (hs * 64u)) + tq * 2u * REG);
#pragma unroll
        for (int c = 0; c < 2; ++c) {
          const unsigned bo = 56u * hd + 32u * c + 8u * p4;
          wa[hs][c] = opaque((4u * hs + g) * REG + q4 * 128u + (((bo >> 4) ^ (fsw << 1)) << 4) + (bo & 8u));
        }
      }
      const unsigned ga = opaque(GW_OFF + g * 16u);
      unsigned xh[2], wq[2];
#pragma unroll
      for (int hs = 0; hs < 2; ++hs) {
        xh[hs] = opaque((li >> 3) * REG + 5120u + (li & 7) * 128u + (((4u * hs + g) ^ ((li >> 1) & 7)) << 4));
        const unsigned bo = 56u * hd + 32u * (tq & 1) + 8u * p4;
        wq[hs] = opaque((4u * hs + g) * REG + q4 * 128u + (((bo >> 4) ^ (fsw << 1)) << 4) + (bo & 8u) + (3u + (tq >> 1)) * 1024u);
      }
      second_stage();
      float mb[2][4];
      const unsigned head = 2 * hp + hd;
      unsigned oa = 0, hwa = 0, nwa = 0, pm = 0;
      auto setupq = [&](auto K_) {
        constexpr int K = decltype(K_)::value, t = K & 1;
        if constexpr (K < 2) {
#pragma unroll
          for (int j = 0; j < 4; ++j) {
            const int kap = 16 * (j >> 1) + (int)(4 * g) + 2 * (j & 1) + (int)(li & 1) - 16;
            const int dl = (int)li - kap;
            const bool ok = (dl >= 0) && (dl <= 14) && (t == 1 || j >= 2 || (rb | tq) != 0);
            mb[t][j] = __uint_as_float(opaque(ok ? 0u : 0xff800000u));
          }
        } else if constexpr (K == 2) {
          oa = sopaque((8 * rb + 2 * tq) * A2_TT + head * 1024u);
          fo_pub = sopaque(WS_F + rb * 1024u + (7 + hp) * 64u);
        } else if constexpr (K == 3) {
          hwa = opaque(hd * 4u * REG + (tq < 2 ? 4096u + lane * 16u + tq * 8u : 4096u + tq * 512u + li * 32u + g * 8u));
          nwa = opaque(wreg + (tq < 3 ? REG + 4096u : 2048u) + lane * 16u);
          pm = opaque(g == 3 ? 0u : 0xffffffffu);
        }
      };
      f32x4 ah;
      bf16x8 hf[2];
      s16x4 hl[2], hh[2];
      f32x4 ac[7][2];
      bf16x8 xf[2][2], gf[2];
      s16x4 wl[2][6], wh[2][6];
      auto rd = [&](auto I_, auto HB_) {
        constexpr int I = decltype(I_)::value, HB = decltype(HB_)::value, hs = HB & 1;
        constexpr unsigned bo = ((HB >> 1) % 3) * STG;
        if constexpr (I == 2 || I == 3) xf[hs][I - 2] = lds128(L, xa[hs] + (I - 2) * REG + bo);
        else if constexpr (I < 14) {
          constexpr int c = I < 2 ? 0 : (I - 2) >> 1;
          if constexpr ((I & 1) == 0) wl[hs][c] = ldstr(L, wa[hs][c & 1] + ppos(2 + (c >> 1)) * 1024u + bo);
          else wh[hs][c] = ldstr(L, wa[hs][c & 1] + ppos(2 + (c >> 1)) * 1024u + bo + 512u);
        } else if constexpr (I == 14) gf[hs] = lds128(L, ga + HB * 64u);
        else if constexpr (I == 15) hf[hs] = lds128(L, xh[hs] + bo);
        else if constexpr (I == 16) hl[hs] = ldstr(L, wq[hs] + bo);
        else if constexpr (I == 17) hh[hs] = ldstr(L, wq[hs] + bo + 512u);
      };
      bf16x8 qf[2], kf[2];
      unsigned vh[2][2][2];
      auto pack_kv = [&](int t) {
        kf[t] = asfrag(pk(ac[2][t][0], ac[2][t][1]), pk(ac[2][t][2], ac[2][t][3]), pk(ac[3][t][0], ac[3][t][1]), pk(ac[3][t][2], ac[3][t][3]));
#pragma unroll
        for (int c = 0; c < 2; ++c) {
          vh[t][c][0] = pk(ac[4 + c][t][0], ac[4 + c][t][1]);
          vh[t][c][1] = pk(ac[4 + c][t][2], ac[4 + c][t][3]);
        }
      };
      auto pack_q = [&](int t) {
        qf[t] = asfrag(pk(ac[0][t][0], ac[0][t][1]), pk(ac[0][t][2], ac[0][t][3]), pm & pk(ac[1][t][0], ac[1][t][1]), pm & pk(ac[1][t][2], ac[1][t][3]));
        asm volatile("" : "+v"(qf[t]));
      };
      float gv[2], og1[2];
      bf16x8 kp;
      v4u vp;
      rep<0, 18>([&](auto I_) { rd(I_, IC<0>{}); });
      rep<0, 14>([&](auto HB_) {
        constexpr int HB = decltype(HB_)::value, hs = HB & 1;
        if constexpr (hs == 0 && (HB > 0 || !FINE0)) wait_l();
        rep<0, 15>([&](auto I_) {
          constexpr int I = decltype(I_)::value;
          constexpr int c = HB < 13 ? I >> 1 : (I == 0 ? 7 : (I < 5 ? 1 + I : (I < 7 ? 6 : (I < 11 ? (I - 7) & 1 : I - 9))));
          constexpr int t = HB < 13 ? I & 1 : (I < 5 ? 1 : (I < 7 ? I - 5 : (I < 11 ? 1 - ((I - 7) >> 1) : 0)));
          constexpr int E = q_at(HB, I, FFN);
          d_m0(IC<E>{});
          if constexpr (c == 7) {
            if constexpr (HB == 0) mma0(ah, cat(hl[hs], hh[hs]), hf[hs]);
            else mma(ah, cat(hl[hs], hh[hs]), hf[hs]);
          } else {
            const bf16x8 wf = c < 6 ? cat(wl[hs][c < 6 ? c : 0], wh[hs][c < 6 ? c : 0]) : gf[hs];
            if constexpr (HB == 0) {
              if constexpr (c == 4 || c == 5) mma0(ac[c][t], xf[hs][t], wf);
              else mma0(ac[c][t], wf, xf[hs][t]);
            } else {
              if constexpr (c == 4 || c == 5) mma(ac[c][t], xf[hs][t], wf);
              else mma(ac[c][t], wf, xf[hs][t]);
            }
          }
          SCHED();
          if constexpr (HB < 13) {
            if constexpr (I < 7) { rd(IC<2 * I>{}, IC<HB + 1>{}); rd(IC<2 * I + 1>{}, IC<HB + 1>{}); }
            else rd(IC<I + 7>{}, IC<HB + 1>{});
            if constexpr (I == 12 && HB < 7) setup2(HB_, IC<1>{});
            if constexpr (I == 12 && HB >= 7 && HB < 11) setupq(IC<HB - 7>{});
            if constexpr (I == 14 && HB == TOUCH2_HB && TOUCH) { if (w >= 6) do_touch(); }
          } else {
            if constexpr (I < 8) {
              b_rd(IC<(I >> 2)>{}, IC<(I & 3)>{}, IC<0>{});
              b_rd(IC<(I >> 2)>{}, IC<(I & 3)>{}, IC<1>{});
            }
            if constexpr (I == 5) {
              *(LAS v2u*)(L + hwa) = v2u{pk(ah[0], ah[1]), pk(ah[2], ah[3])};
            }
            if constexpr (I == 8) {
              pack_kv(1);
              *(LAS bf16x8*)(L + nwa) = kf[1];
              *(LAS v4u*)(L + nwa + 1024u) = v4u{vh[1][0][0], vh[1][0][1], vh[1][1][0], vh[1][1][1]};
            }
            if constexpr (I == 10) {
#pragma unroll
              for (int t2 = 0; t2 < 2; ++t2) {
                gv[t2] = rbf(__builtin_amdgcn_rcpf(1.0f + __builtin_amdgcn_exp2f(rbf(ac[6][t2][0]) * -1.4426950408889634f)));
                og1[t2] = fmaxf(rbf(1.0f - gv[t2]), 9.094947017729282e-13f);
                keep(ac[6][t2]);
                asm volatile("" : "+v"(gv[t2]));
              }
            }
            if constexpr (I == 11) {
              wait_l();
              bar();
              kp = lds128(L, wreg + 4096u + lane * 16u);
              if (tq == 0) vp = __builtin_bit_cast(v4u, cat(ldstr(L, wreg + 5120u + lane * 8u), ldstr(L, wreg + 5632u + lane * 8u)));
              else vp = *(LAS v4u*)(L + wreg + 5120u + lane * 16u);
              pack_q(1);
            }
            if constexpr (I == 13) pack_q(0);
          }
          SCHED();
          d_go(IC<E>{});
        });
        if constexpr (hs == 0) mid(IC<(HB >> 1)>{});
      });
      STAMP(2);
      auto slip = [&](auto I_) {
        if constexpr (decltype(I_)::value < NA) {
          SCHED();
          r_dma(I_);
          SCHED();
        }
      };
      const f32x4 z = {0.f, 0.f, 0.f, 0.f};
      f32x4 s0[2], s1[2];
      s1[1] = mfma(kf[1], qf[1], z);
      s0[0] = mfma(kp, qf[0], z);
      SCHED();
      asm volatile("s_nop 4");
      SCHED();
      pack_kv(0);
      s0[1] = mfma(kf[0], qf[1], z);
      s1[0] = mfma(kf[0], qf[0], z);
      slip(IC<0>{});
      const bool odd = li & 1;
      const unsigned psh = odd ? 16u : 0u;
      float sv[2][4];
      auto pick = [&](f32x4 v, int u) {
        float lo = v[2 * u], hi = v[2 * u + 1];
        asm("" : "+v"(hi));
        return odd ? hi : lo;
      };
      const float SC = 0.18898223650461362f * 1.4426950408889634f;
#pragma unroll
      for (int u = 0; u < 2; ++u) {
        sv[1][2 + u] = __builtin_fmaf(pick(s1[1], u), SC, mb[1][2 + u]);
        sv[0][u] = __builtin_fmaf(pick(s0[0], u), SC, mb[0][u]);
      }
#pragma unroll
      for (int u = 0; u < 2; ++u) {
        sv[1][u] = __builtin_fmaf(pick(s0[1], u), SC, mb[1][u]);
        sv[0][2 + u] = __builtin_fmaf(pick(s1[0], u), SC, mb[0][2 + u]);
      }
      slip(IC<1>{});
      float m[2];
      {
        const float m0 = fmaxf(fmaxf(sv[0][0], sv[0][1]), fmaxf(sv[0][2], sv[0][3])), m1 = fmaxf(fmaxf(sv[1][0], sv[1][1]), fmaxf(sv[1][2], sv[1][3]));
        auto a = __builtin_amdgcn_permlane32_swap(__float_as_uint(m0), __float_as_uint(m1), false, false);
        const float x = fmx(__uint_as_float(a[0]), __uint_as_float(a[1]));
        auto b2 = __builtin_amdgcn_permlane16_swap(__float_as_uint(x), __float_as_uint(x), false, false);
        const float y = fmx(__uint_as_float(b2[0]), __uint_as_float(b2[1]));
        auto c2 = __builtin_amdgcn_permlane32_swap(__float_as_uint(y), __float_as_uint(y), false, false);
        m[0] = __uint_as_float(c2[0]);
        m[1] = __uint_as_float(c2[1]);
      }
      slip(IC<2>{});
      const bf16x8 ones = asfrag(0x3f803f80u, 0x3f803f80u, 0x3f803f80u, 0x3f803f80u);
      f32x4 o[2][2], sm[2];
#pragma unroll
      for (int t = 0; t < 2; ++t) {
        unsigned pw[4];
#pragma unroll
        for (int j = 0; j < 4; ++j) pw[j] = pk(__builtin_amdgcn_exp2f(sv[t][j] - m[t]), 0.f) << psh;
        const bf16x8 pf = asfrag(pw[0], pw[1], pw[2], pw[3]);
        sm[t] = mfma(ones, pf, z);
#pragma unroll
        for (int c = 0; c < 2; ++c) {
          const bf16x8 vf = t == 0 ? asfrag(vp[2 * c], vp[2 * c + 1], vh[0][c][0], vh[0][c][1]) : asfrag(vh[0][c][0], vh[0][c][1], vh[1][c][0], vh[1][c][1]);
          o[t][c] = mfma(vf, pf, z);
        }
      }
      slip(IC<3>{});
      v4u ov[2];
#pragma unroll
      for (int t = 0; t < 2; ++t) {
        const float sc = RATIO ? gv[t] * __builtin_amdgcn_rcpf(sm[t][0] * og1[t]) : gv[t] * __builtin_amdgcn_rcpf(sm[t][0]);
        const f32x4 o0 = o[t][0] * sc, o1 = o[t][1] * sc;
        ov[t] = v4u{pk(o0[0], o0[1]), pk(o0[2], o0[3]), pm & pk(o1[0], o1[1]), pm & pk(o1[2], o1[3])};
      }
      SCHED();
      st128(ov[0], rws, lane * 16u, oa);
      st128(ov[1], rws, lane * 16u, oa + A2_TT);
      if (hd == 0 && g < 2) __builtin_amdgcn_raw_buffer_store_b32(__float_as_uint(RATIO ? (g ? og1[1] : og1[0]) : (g ? gv[1] : gv[0])), rws, lane * 4u, WS_G + hp * 8192u + (128 * rb + 32 * tq) * 4u, 16);
    }
  };
  if (ffn) phase1(IC<1>{});
  else if (qkv) phase1(IC<0>{});
  else {
    SCHED();
    wait_args(px, pw, pv, tptr);
    SCHED();
    prep_touch();
    if (TOUCH) do_touch();
    rep<0, 7>([&](auto K_) { setup2(K_, IC<0>{}); });
    for (int i = 0; i < FREE_SLEEP; ++i) __builtin_amdgcn_s_sleep(16);
    rep<0, (RUNA ? 14 : 16)>(b_dma);
    wait_v<0>();
    rep<0, 16>([&](auto I_) { constexpr int I = decltype(I_)::value; b_rd(IC<(I >> 3)>{}, IC<((I >> 1) & 3)>{}, IC<(I & 1)>{}); });
    fo_pub = WS_F + 15 * 64u;
  }
  STAMP(3);
  ct = __builtin_amdgcn_readfirstlane(ct);
  tt0 = __builtin_amdgcn_readfirstlane(tt0);
  const u64 tag = (gm_dispatch_id() + 1) * 0x9E3779B97F4A7C15ull ^ (u64)__builtin_amdgcn_queue_ptr();
  unsigned va[4];
#pragma unroll
  for (int t = 0; t < 4; ++t) va[t] = opaque(lane * 16u + t * A2_TT);
  const unsigned aob = tt0 * A2_TT + (16u + 7u * w) * 1024u, aoa = tt0 * A2_TT + 2u * w * 1024u;
  const unsigned frow = WS_F + (tt0 >> 3) * 1024u, frows = (tt0 & 7) == 7 ? 1u : 0u;
  auto by_variant = [&](auto&& f) {
    const unsigned v = VROT ? slot & 3 : 0u;
    if (v == 0) f(IC<0>{});
    else if (v == 1) f(IC<1>{});
    else if (v == 2) f(IC<2>{});
    else f(IC<3>{});
  };
  v4u b2v;
  v2u fh = {(unsigned)tag, (unsigned)(tag >> 32)};
  const bool fhme = li >= ((7 * w) >> 3) && li <= ((7 * w + 6) >> 3) && g <= frows;
  SCHED();
  if (!qkv) {
    wait_l();
    bar();
    if (!ffn) rep<16, 16 + NE>(b_dma);
    rep<16 + NE, 16 + NE + NMF>(b_dma);
    wait_v<NMF>();
  } else {
    if (QPEEK) { if (fhme) ld64(fh, lane * 64u, rws, frow); }
    rep<NA, NA + NMQ>(r_dma);
    wait_v<NMQ>();
  }
  auto rd_step = [&](auto J_) { rep<0, 8>([&](auto I_) { constexpr int I = decltype(I_)::value; b_rd(J_, IC<(I >> 1)>{}, IC<(I & 1)>{}); }); };
  rd_step(IC<2>{});
  rd_step(IC<3>{});
  SCHED();
  STAMP(4);
  bar();
  if (tid < 4) {
    const v2q tv = {tag, tag};
    st128(__builtin_bit_cast(v4u, tv), rws, lane * 16u, fo_pub);
  }
  const unsigned oct = (li & 4) | ((li & 1) << 1) | ((li >> 1) & 1);
  ld128<0>(b2v, oct * 16u, mk(p.b2, D * 2), ct * 128u);
  if (!qkv) {
    wait_l();
    rep<16 + NE + NMF, 36 - INL4>(b_dma);
  } else {
    rep<NA + NMQ, 8>(r_dma);
  }
  STAMP(5);
  if (!qkv || !QPEEK) {
    if (fhme) ld64(fh, lane * 64u, rws, frow);
    wait_v<0>();
  }
  wait_l();

  auto poll = [&](bool todo) {
#pragma unroll 1
    for (unsigned spin = 0; spin < SPIN_CAP; ++spin) {
      if (todo) {
        const v2u v = __builtin_amdgcn_raw_buffer_load_b64(rws, lane * 64u, frow, POLL_AUX);
        todo = __builtin_bit_cast(u64, v) != tag;
      }
      asm volatile("" ::: "memory");
      if (!__any(todo)) break;
      __builtin_amdgcn_s_sleep(POLL_SLEEP);
      if (!ffn && !qkv) __builtin_amdgcn_s_sleep(12);
    }
  };
  asm volatile("" : "+v"(fh));
  if (FSLEEP && !ffn && !qkv) { for (int i = 0; i < FSLEEP; ++i) __builtin_amdgcn_s_sleep(16); }
  {
    const bool todo = __builtin_bit_cast(u64, fh) != tag;
    if (__any(todo)) poll(todo);
  }
  STAMP(6);
  unsigned ra[2][2], oo = 0, ga2 = 0;
  bf16x8 sel[4];
  f32x4 blo, bhi;
  float gl;
  auto final_setup = [&](auto K_) {
    constexpr int K = decltype(K_)::value;
    if constexpr (K < 2) {
#pragma unroll
      for (int e = 0; e < 2; ++e)
        ra[K][e] = opaque((q4 + 4u * e) * REG + (w >> 1) * 2048u + ((g >> 1) * 32u + (p4 | ((((K | ((w & 1u) << 1)) + q4) & 3u) << 2))) * 16u + (g & 1) * 8u);
    } else if constexpr (K == 2) {
      const unsigned tk = 8 * w + 2 * g + (li >> 3);
      oo = opaque((16 * tt0 + tk) * 896u + ct * 128u + oct * 16u);
      ga2 = opaque(SPARE + tk * 4u);
    } else if constexpr (K == 4 || K == 5) {
#pragma unroll
      for (int u = 2 * (K - 4); u < 2 * (K - 4) + 2; ++u) {
        const unsigned one = li == 4 * u + g ? 0x3f803f80u : 0u;
        sel[u] = asfrag(one, one, one, one);
        asm volatile("" : "+v"(sel[u]));
      }
    } else if constexpr (K == 6) {
      asm volatile("" : "+v"(b2v));
      blo = f32x4{bflo(b2v[0]), bfhi(b2v[0]), bflo(b2v[1]), bfhi(b2v[1])};
      bhi = f32x4{bflo(b2v[2]), bfhi(b2v[2]), bflo(b2v[3]), bfhi(b2v[3])};
      asm volatile("" : "+v"(blo), "+v"(bhi));
    }
  };
  auto phase2 = [&](auto NT_) {
    constexpr int NT = decltype(NT_)::value;
    constexpr int INL = NT == 3 ? 12 : INL4;
    v4u af[WIN][NT], aa[2][NT];
    float og[4];
    v2u fl = {(unsigned)tag, (unsigned)(tag >> 32)};
    const bool fme = li == 7 + w && g <= frows;
    auto a_load = [&](auto J_, auto T_) {
      constexpr int j = decltype(J_)::value, t = decltype(T_)::value;
      if constexpr (j < 7) ld128<0>(af[j % WIN][t], va[t], rws, aob + jr(j) * 1024u);
      else ld128<(j - 7) * 1024u>(aa[j - 7][t], va[t], rws, aoa);
    };
    if (PRIO) __builtin_amdgcn_s_setprio(3);
    rep<0, NT * WIN>([&](auto I_) { constexpr int I = decltype(I_)::value; a_load(IC<I / NT>{}, IC<I % NT>{}); });
    f32x4 acc[4][NT];
    unsigned pa = 0;
    auto put = [&](int t) {
#pragma unroll
      for (int cp = 0; cp < 2; ++cp) {
        const f32x4 x = acc[2 * cp][t], y = acc[2 * cp + 1][t];
        *(LAS v4u*)(L + pa + (t * 2 + cp) * 1024u) = v4u{pk(x[0], x[1]), pk(x[2], x[3]), pk(y[0], y[1]), pk(y[2], y[3])};
      }
    };
    rep<0, 7>([&](auto J_) {
      constexpr int j = decltype(J_)::value;
      if (PRIO && j == 2) __builtin_amdgcn_s_setprio(2);
      if (PRIO && j == 4) __builtin_amdgcn_s_setprio(1);
      if (PRIO && j == 6) __builtin_amdgcn_s_setprio(0);
      if constexpr (j == 5) {
        wait_for<p2_before(5, 0, NT, INL) - p2_before(3, 1, NT, INL)>(fl);
        const bool todo = __builtin_bit_cast(u64, fl) != tag;
        if (__any(todo)) poll(todo);
        SCHED();
      }
      if constexpr (j == 6) pa = opaque(wreg + (g * 16u + (((li >> 1) & 3u) | (((((li & 1u) | ((li >> 3) << 1)) + w) & 3u) << 2))) * 16u);
      if constexpr (j >= 4) wait_l();
      rep<0, NT>([&](auto T_) {
        constexpr int t = decltype(T_)::value, U = p2_unit(j, t, NT, INL);
        if constexpr (U >= 0) b_m0(IC<U>{});
        wait_v<p2_before(j, t, NT, INL) - p2_pos(j, t, NT, INL) - 1>();
        const bf16x8 a = __builtin_bit_cast(bf16x8, af[j % WIN][t]);
#pragma unroll
        for (int c = 0; c < 4; ++c) {
          if constexpr (j == 0) mma0(acc[c][t], cat(bl[0][c], bh[0][c]), a);
          else mma(acc[c][t], cat(bl[j & 3][c], bh[j & 3][c]), a);
        }
        SCHED();
        if constexpr (U >= 0) b_go(IC<U>{});
        if constexpr (j < 6) a_load(IC<j + 2>{}, T_);
        if constexpr (j == 5) a_load(IC<8>{}, T_);
        if constexpr (j == 5 && t == 0) ld32<0>(gl, (RATIO ? 8 * w + 2 * g + (li >> 3) : lane) * 4u, rws, WS_G + w * 8192u + tt0 * 64u);
        if constexpr (j == 3 && t == 0) { if (fme) ld64(fl, lane * 64u, rws, frow); }
        if constexpr (j >= 2) {
          b_rd(IC<j + 2>{}, T_, IC<0>{});
          b_rd(IC<j + 2>{}, T_, IC<1>{});
          if constexpr (NT == 3 && t == 2) {
            b_rd(IC<j + 2>{}, IC<3>{}, IC<0>{});
            b_rd(IC<j + 2>{}, IC<3>{}, IC<1>{});
          }
        }
        if constexpr (j == 6) final_setup(T_);
        SCHED();
      });
      if constexpr (j == 0) STAMP(16);
      if constexpr (j == 2) STAMP(17);
      if constexpr (j == 4) STAMP(18);
      if constexpr (j == 6) STAMP(19);
    });
    constexpr int NALL = p2_before(6, 0, NT, INL);
    wait_l();
    STAMP(27);
    rep<0, NT>([&](auto T_) {
      constexpr int t = decltype(T_)::value;
#pragma unroll
      for (int c = 0; c < 4; ++c) { if (!RATIO) acc[c][t] *= og[t]; }
      SCHED();
      if constexpr (t >= 1 && !RATIO) put(t - 1);
      SCHED();
      wait_v<NALL - p2_pos(8, t, NT, INL) - 1>();
      asm volatile("s_nop 0");
#pragma unroll
      for (int c = 0; c < 4; ++c) mma(acc[c][t], cat(bl[3][c], bh[3][c]), __builtin_bit_cast(bf16x8, aa[0][t]));
#pragma unroll
      for (int c = 0; c < 4; ++c) mma(acc[c][t], cat(bl[0][c], bh[0][c]), __builtin_bit_cast(bf16x8, aa[1][t]));
      SCHED();
      if constexpr (t >= 1 && RATIO) put(t - 1);
      final_setup(IC<4 + t>{});
      SCHED();
    });
    STAMP(7);
    asm volatile("s_nop 7" ::: "memory");
    SCHED();
    put(NT - 1);
  };
  if (qkv) phase2(IC<3>{});
  else phase2(IC<4>{});
  wait_l();
  bar();
  STAMP(8);
  if (!qkv || w < 6) {
    asm volatile("" : "+v"(gl));
    const float og = RATIO ? 1.0f : *(LAS float*)(L + ga2);
    bf16x8 pf[2][4];
#pragma unroll
    for (int u = 0; u < 4; ++u)
#pragma unroll
      for (int a = 0; a < 2; ++a) pf[a][u] = cat(ldstr(L, ra[u >> 1][0] + (u & 1) * 1024u + a * 256u), ldstr(L, ra[u >> 1][1] + (u & 1) * 1024u + a * 256u));
    f32x4 alo = blo * og, ahi = bhi * og;
#pragma unroll
    for (int u = 0; u < 4; ++u) {
      alo = mfma(pf[0][u], sel[u], alo);
      ahi = mfma(pf[1][u], sel[u], ahi);
    }
    if (RATIO) {
      alo *= gl;
      ahi *= gl;
    }
    const v4u o = {pk(alo[0], alo[1]), pk(alo[2], alo[3]), pk(ahi[0], ahi[1]), pk(ahi[2], ahi[3])};
    *(GAS v4u*)((char*)p.out + oo) = o;
  }
  STAMP(9);
  STAMP_FLUSH();
}
}

extern "C" void kernel_launch(void* const* d_in, const int* in_sizes, int n_in, void* d_out, int out_size, void* d_ws, size_t ws_size,
                              hipStream_t stream) {
  gm::Params p;
  p.x = d_in[0];
  p.wqkv = d_in[1];
  p.wo = d_in[2];
  p.w1 = d_in[4];
  p.b1 = d_in[5];
  p.w2 = d_in[6];
  p.b2 = d_in[7];
  p.gw = d_in[8];
  p.out = d_out;
  p.ws = d_ws;
  gm::gm_kernel<<<256, 512, 0, stream>>>(p);
}
```

```cpp
#include <hip/hip_runtime.h>
#include <stdint.h>

typedef short bf16x8 __attribute__((ext_vector_type(8)));
typedef short s16x4 __attribute__((ext_vector_type(4)));
typedef float f32x4 __attribute__((ext_vector_type(4)));
typedef unsigned v4u __attribute__((ext_vector_type(4)));
typedef unsigned v2u __attribute__((ext_vector_type(2)));
typedef __bf16 bf2_t __attribute__((ext_vector_type(2)));
typedef float f32x2 __attribute__((ext_vector_type(2)));
typedef unsigned long long u64;
typedef u64 v2q __attribute__((ext_vector_type(2)));
typedef __amdgpu_buffer_rsrc_t rsrc_t;
#define LAS __attribute__((address_space(3)))
#define GAS __attribute__((address_space(1)))
#define DI __device__ __forceinline__

#define CODE_KB 23
#define NMID 4
#define RATIO 0
#define FREE_SLEEP 0

extern "C" __device__ u64 gm_dispatch_id() __asm("llvm.amdgcn.dispatch.id");

namespace gm {
constexpr int S = 2048, D = 448, F = 1792, NQKV = 1344;
constexpr int KS_A = 16, KS = 72;
constexpr unsigned A2_TT = KS * 1024;
constexpr unsigned WS_G = 128 * A2_TT, WS_F = WS_G + 8 * 8192, WS_END = WS_F + 16 * 1024;
constexpr unsigned REG = 20480, STG = 6144, SPARE = 3 * STG;
constexpr int LDS_BYTES = 163840;
constexpr unsigned GW_OFF = 2 * REG + 5120;
constexpr unsigned SPIN_CAP = 1u << 16;
constexpr int TOUCH2_HB = 3;

struct Params {
  const void *x, *wqkv, *wo, *w1, *b1, *w2, *b2, *gw;
  void *out, *ws;
};
template <int N> struct IC { static constexpr int value = N; };
template <int I, int N, class Fn> DI void rep(Fn&& f) {
  if constexpr (I < N) {
    f(IC<I>{});
    rep<I + 1, N>(f);
  }
}

#define SCHED() __builtin_amdgcn_sched_barrier(0)
#define NOP1() asm volatile("s_nop 0")
DI rsrc_t mk(const void* p, unsigned n) { return __builtin_amdgcn_make_buffer_rsrc((void*)p, 0, n, 0x00020000); }
template <unsigned C> DI void set_m0(unsigned base) { asm volatile("s_add_u32 m0, %0, %1" ::"s"(base), "i"(C) : "scc", "memory"); }
template <unsigned IMM> DI void dma_go(unsigned voff, rsrc_t rs, unsigned soff) {
  asm volatile("buffer_load_dwordx4 %0, %1, %2 offen offset:%3 lds" ::"v"(voff), "s"(rs), "s"(soff), "i"(IMM) : "memory");
}
template <unsigned IMM> DI void dma_go0(unsigned voff, rsrc_t rs) {
  asm volatile("buffer_load_dwordx4 %0, %1, 0 offen offset:%2 lds" ::"v"(voff), "s"(rs), "i"(IMM) : "memory");
}
DI void touch(unsigned lds, unsigned voff, u64 base) {
  asm volatile("s_mov_b32 m0, %0\n\ts_nop 0\n\tglobal_load_lds_dword %1, %2" ::"s"(lds), "v"(voff), "s"(base) : "memory");
}
template <unsigned IMM> DI void ld128(v4u& d, unsigned voff, rsrc_t rs, unsigned soff) {
  asm volatile("buffer_load_dwordx4 %0, %1, %2, %3 offen offset:%4 sc1" : "=v"(d) : "v"(voff), "s"(rs), "s"(soff), "i"(IMM) : "memory");
}
DI void ld32(float& d, unsigned voff, rsrc_t rs, unsigned soff) {
  asm volatile("buffer_load_dword %0, %1, %2, %3 offen sc1" : "=v"(d) : "v"(voff), "s"(rs), "s"(soff) : "memory");
}
DI void ld64(v2u& d, unsigned voff, rsrc_t rs, unsigned soff) {
  asm volatile("buffer_load_dwordx2 %0, %1, %2, %3 offen sc1" : "+v"(d) : "v"(voff), "s"(rs), "s"(soff) : "memory");
}
DI void st128(v4u v, rsrc_t rs, unsigned voff, unsigned soff) {
  __builtin_amdgcn_raw_buffer_store_b128(v, rs, voff, soff, 16);
  SCHED();
  asm volatile("s_nop 1");
  SCHED();
}
template <int N> DI void wait_v() { asm volatile("s_waitcnt vmcnt(%0)" ::"i"(N) : "memory"); }
template <int N, class T> DI void wait_for(T& v) { asm volatile("s_waitcnt vmcnt(%1)" : "+v"(v) : "i"(N) : "memory"); }
DI void wait_l() {
  SCHED();
  __builtin_amdgcn_s_waitcnt(0xC07F);
  asm volatile("" ::: "memory");
  SCHED();
}
DI void bar() { asm volatile("s_barrier" ::: "memory"); }
DI f32x4 mfma(bf16x8 a, bf16x8 b, f32x4 c) { return __builtin_amdgcn_mfma_f32_16x16x32_bf16(a, b, c, 0, 0, 0); }
DI void mma(f32x4& c, bf16x8 a, bf16x8 b) { asm volatile("v_mfma_f32_16x16x32_bf16 %0, %1, %2, %0" : "+v"(c) : "v"(a), "v"(b)); }
DI void mma0(f32x4& c, bf16x8 a, bf16x8 b) { asm volatile("v_mfma_f32_16x16x32_bf16 %0, %1, %2, 0" : "=&v"(c) : "v"(a), "v"(b)); }
DI void mmac(f32x4& d, bf16x8 a, bf16x8 b, f32x4 c) { asm volatile("v_mfma_f32_16x16x32_bf16 %0, %1, %2, %3" : "=&v"(d) : "v"(a), "v"(b), "v"(c)); }
DI float relu(float v) {
  float r;
  asm("v_max_f32 %0, 0, %1" : "=v"(r) : "v"(v));
  return r;
}
DI float fmx(float a, float b) {
  float r;
  asm("v_max_f32 %0, %1, %2" : "=v"(r) : "v"(a), "v"(b));
  return r;
}
DI unsigned pk(float lo, float hi) {
  f32x2 v = {lo, hi};
  return __builtin_bit_cast(unsigned, __builtin_convertvector(v, bf2_t));
}
DI float rbf(float f) { return __uint_as_float(pk(f, 0.f) << 16); }
DI float bflo(unsigned u) { return __uint_as_float(u << 16); }
DI float bfhi(unsigned u) { return __uint_as_float(u & 0xffff0000u); }
DI bf16x8 asfrag(unsigned a, unsigned b, unsigned c, unsigned d) {
  v4u v = {a, b, c, d};
  return __builtin_bit_cast(bf16x8, v);
}
DI bf16x8 cat(s16x4 lo, s16x4 hi) { return __builtin_shufflevector(lo, hi, 0, 1, 2, 3, 4, 5, 6, 7); }
DI bf16x8 lds128(LAS char* L, unsigned a) { return *(LAS bf16x8*)(L + a); }
DI s16x4 ldstr(LAS char* L, unsigned a) { return __builtin_amdgcn_ds_read_tr16_b64_v4i16((LAS s16x4*)(L + a)); }
DI unsigned opaque(unsigned v) {
  asm volatile("" : "+v"(v));
  return v;
}
DI unsigned sopaque(unsigned v) {
  asm volatile("" : "+s"(v));
  return v;
}
template <class T> DI void pin(T& v) { asm volatile("" : "+v"(v)); }
DI void keep(f32x4 v) { asm volatile("" ::"v"(v)); }

#define STAMP(k)
#define FSTAMP(k)

constexpr unsigned hloc(int h) {
  h = h >= 10 ? h - 10 : h;
  return h == 0 ? SPARE : (h < 4 ? STG + (h - 1) * 2048u : (h < 7 ? 2 * STG + (h - 4) * 2048u : (h - 7) * 2048u));
}
constexpr unsigned ppos(int i) { return i < 2 ? i + 1 : (i == 2 ? 0 : i); }

constexpr int q_item(int i, bool ffn) { return i < 36 ? 8 * (1 + i / 6) + (ffn ? i % 6 : (i % 6 + 5) % 6) : 100 + (i - 36); }
constexpr int q_from(int item) {
  if (item < 100) return (item >> 3) <= 2 ? 0 : 2 * ((item >> 3) - 3) + 1;
  const int h = (item - 100) >> 1;
  return h == 0 ? 1 : (h < 4 ? 9 : (h < 7 ? 11 : 13));
}
constexpr int q_start(int hb) {
  int i = 6;
  for (int h = 0; h < hb; ++h)
    for (int k = 0; k < 4; ++k)
      if (i < 52 && q_from(q_item(i, true)) <= h) ++i;
  return i;
}
constexpr int q_at(int hb, int i, bool ffn) {
  return (i & 3) == 1 && q_start(hb) + (i >> 2) < q_start(hb + 1) ? q_item(q_start(hb) + (i >> 2), ffn) : -1;
}
constexpr int q_count(int from, int to, bool ffn) {
  int n = 0;
  for (int i = from; i < to; ++i) n += (ffn || i >= 36 || i % 6 != 0) ? 1 : 0;
  return n;
}
constexpr int q_wait(int s, bool ffn) { return q_count(s < 6 ? 6 * (s + 1) : 44, q_start(2 * s + 1), ffn); }
static_assert(q_start(14) == 52 && q_start(3) >= 12 && q_start(5) >= 18 && q_start(7) >= 24 && q_start(9) >= 30 && q_start(11) >= 36 &&
                  q_start(13) >= 44 && q_start(13) - q_start(12) <= 2 && q_start(14) - q_start(13) <= 2,
              "queue order");
constexpr int r_unit(int i) { return i < 2 ? 16 + i : (i < 6 ? 18 + i : 12 + i); }

constexpr int p2_unit(int j, int t, int nt) { return nt == 3 && 3 * j + t < 12 ? 24 + 3 * j + t : -1; }
constexpr int p2_after(int j, int t, int nt) {
  return (p2_unit(j, t, nt) >= 0 ? 1 : 0) + (j < 5 ? 1 : (j == 5 ? 2 : 0))
         + (j == 5 && t == 0 ? 1 : 0)
         + (j == 3 && t == 0 ? 1 : 0);
}
constexpr int p2_before(int j, int t, int nt) {
  int n = 2 * nt;
  for (int i = 0; i < nt * j + t; ++i) n += p2_after(i / nt, i % nt, nt);
  return n;
}
constexpr int p2_pos(int j, int t, int nt) {
  return j < 2 ? nt * j + t : (j == 8 ? p2_before(5, t, nt) + 1 : p2_before(j - 2, t, nt) + (p2_unit(j - 2, t, nt) >= 0 ? 1 : 0));
}

__global__ __launch_bounds__(512, 2) void gm_kernel(Params p) {
  __shared__ __attribute__((aligned(1024))) char smem[LDS_BYTES];
  LAS char* L = (LAS char*)smem;
  const unsigned tid = threadIdx.x, lane = tid & 63;
  const unsigned b = blockIdx.x, xcd = b & 7, slot = b >> 3;
  const bool ffn = b < 112, qkv = b >= 128;
  const unsigned w = __builtin_amdgcn_readfirstlane(tid >> 6);
  u64 px, pw, pv, pt;
  asm volatile("s_load_dwordx2 %0, %4, 0x0\n\ts_load_dwordx2 %1, %4, %5\n\ts_load_dwordx2 %2, %4, %6\n\ts_load_dwordx2 %3, %4, %7"
               : "=&s"(px), "=&s"(pw), "=&s"(pv), "=&s"(pt)
               : "s"(__builtin_amdgcn_kernarg_segment_ptr()), "s"(qkv ? 8u : 24u), "s"(qkv ? 56u : 32u), "s"(((0x25130130u >> (4 * w)) & 7u) * 8u));
  if (tid < CODE_KB) {
    u64 pc;
    asm volatile("s_getpc_b64 %0" : "=s"(pc));
    touch(SPARE + 1024u, ((slot & 7) * CODE_KB + tid) * 128u, pc);
  }
  const unsigned wreg = w * REG;
  const unsigned li = lane & 15, g = lane >> 4, rr = lane >> 3, pos = lane & 7;
  const unsigned rq = xcd >> 1, side = xcd & 1;
  const unsigned tcnt = (unsigned)(0x0C31151C38101520ull >> (8 * w)) & 0xffu;
  const unsigned tfirst = ((unsigned)(0x00003F5400000000ull >> (8 * w)) & 0xffu) * 64u;
  const unsigned tsh = w ? 6u : 2u;
  const unsigned tln = opaque(((32 * xcd + slot) * (w ? tcnt : 56u) + tfirst + (lane >> tsh) * 7u + (lane & ((1u << tsh) - 1u))) * 128u);
  auto do_touch = [&]() {
    if (lane < tcnt) touch(wreg + SPARE + 1024u, tln, pt);
  };
  const bool mid3 = (slot >> 2) == 3;
  const unsigned rb = 4 * rq + (mid3 ? 2 * side + (slot & 1) : slot & 3), cs = mid3 ? 3u : side * 4 + ((slot >> 2) & 3);
  unsigned vx[2];
#pragma unroll
  for (int h = 0; h < 2; ++h) vx[h] = opaque((128 * rb + 16 * w + 8 * h + rr) * 896u + ((pos ^ (4u * h + (rr >> 1))) << 4));
  const unsigned vw = opaque((8 * w + rr) * (qkv ? 2688u : 3584u) + (min(pos ^ ((((rr >> 1) & 1) | ((w & 1) << 1)) << 1), qkv ? 6u : 7u) << 4) +
                             cs * (qkv ? 112u : 512u));
  SCHED();
  asm volatile("s_waitcnt lgkmcnt(0)" : "+s"(px), "+s"(pw), "+s"(pv), "+s"(pt)::"memory");
  SCHED();
  STAMP(10);
  const rsrc_t rx = mk((const void*)px, S * D * 2);
  const rsrc_t rw = mk((const void*)pw, qkv ? D * NQKV * 2 : D * F * 2);
  if (w < 3) do_touch();
  if (ffn || qkv) {
    const unsigned wus = qkv ? 896u : 128u;
    rep<0, 5>([&](auto I_) {
      constexpr int I = decltype(I_)::value;
      set_m0<ppos(I) * 1024u>(wreg);
      NOP1();
      if constexpr (I < 2) dma_go0<0>(vx[I], rx);
      else dma_go<0>(vw, rw, (I - 2) * wus);
    });
  }
  SCHED();

  rsrc_t rws, rw2, rwo;
  unsigned vb[2], ba[4], fo_pub, ct = 0, tt0 = 0;
  const unsigned bo2 = w * (224u * 896u), boo = w * (56u * 896u);
  const unsigned q4 = (lane >> 2) & 3, p4 = lane & 3;
  const unsigned fsw = (q4 >> 1) | ((g & 1) << 1);
  s16x4 bl[4][4], bh[4][4];
  auto b_m0 = [&](auto N_) {
    constexpr int N = decltype(N_)::value;
    set_m0<hloc(N >> 1) + (N & 1) * 1024u>(wreg);
  };
  auto b_go = [&](auto N_) {
    constexpr int N = decltype(N_)::value, j = N >> 2;
    if constexpr (j < 7) dma_go<0>(vb[N & 1], rw2, bo2 + (N >> 1) * (16u * 896u));
    else dma_go<0>(vb[N & 1], rwo, boo + ((j - 7) * 28u + ((N >> 1) & 1) * 16u) * 896u);
  };
  auto b_dma = [&](auto N_) { b_m0(N_); NOP1(); b_go(N_); };
  auto r_dma = [&](auto I_) { b_dma(IC<r_unit(decltype(I_)::value)>{}); };
  auto b_rd = [&](auto J_, auto C_, auto E_) {
    constexpr int J = decltype(J_)::value, C = decltype(C_)::value, E = decltype(E_)::value;
    if constexpr (E == 0) bl[J & 3][C] = ldstr(L, ba[C] + hloc(2 * J));
    else bh[J & 3][C] = ldstr(L, ba[C] + hloc(2 * J + 1));
  };
  auto rd_step = [&](auto J_) { rep<0, 8>([&](auto I_) { constexpr int I = decltype(I_)::value; b_rd(J_, IC<(I >> 1)>{}, IC<(I & 1)>{}); }); };
  auto setup2 = [&](auto K_, auto Q_) {
    constexpr int K = decltype(K_)::value;
    constexpr bool Q = decltype(Q_)::value;
    if constexpr (K == 0) {
      ct = Q ? (slot >> 2) - 1 : (unsigned)max((int)(slot >> 2), (int)slot - 9);
      tt0 = 16 * xcd + (Q ? 4 + 3 * (slot & 3) : (slot < 12 ? 4 * (slot & 3) : 0u));
      rws = mk(p.ws, WS_END);
      rw2 = mk(p.w2, F * D * 2);
      rwo = mk(p.wo, D * D * 2);
    } else if constexpr (K == 2) {
      vb[0] = opaque(rr * 896u + ((pos ^ (((rr >> 1) & 3) << 1)) << 4) + ct * 128u);
      vb[1] = opaque(vb[0] + 8u * 896u);
    } else if constexpr (K >= 3 && K < 7) {
      ba[K - 3] = opaque(wreg + (4 * g + q4) * 128u + ((2u * ((K - 3) ^ fsw) + (p4 >> 1)) << 4) + (p4 & 1) * 8u);
    }
  };

  auto phase1 = [&](auto FFN_) {
    constexpr bool FFN = decltype(FFN_)::value;
    constexpr unsigned LDW = FFN ? 3584u : 2688u, WUS = FFN ? 128u : 896u;
    unsigned vh5 = 0;
    if (!FFN) vh5 = opaque(((rb ? 128 * rb - 16 : 0u) + 8 * (w & 1) + rr) * 896u + ((pos ^ (4u * (w & 1) + (rr >> 1))) << 4));
    auto d_m0 = [&](auto E_) {
      constexpr int E = decltype(E_)::value, I = E & 7, ST = E >> 3;
      if constexpr (E >= 100) b_m0(IC<E - 100>{});
      else if constexpr (E >= 0) set_m0<(ST % 3) * STG + ppos(I) * 1024u - ((I < 2 || (!FFN && I == 5)) ? ST * 128u : (I - 2) * WUS)>(wreg);
    };
    auto d_go = [&](auto E_) {
      constexpr int E = decltype(E_)::value, I = E & 7, ST = E >> 3;
      if constexpr (E >= 100) b_go(IC<E - 100>{});
      else if constexpr (E >= 0) {
        if constexpr (I < 2) dma_go0<ST * 128u>(vx[I], rx);
        else if constexpr (!FFN && I == 5) { if (w < 2) dma_go0<ST * 128u>(vh5, rx); }
        else if constexpr (ST == 0) dma_go0<(I - 2) * WUS>(vw, rw);
        else dma_go<(I - 2) * WUS>(vw, rw, ST * 64u * LDW);
      }
    };
    d_m0(IC<5>{});
    NOP1();
    d_go(IC<5>{});
    if (w == 2) {
      set_m0<FFN ? SPARE : GW_OFF>(0);
      NOP1();
      dma_go0<0>(lane * 16, mk((const char*)pv + (FFN ? cs * 512u : 0u), FFN ? 512 : 896));
    }
    SCHED();
    STAMP(11);
    const unsigned xa0 = 1024u + (li >> 3) * 1024u + (li & 7) * 128u + ((g ^ ((li >> 1) & 7)) << 4);
    auto second_stage = [&]() {
      SCHED();
      STAMP(12);
      bar();
      rep<0, 6>([&](auto I_) { constexpr int E = q_item(decltype(I_)::value, FFN); d_m0(IC<E>{}); NOP1(); d_go(IC<E>{}); });
      STAMP(14);
      if (w >= 3 && w < 6) {
        do_touch();
        wait_v<q_count(0, 6, FFN) + 1>();
      } else {
        wait_v<q_count(0, 6, FFN)>();
      }
      bar();
      STAMP(1);
    };
    auto mid = [&](auto S_) {
      constexpr int s = decltype(S_)::value;
      constexpr bool t2 = TOUCH2_HB <= 2 * s && q_start(TOUCH2_HB + 1) >= 6 * (s + 1);
      if ((s == 0 && w >= 3 && w < 6) || (t2 && w >= 6)) wait_v<q_wait(s, FFN) + 1>();
      else wait_v<q_wait(s, FFN)>();
      wait_l();
      bar();
      FSTAMP(20 + s);
    };

    if constexpr (FFN) {
      const unsigned mh = w >> 2, nq = w & 3;
      unsigned xa[2][2], wa[2][4];
#pragma unroll
      for (int hs = 0; hs < 2; ++hs) {
#pragma unroll
        for (int t2 = 0; t2 < 2; ++t2) xa[hs][t2] = opaque((xa0 ^ (hs * 64u)) + (mh * 4u + t2 * 2u) * REG);
#pragma unroll
        for (int c = 0; c < 4; ++c)
          wa[hs][c] = opaque((4u * hs + g) * REG + (nq ? 2u + nq : 0u) * 1024u + q4 * 128u + ((2u * (c ^ fsw) + (p4 >> 1)) << 4) + (p4 & 1) * 8u);
      }
      second_stage();
      bf16x8 xf[2][4];
      s16x4 wl[2][4], wh[2][4];
      f32x4 acc[4][4];
      auto rd = [&](auto I_, auto HB_) {
        constexpr int I = decltype(I_)::value, HB = decltype(HB_)::value, hs = HB & 1;
        constexpr unsigned bo = ((HB >> 1) % 3) * STG;
        if constexpr (I >= 2 && I < 6) xf[hs][I - 2] = lds128(L, xa[hs][(I - 2) >> 1] + ((I - 2) & 1) * REG + bo);
        else if constexpr (I < 12) {
          constexpr int c = I < 2 ? 0 : (I - 4) >> 1;
          if constexpr ((I & 1) == 0) wl[hs][c] = ldstr(L, wa[hs][c] + bo);
          else wh[hs][c] = ldstr(L, wa[hs][c] + bo + 512u);
        }
      };
      f32x4 bv[4];
#pragma unroll
      for (int c = 0; c < 4; ++c) {
        const v2u bb = *(LAS v2u*)(L + SPARE + nq * 128u + c * 32u + g * 8u);
        bv[c] = f32x4{bflo(bb.x), bfhi(bb.x), bflo(bb.y), bfhi(bb.y)};
      }
      rep<0, 12>([&](auto I_) { rd(I_, IC<0>{}); });
#pragma unroll
      for (int c = 0; c < 4; ++c) pin(bv[c]);
      unsigned so = 0;
      auto epi = [&](int t, int u) {
        float hv[8];
#pragma unroll
        for (int j = 0; j < 8; ++j) {
          const float r = relu(acc[2 * u + (j >> 2)][t][j & 3]);
          hv[j] = r * r;
        }
        const v4u o = {pk(hv[0], hv[1]), pk(hv[2], hv[3]), pk(hv[4], hv[5]), pk(hv[6], hv[7])};
        st128(o, rws, lane * 16u + u * 1024u, so + t * A2_TT);
      };
      rep<0, 12>([&](auto HB_) {
        constexpr int HB = decltype(HB_)::value, hs = HB & 1;
        if constexpr (HB == 0) asm volatile("s_nop 1");
        else if constexpr (hs == 0) wait_l();
        rep<0, 16>([&](auto I_) {
          constexpr int I = decltype(I_)::value;
          constexpr int E = q_at(HB, I, FFN);
          d_m0(IC<E>{});
          if constexpr (HB == 0) mmac(acc[I >> 2][I & 3], cat(wl[hs][I >> 2], wh[hs][I >> 2]), xf[hs][I & 3], bv[I >> 2]);
          else mma(acc[I >> 2][I & 3], cat(wl[hs][I >> 2], wh[hs][I >> 2]), xf[hs][I & 3]);
          SCHED();
          rd(I_, IC<HB + 1>{});
          if constexpr (I == 14 && HB < 7) setup2(HB_, IC<0>{});
          if constexpr (I == 14 && HB == 7) {
            so = sopaque((8 * rb + 4 * mh) * A2_TT + (KS_A + 8 * cs + 2 * nq) * 1024u);
            fo_pub = sopaque(WS_F + rb * 1024u + cs * 64u);
          }
          if constexpr (I == 15 && HB == TOUCH2_HB) { if (w >= 6) do_touch(); }
          SCHED();
          d_go(IC<E>{});
        });
        if constexpr (hs == 0) mid(IC<(HB >> 1)>{});
      });
      wait_l();
      rep<0, 32>([&](auto M_) {
        constexpr int M = decltype(M_)::value, G = M >> 2, c = M & 3;
        constexpr int hs = G < 4 ? G >> 1 : G & 1, t = G < 4 ? G & 1 : G >> 1;
        constexpr int E = M < 8 ? q_at(12, M, FFN) : (M < 16 ? q_at(13, M - 8, FFN) : -1);
        d_m0(IC<E>{});
        mma(acc[c][t], cat(wl[hs][c], wh[hs][c]), xf[hs][t]);
        SCHED();
        if constexpr (M < 6) { rd(IC<2 * M>{}, IC<13>{}); rd(IC<2 * M + 1>{}, IC<13>{}); }
        if constexpr (M >= 8 && M < 14) {
          rep<0, (M < 12 ? 3 : 2)>([&](auto Q_) {
            constexpr int Q = (M < 12 ? 3 * (M - 8) : 12 + 2 * (M - 12)) + decltype(Q_)::value;
            b_rd(IC<(Q >> 3)>{}, IC<((Q >> 1) & 3)>{}, IC<(Q & 1)>{});
          });
        }
        if constexpr (M >= 14 && M < 32 && (M - 14) % 3 == 0) epi((M - 14) / 6, ((M - 14) / 3) & 1);
        SCHED();
        d_go(IC<E>{});
        if constexpr (M == 7) mid(IC<6>{});
      });
      STAMP(2);
      asm volatile("s_nop 5" ::: "memory");
      SCHED();
      epi(3, 0);
      epi(3, 1);
    } else {
      const unsigned tq = w & 3, hd = w >> 2, hp = cs;
      unsigned xa[2], wa[2][2], xh[2], wq[2];
#pragma unroll
      for (int hs = 0; hs < 2; ++hs) {
        xa[hs] = opaque((xa0 ^ (hs * 64u)) + tq * 2u * REG);
#pragma unroll
        for (int c = 0; c < 2; ++c) {
          const unsigned bo = 56u * hd + 32u * c + 8u * p4;
          wa[hs][c] = opaque((4u * hs + g) * REG + q4 * 128u + (((bo >> 4) ^ (fsw << 1)) << 4) + (bo & 8u));
        }
        xh[hs] = opaque((li >> 3) * REG + 5120u + (li & 7) * 128u + (((4u * hs + g) ^ ((li >> 1) & 7)) << 4));
        const unsigned bo = 56u * hd + 32u * (tq & 1) + 8u * p4;
        wq[hs] = opaque((4u * hs + g) * REG + q4 * 128u + (((bo >> 4) ^ (fsw << 1)) << 4) + (bo & 8u) + (3u + (tq >> 1)) * 1024u);
      }
      const unsigned ga = opaque(GW_OFF + g * 16u);
      second_stage();
      float mb[2][4];
      unsigned oa = 0, hwa = 0, nwa = 0, pm = 0;
      auto setupq = [&](auto K_) {
        constexpr int K = decltype(K_)::value, t = K & 1;
        if constexpr (K < 2) {
#pragma unroll
          for (int j = 0; j < 4; ++j) {
            const int kap = 16 * (j >> 1) + (int)(4 * g) + 2 * (j & 1) + (int)(li & 1) - 16;
            const int dl = (int)li - kap;
            const bool ok = (dl >= 0) && (dl <= 14) && (t == 1 || j >= 2 || (rb | tq) != 0);
            mb[t][j] = __uint_as_float(opaque(ok ? 0u : 0xff800000u));
          }
        } else if constexpr (K == 2) {
          oa = sopaque((8 * rb + 2 * tq) * A2_TT + (2 * hp + hd) * 1024u);
          fo_pub = sopaque(WS_F + rb * 1024u + (7 + hp) * 64u);
        } else if constexpr (K == 3) {
          hwa = opaque(hd * 4u * REG + (tq < 2 ? 4096u + lane * 16u + tq * 8u : 4096u + tq * 512u + li * 32u + g * 8u));
          nwa = opaque(wreg + (tq < 3 ? REG + 4096u : 2048u) + lane * 16u);
          pm = opaque(g == 3 ? 0u : 0xffffffffu);
        }
      };
      f32x4 ac[7][2], ah;
      bf16x8 xf[2][2], gf[2], hf[2];
      s16x4 wl[2][6], wh[2][6], hl[2], hh[2];
      auto rd = [&](auto I_, auto HB_) {
        constexpr int I = decltype(I_)::value, HB = decltype(HB_)::value, hs = HB & 1;
        constexpr unsigned bo = ((HB >> 1) % 3) * STG;
        if constexpr (I == 2 || I == 3) xf[hs][I - 2] = lds128(L, xa[hs] + (I - 2) * REG + bo);
        else if constexpr (I < 14) {
          constexpr int c = I < 2 ? 0 : (I - 2) >> 1;
          if constexpr ((I & 1) == 0) wl[hs][c] = ldstr(L, wa[hs][c & 1] + ppos(2 + (c >> 1)) * 1024u + bo);
          else wh[hs][c] = ldstr(L, wa[hs][c & 1] + ppos(2 + (c >> 1)) * 1024u + bo + 512u);
        } else if constexpr (I == 14) gf[hs] = lds128(L, ga + HB * 64u);
        else if constexpr (I == 15) hf[hs] = lds128(L, xh[hs] + bo);
        else if constexpr (I == 16) hl[hs] = ldstr(L, wq[hs] + bo);
        else if constexpr (I == 17) hh[hs] = ldstr(L, wq[hs] + bo + 512u);
      };
      bf16x8 qf[2], kf[2], kp;
      unsigned vh[2][2][2];
      float gv[2], og[2];
      v4u vp;
      auto pack_kv = [&](int t) {
        kf[t] = asfrag(pk(ac[2][t][0], ac[2][t][1]), pk(ac[2][t][2], ac[2][t][3]), pk(ac[3][t][0], ac[3][t][1]), pk(ac[3][t][2], ac[3][t][3]));
#pragma unroll
        for (int c = 0; c < 2; ++c) {
          vh[t][c][0] = pk(ac[4 + c][t][0], ac[4 + c][t][1]);
          vh[t][c][1] = pk(ac[4 + c][t][2], ac[4 + c][t][3]);
        }
      };
      auto pack_q = [&](int t) {
        qf[t] = asfrag(pk(ac[0][t][0], ac[0][t][1]), pk(ac[0][t][2], ac[0][t][3]), pm & pk(ac[1][t][0], ac[1][t][1]), pm & pk(ac[1][t][2], ac[1][t][3]));
        pin(qf[t]);
      };
      rep<0, 18>([&](auto I_) { rd(I_, IC<0>{}); });
      rep<0, 14>([&](auto HB_) {
        constexpr int HB = decltype(HB_)::value, hs = HB & 1;
        if constexpr (hs == 0 && HB > 0) wait_l();
        rep<0, 15>([&](auto I_) {
          constexpr int I = decltype(I_)::value;
          constexpr int c = HB < 13 ? I >> 1 : (I == 0 ? 7 : (I < 5 ? 1 + I : (I < 7 ? 6 : (I < 11 ? (I - 7) & 1 : I - 9))));
          constexpr int t = HB < 13 ? I & 1 : (I < 5 ? 1 : (I < 7 ? I - 5 : (I < 11 ? 1 - ((I - 7) >> 1) : 0)));
          constexpr int E = q_at(HB, I, FFN);
          d_m0(IC<E>{});
          if constexpr (c == 7) {
            if constexpr (HB == 0) mma0(ah, cat(hl[hs], hh[hs]), hf[hs]);
            else mma(ah, cat(hl[hs], hh[hs]), hf[hs]);
          } else {
            const bf16x8 wf = c < 6 ? cat(wl[hs][c < 6 ? c : 0], wh[hs][c < 6 ? c : 0]) : gf[hs];
            if constexpr (HB == 0) {
              if constexpr (c == 4 || c == 5) mma0(ac[c][t], xf[hs][t], wf);
              else mma0(ac[c][t], wf, xf[hs][t]);
            } else {
              if constexpr (c == 4 || c == 5) mma(ac[c][t], xf[hs][t], wf);
              else mma(ac[c][t], wf, xf[hs][t]);
            }
          }
          SCHED();
          if constexpr (HB < 13) {
            if constexpr (I < 7) { rd(IC<2 * I>{}, IC<HB + 1>{}); rd(IC<2 * I + 1>{}, IC<HB + 1>{}); }
            else rd(IC<I + 7>{}, IC<HB + 1>{});
            if constexpr (I == 12 && HB < 7) setup2(HB_, IC<1>{});
            if constexpr (I == 12 && HB >= 7 && HB < 11) setupq(IC<HB - 7>{});
            if constexpr (I == 14 && HB == TOUCH2_HB) { if (w >= 6) do_touch(); }
          } else {
            if constexpr (I < 8) {
              b_rd(IC<(I >> 2)>{}, IC<(I & 3)>{}, IC<0>{});
              b_rd(IC<(I >> 2)>{}, IC<(I & 3)>{}, IC<1>{});
            }
            if constexpr (I == 5) {
              *(LAS v2u*)(L + hwa) = v2u{pk(ah[0], ah[1]), pk(ah[2], ah[3])};
            }
            if constexpr (I == 8) {
              pack_kv(1);
              *(LAS bf16x8*)(L + nwa) = kf[1];
              *(LAS v4u*)(L + nwa + 1024u) = v4u{vh[1][0][0], vh[1][0][1], vh[1][1][0], vh[1][1][1]};
            }
            if constexpr (I == 10) {
#pragma unroll
              for (int t2 = 0; t2 < 2; ++t2) {
                gv[t2] = rbf(__builtin_amdgcn_rcpf(1.0f + __builtin_amdgcn_exp2f(rbf(ac[6][t2][0]) * -1.4426950408889634f)));
                og[t2] = rbf(1.0f - gv[t2]);
                if (RATIO) og[t2] = fmaxf(og[t2], 9.094947017729282e-13f);
                keep(ac[6][t2]);
                pin(gv[t2]);
                pin(og[t2]);
              }
            }
            if constexpr (I == 11) {
              wait_l();
              bar();
              kp = lds128(L, wreg + 4096u + lane * 16u);
              if (tq == 0) vp = __builtin_bit_cast(v4u, cat(ldstr(L, wreg + 5120u + lane * 8u), ldstr(L, wreg + 5632u + lane * 8u)));
              else vp = *(LAS v4u*)(L + wreg + 5120u + lane * 16u);
              pack_q(1);
            }
            if constexpr (I == 13) pack_q(0);
          }
          SCHED();
          d_go(IC<E>{});
        });
        if constexpr (hs == 0) mid(IC<(HB >> 1)>{});
      });
      STAMP(2);
      const f32x4 z = {0.f, 0.f, 0.f, 0.f};
      f32x4 s0[2], s1[2];
      s1[1] = mfma(kf[1], qf[1], z);
      s0[0] = mfma(kp, qf[0], z);
      SCHED();
      asm volatile("s_nop 4");
      SCHED();
      pack_kv(0);
      s0[1] = mfma(kf[0], qf[1], z);
      s1[0] = mfma(kf[0], qf[0], z);
      const bool odd = li & 1;
      const unsigned psh = odd ? 16u : 0u;
      float sv[2][4];
      auto pick = [&](f32x4 v, int u) {
        float lo = v[2 * u], hi = v[2 * u + 1];
        asm("" : "+v"(hi));
        return odd ? hi : lo;
      };
      const float SC = 0.18898223650461362f * 1.4426950408889634f;
#pragma unroll
      for (int u = 0; u < 2; ++u) {
        sv[1][2 + u] = __builtin_fmaf(pick(s1[1], u), SC, mb[1][2 + u]);
        sv[0][u] = __builtin_fmaf(pick(s0[0], u), SC, mb[0][u]);
      }
#pragma unroll
      for (int u = 0; u < 2; ++u) {
        sv[1][u] = __builtin_fmaf(pick(s0[1], u), SC, mb[1][u]);
        sv[0][2 + u] = __builtin_fmaf(pick(s1[0], u), SC, mb[0][2 + u]);
      }
      float m[2];
      {
        const float m0 = fmaxf(fmaxf(sv[0][0], sv[0][1]), fmaxf(sv[0][2], sv[0][3])), m1 = fmaxf(fmaxf(sv[1][0], sv[1][1]), fmaxf(sv[1][2], sv[1][3]));
        auto a = __builtin_amdgcn_permlane32_swap(__float_as_uint(m0), __float_as_uint(m1), false, false);
        const float x = fmx(__uint_as_float(a[0]), __uint_as_float(a[1]));
        auto b2 = __builtin_amdgcn_permlane16_swap(__float_as_uint(x), __float_as_uint(x), false, false);
        const float y = fmx(__uint_as_float(b2[0]), __uint_as_float(b2[1]));
        auto c2 = __builtin_amdgcn_permlane32_swap(__float_as_uint(y), __float_as_uint(y), false, false);
        m[0] = __uint_as_float(c2[0]);
        m[1] = __uint_as_float(c2[1]);
      }
      const bf16x8 ones = asfrag(0x3f803f80u, 0x3f803f80u, 0x3f803f80u, 0x3f803f80u);
      f32x4 o[2][2], sm[2];
#pragma unroll
      for (int t = 0; t < 2; ++t) {
        unsigned pw4[4];
#pragma unroll
        for (int j = 0; j < 4; ++j) pw4[j] = pk(__builtin_amdgcn_exp2f(sv[t][j] - m[t]), 0.f) << psh;
        const bf16x8 pf = asfrag(pw4[0], pw4[1], pw4[2], pw4[3]);
        sm[t] = mfma(ones, pf, z);
#pragma unroll
        for (int c = 0; c < 2; ++c) {
          const bf16x8 vf = t == 0 ? asfrag(vp[2 * c], vp[2 * c + 1], vh[0][c][0], vh[0][c][1]) : asfrag(vh[0][c][0], vh[0][c][1], vh[1][c][0], vh[1][c][1]);
          o[t][c] = mfma(vf, pf, z);
        }
      }
      v4u ov[2];
#pragma unroll
      for (int t = 0; t < 2; ++t) {
        const float sc = gv[t] * __builtin_amdgcn_rcpf(RATIO ? sm[t][0] * og[t] : sm[t][0]);
        const f32x4 o0 = o[t][0] * sc, o1 = o[t][1] * sc;
        ov[t] = v4u{pk(o0[0], o0[1]), pk(o0[2], o0[3]), pm & pk(o1[0], o1[1]), pm & pk(o1[2], o1[3])};
      }
      SCHED();
      st128(ov[0], rws, lane * 16u, oa);
      st128(ov[1], rws, lane * 16u, oa + A2_TT);
      if (hd == 0 && g < 2)
        __builtin_amdgcn_raw_buffer_store_b32(__float_as_uint(g ? og[1] : og[0]), rws, lane * 4u, WS_G + hp * 8192u + (128 * rb + 32 * tq) * 4u, 16);
    }
  };
  if (ffn) phase1(IC<1>{});
  else if (qkv) phase1(IC<0>{});
  else {
    if (w >= 3) do_touch();
    rep<0, 7>([&](auto K_) { setup2(K_, IC<0>{}); });
    for (int i = 0; i < FREE_SLEEP; ++i) __builtin_amdgcn_s_sleep(16);
    rep<0, 16>(b_dma);
    wait_v<0>();
    rd_step(IC<0>{});
    rd_step(IC<1>{});
    fo_pub = WS_F + 15 * 64u;
  }
  STAMP(3);
  ct = __builtin_amdgcn_readfirstlane(ct);
  tt0 = __builtin_amdgcn_readfirstlane(tt0);

  const u64 tag = (gm_dispatch_id() + 1) * 0x9E3779B97F4A7C15ull ^ (u64)__builtin_amdgcn_dispatch_ptr();
  unsigned va[4];
#pragma unroll
  for (int t = 0; t < 4; ++t) va[t] = opaque(lane * 16u + t * A2_TT);
  const unsigned aob = tt0 * A2_TT + (16u + 7u * w) * 1024u, aoa = tt0 * A2_TT + 2u * w * 1024u;
  const unsigned frow = WS_F + (tt0 >> 3) * 1024u, frows = (tt0 & 7) == 7 ? 1u : 0u;
  SCHED();
  if (!qkv) {
    wait_l();
    if (NMID) bar();
    rep<16, 16 + NMID>(b_dma);
  } else {
    rep<0, NMID>(r_dma);
  }
  wait_v<NMID>();
  rd_step(IC<2>{});
  rd_step(IC<3>{});
  SCHED();
  STAMP(4);
  bar();
  if (tid < 4) {
    const v2q tv = {tag, tag};
    st128(__builtin_bit_cast(v4u, tv), rws, lane * 16u, fo_pub);
  }
  const unsigned oct = (li & 4) | ((li & 1) << 1) | ((li >> 1) & 1);
  v4u b2v;
  ld128<0>(b2v, oct * 16u, mk(p.b2, D * 2), ct * 128u);
  wait_l();
  if (!qkv) rep<16 + NMID, 36>(b_dma);
  else rep<NMID, 8>(r_dma);
  STAMP(5);

  auto poll = [&](bool todo) {
#pragma unroll 1
    for (unsigned spin = 0; spin < SPIN_CAP; ++spin) {
      if (todo) {
        const v2u v = __builtin_amdgcn_raw_buffer_load_b64(rws, lane * 64u, frow, 16);
        todo = __builtin_bit_cast(u64, v) != tag;
      }
      asm volatile("" ::: "memory");
      if (!__any(todo)) break;
      __builtin_amdgcn_s_sleep(1);
      if (!ffn && !qkv) __builtin_amdgcn_s_sleep(12);
    }
  };
  poll(li >= ((7 * w) >> 3) && li <= ((7 * w + 6) >> 3) && g <= frows);
  STAMP(6);
  unsigned ra[2][2], oo = 0, ga2 = 0;
  bf16x8 sel[4];
  f32x4 blo, bhi;
  float gl;
  auto final_setup = [&](auto K_) {
    constexpr int K = decltype(K_)::value;
    if constexpr (K < 2) {
#pragma unroll
      for (int e = 0; e < 2; ++e)
        ra[K][e] = opaque((q4 + 4u * e) * REG + (w >> 1) * 2048u + ((g >> 1) * 32u + (p4 | ((((K | ((w & 1u) << 1)) + q4) & 3u) << 2))) * 16u + (g & 1) * 8u);
    } else if constexpr (K == 2) {
      const unsigned tk = 8 * w + 2 * g + (li >> 3);
      oo = opaque((16 * tt0 + tk) * 896u + ct * 128u + oct * 16u);
      if (!RATIO) ga2 = opaque(SPARE + tk * 4u);
    } else if constexpr (K == 4 || K == 5) {
#pragma unroll
      for (int u = 2 * (K - 4); u < 2 * (K - 4) + 2; ++u) {
        const unsigned one = li == 4 * u + g ? 0x3f803f80u : 0u;
        sel[u] = asfrag(one, one, one, one);
        pin(sel[u]);
      }
    } else if constexpr (K == 6) {
      pin(b2v);
      blo = f32x4{bflo(b2v[0]), bfhi(b2v[0]), bflo(b2v[1]), bfhi(b2v[1])};
      bhi = f32x4{bflo(b2v[2]), bfhi(b2v[2]), bflo(b2v[3]), bfhi(b2v[3])};
      pin(blo);
      pin(bhi);
    }
  };
  auto phase2 = [&](auto NT_) {
    constexpr int NT = decltype(NT_)::value;
    v4u af[2][NT], aa[2][NT];
    v2u fl = {(unsigned)tag, (unsigned)(tag >> 32)};
    const bool fme = li == 7 + w && g <= frows;
    auto a_load = [&](auto J_, auto T_) {
      constexpr int j = decltype(J_)::value, t = decltype(T_)::value;
      if constexpr (j < 4) ld128<j * 1024u>(af[j & 1][t], va[t], rws, aob);
      else if constexpr (j < 7) ld128<(j - 4) * 1024u>(af[j & 1][t], va[t], rws, aob + 4096u);
      else ld128<(j - 7) * 1024u>(aa[j - 7][t], va[t], rws, aoa);
    };
    __builtin_amdgcn_s_setprio(3);
    rep<0, 2 * NT>([&](auto I_) { constexpr int I = decltype(I_)::value; a_load(IC<I / NT>{}, IC<I % NT>{}); });
    f32x4 acc[4][NT];
    unsigned pa = 0;
    auto put = [&](int t) {
#pragma unroll
      for (int cp = 0; cp < 2; ++cp) {
        const f32x4 x = acc[2 * cp][t], y = acc[2 * cp + 1][t];
        *(LAS v4u*)(L + pa + (t * 2 + cp) * 1024u) = v4u{pk(x[0], x[1]), pk(x[2], x[3]), pk(y[0], y[1]), pk(y[2], y[3])};
      }
    };
    rep<0, 7>([&](auto J_) {
      constexpr int j = decltype(J_)::value;
      if constexpr (j == 2 || j == 4 || j == 6) __builtin_amdgcn_s_setprio(3 - j / 2);
      if constexpr (j == 5) {
        wait_for<p2_before(5, 0, NT) - p2_before(3, 1, NT)>(fl);
        const bool todo = __builtin_bit_cast(u64, fl) != tag;
        if (__any(todo)) poll(todo);
        SCHED();
      }
      if constexpr (j == 6) pa = opaque(wreg + (g * 16u + (((li >> 1) & 3u) | (((((li & 1u) | ((li >> 3) << 1)) + w) & 3u) << 2))) * 16u);
      if constexpr (j >= 4) wait_l();
      rep<0, NT>([&](auto T_) {
        constexpr int t = decltype(T_)::value, U = p2_unit(j, t, NT);
        if constexpr (U >= 0) b_m0(IC<U>{});
        wait_v<p2_before(j, t, NT) - p2_pos(j, t, NT) - 1>();
        const bf16x8 a = __builtin_bit_cast(bf16x8, af[j & 1][t]);
#pragma unroll
        for (int c = 0; c < 4; ++c) {
          if constexpr (j == 0) mma0(acc[c][t], cat(bl[0][c], bh[0][c]), a);
          else mma(acc[c][t], cat(bl[j & 3][c], bh[j & 3][c]), a);
        }
        SCHED();
        if constexpr (U >= 0) b_go(IC<U>{});
        if constexpr (j < 6) a_load(IC<j + 2>{}, T_);
        if constexpr (j == 5) a_load(IC<8>{}, T_);
        if constexpr (j == 5 && t == 0) ld32(gl, (RATIO ? 8 * w + 2 * g + (li >> 3) : lane) * 4u, rws, WS_G + w * 8192u + tt0 * 64u);
        if constexpr (j == 3 && t == 0) { if (fme) ld64(fl, lane * 64u, rws, frow); }
        if constexpr (j >= 2) {
          b_rd(IC<j + 2>{}, T_, IC<0>{});
          b_rd(IC<j + 2>{}, T_, IC<1>{});
          if constexpr (NT == 3 && t == 2) {
            b_rd(IC<j + 2>{}, IC<3>{}, IC<0>{});
            b_rd(IC<j + 2>{}, IC<3>{}, IC<1>{});
          }
        }
        if constexpr (j == 6) final_setup(T_);
        SCHED();
      });
      if constexpr (j == 0) STAMP(16);
      if constexpr (j == 2) STAMP(17);
      if constexpr (j == 4) STAMP(18);
      if constexpr (j == 6) STAMP(19);
    });
    constexpr int NALL = p2_before(6, 0, NT);
    float og[4];
    if (!RATIO) {
      wait_for<NALL - p2_before(5, 1, NT)>(gl);
      auto h2 = __builtin_amdgcn_permlane32_swap(__float_as_uint(gl), __float_as_uint(gl), false, false);
      auto lo2 = __builtin_amdgcn_permlane16_swap(h2[0], h2[0], false, false);
      auto hi2 = __builtin_amdgcn_permlane16_swap(h2[1], h2[1], false, false);
      og[0] = __uint_as_float(lo2[0]); og[1] = __uint_as_float(lo2[1]); og[2] = __uint_as_float(hi2[0]); og[3] = __uint_as_float(hi2[1]);
    }
    wait_l();
    STAMP(27);
    if (!RATIO) { if (w == 0) *(LAS float*)(L + SPARE + lane * 4u) = gl; }
    rep<0, NT>([&](auto T_) {
      constexpr int t = decltype(T_)::value;
      if (!RATIO) {
#pragma unroll
        for (int c = 0; c < 4; ++c) acc[c][t] *= og[t];
        SCHED();
        if constexpr (t >= 1) put(t - 1);
        SCHED();
      }
      wait_v<NALL - p2_pos(8, t, NT) - 1>();
      asm volatile("s_nop 0");
#pragma unroll
      for (int c = 0; c < 4; ++c) mma(acc[c][t], cat(bl[3][c], bh[3][c]), __builtin_bit_cast(bf16x8, aa[0][t]));
#pragma unroll
      for (int c = 0; c < 4; ++c) mma(acc[c][t], cat(bl[0][c], bh[0][c]), __builtin_bit_cast(bf16x8, aa[1][t]));
      SCHED();
      if constexpr (t >= 1 && RATIO) put(t - 1);
      final_setup(IC<4 + t>{});
      SCHED();
    });
    STAMP(7);
    asm volatile("s_nop 7" ::: "memory");
    SCHED();
    put(NT - 1);
  };
  if (qkv) phase2(IC<3>{});
  else phase2(IC<4>{});
  wait_l();
  bar();
  STAMP(8);
  if (!qkv || w < 6) {
    pin(gl);
    const float og = RATIO ? 1.0f : *(LAS float*)(L + ga2);
    bf16x8 pf[2][4];
#pragma unroll
    for (int u = 0; u < 4; ++u)
#pragma unroll
      for (int a = 0; a < 2; ++a) pf[a][u] = cat(ldstr(L, ra[u >> 1][0] + (u & 1) * 1024u + a * 256u), ldstr(L, ra[u >> 1][1] + (u & 1) * 1024u + a * 256u));
    f32x4 alo = blo * og, ahi = bhi * og;
#pragma unroll
    for (int u = 0; u < 4; ++u) {
      alo = mfma(pf[0][u], sel[u], alo);
      ahi = mfma(pf[1][u], sel[u], ahi);
    }
    if (RATIO) {
      alo *= gl;
      ahi *= gl;
    }
    const v4u o = {pk(alo[0], alo[1]), pk(alo[2], alo[3]), pk(ahi[0], ahi[1]), pk(ahi[2], ahi[3])};
    *(GAS v4u*)((char*)p.out + oo) = o;
  }
  STAMP(9);
}
}

extern "C" void kernel_launch(void* const* d_in, const int* in_sizes, int n_in, void* d_out, int out_size, void* d_ws, size_t ws_size,
                              hipStream_t stream) {
  gm::Params p;
  p.x = d_in[0];
  p.wqkv = d_in[1];
  p.wo = d_in[2];
  p.w1 = d_in[4];
  p.b1 = d_in[5];
  p.w2 = d_in[6];
  p.b2 = d_in[7];
  p.gw = d_in[8];
  p.out = d_out;
  p.ws = d_ws;
  gm::gm_kernel<<<256, 512, 0, stream>>>(p);
}
```

```cpp
#include <hip/hip_runtime.h>
#include <stdint.h>

typedef short bf16x8 __attribute__((ext_vector_type(8)));
typedef short s16x4 __attribute__((ext_vector_type(4)));
typedef float f32x4 __attribute__((ext_vector_type(4)));
typedef unsigned v4u __attribute__((ext_vector_type(4)));
typedef unsigned v2u __attribute__((ext_vector_type(2)));
typedef __bf16 bf2_t __attribute__((ext_vector_type(2)));
typedef float f32x2 __attribute__((ext_vector_type(2)));
typedef unsigned long long u64;
typedef u64 v2q __attribute__((ext_vector_type(2)));
typedef __amdgpu_buffer_rsrc_t rsrc_t;
#define LAS __attribute__((address_space(3)))
#define GAS __attribute__((address_space(1)))
#define DI __device__ __forceinline__

#define CODE_KB 23
#define NMID 4
#define T2HB 3
#define TMODE 1
#define TCNT 0x0C31151C38101520ull
#define TFIRST 0x00003F5400000000ull
#define TSH 2u
#define RATIO 1
#define FREE_SLEEP 0

extern "C" __device__ u64 gm_dispatch_id() __asm("llvm.amdgcn.dispatch.id");

namespace gm {
constexpr int S = 2048, D = 448, F = 1792, NQKV = 1344;
constexpr int KS_A = 16, KS = 72;
constexpr unsigned A2_TT = KS * 1024;
constexpr unsigned WS_G = 128 * A2_TT, WS_F = WS_G + 8 * 8192, WS_END = WS_F + 16 * 1024;
constexpr unsigned REG = 20480, STG = 6144, SPARE = 3 * STG;
constexpr int LDS_BYTES = 163840;
constexpr unsigned GW_OFF = 2 * REG + 5120;
constexpr unsigned SPIN_CAP = 1u << 16;
constexpr int TOUCH2_HB = T2HB;

struct Params {
  const void *x, *wqkv, *wo, *w1, *b1, *w2, *b2, *gw;
  void *out, *ws;
};
template <int N> struct IC { static constexpr int value = N; };
template <int I, int N, class Fn> DI void rep(Fn&& f) {
  if constexpr (I < N) {
    f(IC<I>{});
    rep<I + 1, N>(f);
  }
}

#define SCHED() __builtin_amdgcn_sched_barrier(0)
#define NOP1() asm volatile("s_nop 0")
DI rsrc_t mk(const void* p, unsigned n) { return __builtin_amdgcn_make_buffer_rsrc((void*)p, 0, n, 0x00020000); }
template <unsigned C> DI void set_m0(unsigned base) { asm volatile("s_add_u32 m0, %0, %1" ::"s"(base), "i"(C) : "scc", "memory"); }
template <unsigned IMM> DI void dma_go(unsigned voff, rsrc_t rs, unsigned soff) {
  asm volatile("buffer_load_dwordx4 %0, %1, %2 offen offset:%3 lds" ::"v"(voff), "s"(rs), "s"(soff), "i"(IMM) : "memory");
}
template <unsigned IMM> DI void dma_go0(unsigned voff, rsrc_t rs) {
  asm volatile("buffer_load_dwordx4 %0, %1, 0 offen offset:%2 lds" ::"v"(voff), "s"(rs), "i"(IMM) : "memory");
}
DI void touch(unsigned lds, unsigned voff, u64 base) {
  asm volatile("s_mov_b32 m0, %0\n\ts_nop 0\n\tglobal_load_lds_dword %1, %2" ::"s"(lds), "v"(voff), "s"(base) : "memory");
}
template <unsigned IMM> DI void ld128(v4u& d, unsigned voff, rsrc_t rs, unsigned soff) {
  asm volatile("buffer_load_dwordx4 %0, %1, %2, %3 offen offset:%4 sc1" : "=v"(d) : "v"(voff), "s"(rs), "s"(soff), "i"(IMM) : "memory");
}
DI void ld32(float& d, unsigned voff, rsrc_t rs, unsigned soff) {
  asm volatile("buffer_load_dword %0, %1, %2, %3 offen sc1" : "=v"(d) : "v"(voff), "s"(rs), "s"(soff) : "memory");
}
DI void ld64(v2u& d, unsigned voff, rsrc_t rs, unsigned soff) {
  asm volatile("buffer_load_dwordx2 %0, %1, %2, %3 offen sc1" : "+v"(d) : "v"(voff), "s"(rs), "s"(soff) : "memory");
}
DI void st128(v4u v, rsrc_t rs, unsigned voff, unsigned soff) {
  __builtin_amdgcn_raw_buffer_store_b128(v, rs, voff, soff, 16);
  SCHED();
  asm volatile("s_nop 1");
  SCHED();
}
template <int N> DI void wait_v() { asm volatile("s_waitcnt vmcnt(%0)" ::"i"(N) : "memory"); }
template <int N, class T> DI void wait_for(T& v) { asm volatile("s_waitcnt vmcnt(%1)" : "+v"(v) : "i"(N) : "memory"); }
DI void wait_l() {
  SCHED();
  __builtin_amdgcn_s_waitcnt(0xC07F);
  asm volatile("" ::: "memory");
  SCHED();
}
DI void bar() { asm volatile("s_barrier" ::: "memory"); }
DI f32x4 mfma(bf16x8 a, bf16x8 b, f32x4 c) { return __builtin_amdgcn_mfma_f32_16x16x32_bf16(a, b, c, 0, 0, 0); }
DI void mma(f32x4& c, bf16x8 a, bf16x8 b) { asm volatile("v_mfma_f32_16x16x32_bf16 %0, %1, %2, %0" : "+v"(c) : "v"(a), "v"(b)); }
DI void mma0(f32x4& c, bf16x8 a, bf16x8 b) { asm volatile("v_mfma_f32_16x16x32_bf16 %0, %1, %2, 0" : "=&v"(c) : "v"(a), "v"(b)); }
DI void mmac(f32x4& d, bf16x8 a, bf16x8 b, f32x4 c) { asm volatile("v_mfma_f32_16x16x32_bf16 %0, %1, %2, %3" : "=&v"(d) : "v"(a), "v"(b), "v"(c)); }
DI float relu(float v) {
  float r;
  asm("v_max_f32 %0, 0, %1" : "=v"(r) : "v"(v));
  return r;
}
DI float fmx(float a, float b) {
  float r;
  asm("v_max_f32 %0, %1, %2" : "=v"(r) : "v"(a), "v"(b));
  return r;
}
DI unsigned pk(float lo, float hi) {
  f32x2 v = {lo, hi};
  return __builtin_bit_cast(unsigned, __builtin_convertvector(v, bf2_t));
}
DI float rbf(float f) { return __uint_as_float(pk(f, 0.f) << 16); }
DI float bflo(unsigned u) { return __uint_as_float(u << 16); }
DI float bfhi(unsigned u) { return __uint_as_float(u & 0xffff0000u); }
DI bf16x8 asfrag(unsigned a, unsigned b, unsigned c, unsigned d) {
  v4u v = {a, b, c, d};
  return __builtin_bit_cast(bf16x8, v);
}
DI bf16x8 cat(s16x4 lo, s16x4 hi) { return __builtin_shufflevector(lo, hi, 0, 1, 2, 3, 4, 5, 6, 7); }
DI bf16x8 lds128(LAS char* L, unsigned a) { return *(LAS bf16x8*)(L + a); }
DI s16x4 ldstr(LAS char* L, unsigned a) { return __builtin_amdgcn_ds_read_tr16_b64_v4i16((LAS s16x4*)(L + a)); }
DI unsigned opaque(unsigned v) {
  asm volatile("" : "+v"(v));
  return v;
}
DI unsigned sopaque(unsigned v) {
  asm volatile("" : "+s"(v));
  return v;
}
template <class T> DI void pin(T& v) { asm volatile("" : "+v"(v)); }
DI void keep(f32x4 v) { asm volatile("" ::"v"(v)); }

#define STAMP(k)
#define FSTAMP(k)

constexpr unsigned hloc(int h) {
  h = h >= 10 ? h - 10 : h;
  return h == 0 ? SPARE : (h < 4 ? STG + (h - 1) * 2048u : (h < 7 ? 2 * STG + (h - 4) * 2048u : (h - 7) * 2048u));
}
constexpr unsigned ppos(int i) { return i < 2 ? i + 1 : (i == 2 ? 0 : i); }

constexpr int q_item(int i, bool ffn) { return i < 36 ? 8 * (1 + i / 6) + (ffn ? i % 6 : (i % 6 + 5) % 6) : 100 + (i - 36); }
constexpr int q_from(int item) {
  if (item < 100) return (item >> 3) <= 2 ? 0 : 2 * ((item >> 3) - 3) + 1;
  const int h = (item - 100) >> 1;
  return h == 0 ? 1 : (h < 4 ? 9 : (h < 7 ? 11 : 13));
}
constexpr int q_start(int hb) {
  int i = 6;
  for (int h = 0; h < hb; ++h)
    for (int k = 0; k < 4; ++k)
      if (i < 52 && q_from(q_item(i, true)) <= h) ++i;
  return i;
}
constexpr int q_at(int hb, int i, bool ffn) {
  return (i & 3) == 1 && q_start(hb) + (i >> 2) < q_start(hb + 1) ? q_item(q_start(hb) + (i >> 2), ffn) : -1;
}
constexpr int q_count(int from, int to, bool ffn) {
  int n = 0;
  for (int i = from; i < to; ++i) n += (ffn || i >= 36 || i % 6 != 0) ? 1 : 0;
  return n;
}
constexpr int q_wait(int s, bool ffn) { return q_count(s < 6 ? 6 * (s + 1) : 44, q_start(2 * s + 1), ffn); }
static_assert(q_start(14) == 52 && q_start(3) >= 12 && q_start(5) >= 18 && q_start(7) >= 24 && q_start(9) >= 30 && q_start(11) >= 36 &&
                  q_start(13) >= 44 && q_start(13) - q_start(12) <= 2 && q_start(14) - q_start(13) <= 2,
              "queue order");
constexpr int r_unit(int i) { return i < 2 ? 16 + i : (i < 6 ? 18 + i : 12 + i); }

constexpr int p2_unit(int j, int t, int nt) { return nt == 3 && 3 * j + t < 12 ? 24 + 3 * j + t : -1; }
constexpr int p2_after(int j, int t, int nt) {
  return (p2_unit(j, t, nt) >= 0 ? 1 : 0) + (j < 5 ? 1 : (j == 5 ? 2 : 0))
         + (j == 5 && t == 0 ? 1 : 0)
         + (j == 3 && t == 0 ? 1 : 0);
}
constexpr int p2_before(int j, int t, int nt) {
  int n = 2 * nt;
  for (int i = 0; i < nt * j + t; ++i) n += p2_after(i / nt, i % nt, nt);
  return n;
}
constexpr int p2_pos(int j, int t, int nt) {
  return j < 2 ? nt * j + t : (j == 8 ? p2_before(5, t, nt) + 1 : p2_before(j - 2, t, nt) + (p2_unit(j - 2, t, nt) >= 0 ? 1 : 0));
}

__global__ __launch_bounds__(512, 2) void gm_kernel(Params p) {
  __shared__ __attribute__((aligned(1024))) char smem[LDS_BYTES];
  LAS char* L = (LAS char*)smem;
  u64 px, pw, pv, pt;
  asm volatile("s_load_dwordx2 %0, %1, 0x0" : "=&s"(px) : "s"(__builtin_amdgcn_kernarg_segment_ptr()));
  SCHED();
  const unsigned tid = threadIdx.x, lane = tid & 63;
  const unsigned b = blockIdx.x, xcd = b & 7, slot = b >> 3;
  const bool ffn = b < 112, qkv = b >= 128;
  const unsigned w = __builtin_amdgcn_readfirstlane(tid >> 6);
  asm volatile("s_load_dwordx2 %0, %3, %4\n\ts_load_dwordx2 %1, %3, %5\n\ts_load_dwordx2 %2, %3, %6"
               : "=&s"(pw), "=&s"(pv), "=&s"(pt)
               : "s"(__builtin_amdgcn_kernarg_segment_ptr()), "s"(qkv ? 8u : 24u), "s"(qkv ? 56u : 32u), "s"(((0x25130130u >> (4 * w)) & 7u) * 8u));
  if (tid < CODE_KB) {
    u64 pc;
    asm volatile("s_getpc_b64 %0" : "=s"(pc));
    touch(SPARE + 1024u, ((slot & 7) * CODE_KB + tid) * 128u, pc);
  }
  const unsigned wreg = w * REG;
  const unsigned li = lane & 15, g = lane >> 4, rr = lane >> 3, pos = lane & 7;
  const unsigned rq = xcd >> 1, side = xcd & 1;
  const unsigned tcnt = (unsigned)(TCNT >> (8 * w)) & 0xffu;
  const unsigned tfirst = ((unsigned)(TFIRST >> (8 * w)) & 0xffu) * 64u;
  const unsigned tsh = w ? 6u : TSH;
  const unsigned tln = opaque(((32 * xcd + slot) * (w ? tcnt : 56u) + tfirst + (lane >> tsh) * 7u + (lane & ((1u << tsh) - 1u))) * 128u);
  auto do_touch = [&]() {
    if (lane < tcnt) touch(wreg + SPARE + 1024u, tln, pt);
  };
  const bool mid3 = (slot >> 2) == 3;
  const unsigned rb = 4 * rq + (mid3 ? 2 * side + (slot & 1) : slot & 3), cs = mid3 ? 3u : side * 4 + ((slot >> 2) & 3);
  unsigned vx[2];
#pragma unroll
  for (int h = 0; h < 2; ++h) vx[h] = opaque((128 * rb + 16 * w + 8 * h + rr) * 896u + ((pos ^ (4u * h + (rr >> 1))) << 4));
  const unsigned vw = opaque((8 * w + rr) * (qkv ? 2688u : 3584u) + (min(pos ^ ((((rr >> 1) & 1) | ((w & 1) << 1)) << 1), qkv ? 6u : 7u) << 4) +
                             cs * (qkv ? 112u : 512u));
  SCHED();
  asm volatile("s_waitcnt lgkmcnt(0)" : "+s"(px), "+s"(pw), "+s"(pv), "+s"(pt)::"memory");
  SCHED();
  STAMP(10);
  const rsrc_t rx = mk((const void*)px, S * D * 2);
  const rsrc_t rw = mk((const void*)pw, qkv ? D * NQKV * 2 : D * F * 2);
  if (w < 3) do_touch();
  if (ffn || qkv) {
    const unsigned wus = qkv ? 896u : 128u;
    rep<0, 5>([&](auto I_) {
      constexpr int I = decltype(I_)::value;
      set_m0<ppos(I) * 1024u>(wreg);
      NOP1();
      if constexpr (I < 2) dma_go0<0>(vx[I], rx);
      else dma_go<0>(vw, rw, (I - 2) * wus);
    });
  }
  SCHED();

  rsrc_t rws, rw2, rwo;
  unsigned vb[2], ba[4], fo_pub, ct = 0, tt0 = 0;
  const unsigned bo2 = w * (224u * 896u), boo = w * (56u * 896u);
  const unsigned q4 = (lane >> 2) & 3, p4 = lane & 3;
  const unsigned fsw = (q4 >> 1) | ((g & 1) << 1);
  s16x4 bl[4][4], bh[4][4];
  auto b_m0 = [&](auto N_) {
    constexpr int N = decltype(N_)::value;
    set_m0<hloc(N >> 1) + (N & 1) * 1024u>(wreg);
  };
  auto b_go = [&](auto N_) {
    constexpr int N = decltype(N_)::value, j = N >> 2;
    if constexpr (j < 7) dma_go<0>(vb[N & 1], rw2, bo2 + (N >> 1) * (16u * 896u));
    else dma_go<0>(vb[N & 1], rwo, boo + ((j - 7) * 28u + ((N >> 1) & 1) * 16u) * 896u);
  };
  auto b_dma = [&](auto N_) { b_m0(N_); NOP1(); b_go(N_); };
  auto r_dma = [&](auto I_) { b_dma(IC<r_unit(decltype(I_)::value)>{}); };
  auto b_rd = [&](auto J_, auto C_, auto E_) {
    constexpr int J = decltype(J_)::value, C = decltype(C_)::value, E = decltype(E_)::value;
    if constexpr (E == 0) bl[J & 3][C] = ldstr(L, ba[C] + hloc(2 * J));
    else bh[J & 3][C] = ldstr(L, ba[C] + hloc(2 * J + 1));
  };
  auto rd_step = [&](auto J_) { rep<0, 8>([&](auto I_) { constexpr int I = decltype(I_)::value; b_rd(J_, IC<(I >> 1)>{}, IC<(I & 1)>{}); }); };
  auto setup2 = [&](auto K_, auto Q_) {
    constexpr int K = decltype(K_)::value;
    constexpr bool Q = decltype(Q_)::value;
    if constexpr (K == 0) {
      ct = Q ? (slot >> 2) - 1 : (unsigned)max((int)(slot >> 2), (int)slot - 9);
      tt0 = 16 * xcd + (Q ? 4 + 3 * (slot & 3) : (slot < 12 ? 4 * (slot & 3) : 0u));
      rws = mk(p.ws, WS_END);
      rw2 = mk(p.w2, F * D * 2);
      rwo = mk(p.wo, D * D * 2);
    } else if constexpr (K == 2) {
      vb[0] = opaque(rr * 896u + ((pos ^ (((rr >> 1) & 3) << 1)) << 4) + ct * 128u);
      vb[1] = opaque(vb[0] + 8u * 896u);
    } else if constexpr (K >= 3 && K < 7) {
      ba[K - 3] = opaque(wreg + (4 * g + q4) * 128u + ((2u * ((K - 3) ^ fsw) + (p4 >> 1)) << 4) + (p4 & 1) * 8u);
    }
  };

  auto phase1 = [&](auto FFN_) {
    constexpr bool FFN = decltype(FFN_)::value;
    constexpr unsigned LDW = FFN ? 3584u : 2688u, WUS = FFN ? 128u : 896u;
    unsigned vh5 = 0;
    if (!FFN) vh5 = opaque(((rb ? 128 * rb - 16 : 0u) + 8 * (w & 1) + rr) * 896u + ((pos ^ (4u * (w & 1) + (rr >> 1))) << 4));
    auto d_m0 = [&](auto E_) {
      constexpr int E = decltype(E_)::value, I = E & 7, ST = E >> 3;
      if constexpr (E >= 100) b_m0(IC<E - 100>{});
      else if constexpr (E >= 0) set_m0<(ST % 3) * STG + ppos(I) * 1024u - ((I < 2 || (!FFN && I == 5)) ? ST * 128u : (I - 2) * WUS)>(wreg);
    };
    auto d_go = [&](auto E_) {
      constexpr int E = decltype(E_)::value, I = E & 7, ST = E >> 3;
      if constexpr (E >= 100) b_go(IC<E - 100>{});
      else if constexpr (E >= 0) {
        if constexpr (I < 2) dma_go0<ST * 128u>(vx[I], rx);
        else if constexpr (!FFN && I == 5) { if (w < 2) dma_go0<ST * 128u>(vh5, rx); }
        else if constexpr (ST == 0) dma_go0<(I - 2) * WUS>(vw, rw);
        else dma_go<(I - 2) * WUS>(vw, rw, ST * 64u * LDW);
      }
    };
    d_m0(IC<5>{});
    NOP1();
    d_go(IC<5>{});
    if (w == 2) {
      set_m0<FFN ? SPARE : GW_OFF>(0);
      NOP1();
      dma_go0<0>(lane * 16, mk((const char*)pv + (FFN ? cs * 512u : 0u), FFN ? 512 : 896));
    }
    SCHED();
    STAMP(11);
    const unsigned xa0 = 1024u + (li >> 3) * 1024u + (li & 7) * 128u + ((g ^ ((li >> 1) & 7)) << 4);
    auto second_stage = [&]() {
      SCHED();
      STAMP(12);
      bar();
      rep<0, 6>([&](auto I_) { constexpr int E = q_item(decltype(I_)::value, FFN); d_m0(IC<E>{}); NOP1(); d_go(IC<E>{}); });
      STAMP(14);
      if (w >= 3 && w < 6) {
        do_touch();
        wait_v<q_count(0, 6, FFN) + 1>();
      } else {
        wait_v<q_count(0, 6, FFN)>();
      }
      bar();
      STAMP(1);
    };
    auto mid = [&](auto S_) {
      constexpr int s = decltype(S_)::value;
      constexpr bool t2 = TOUCH2_HB <= 2 * s && q_start(TOUCH2_HB + 1) >= 6 * (s + 1);
      if ((s == 0 && w >= 3 && w < 6) || (t2 && w >= 6)) wait_v<q_wait(s, FFN) + 1>();
      else wait_v<q_wait(s, FFN)>();
      wait_l();
      bar();
      FSTAMP(20 + s);
    };

    if constexpr (FFN) {
      const unsigned mh = w >> 2, nq = w & 3;
      unsigned xa[2][2], wa[2][4];
#pragma unroll
      for (int hs = 0; hs < 2; ++hs) {
#pragma unroll
        for (int t2 = 0; t2 < 2; ++t2) xa[hs][t2] = opaque((xa0 ^ (hs * 64u)) + (mh * 4u + t2 * 2u) * REG);
#pragma unroll
        for (int c = 0; c < 4; ++c)
          wa[hs][c] = opaque((4u * hs + g) * REG + (nq ? 2u + nq : 0u) * 1024u + q4 * 128u + ((2u * (c ^ fsw) + (p4 >> 1)) << 4) + (p4 & 1) * 8u);
      }
      second_stage();
      bf16x8 xf[2][4];
      s16x4 wl[2][4], wh[2][4];
      f32x4 acc[4][4];
      auto rd = [&](auto I_, auto HB_) {
        constexpr int I = decltype(I_)::value, HB = decltype(HB_)::value, hs = HB & 1;
        constexpr unsigned bo = ((HB >> 1) % 3) * STG;
        if constexpr (I >= 2 && I < 6) xf[hs][I - 2] = lds128(L, xa[hs][(I - 2) >> 1] + ((I - 2) & 1) * REG + bo);
        else if constexpr (I < 12) {
          constexpr int c = I < 2 ? 0 : (I - 4) >> 1;
          if constexpr ((I & 1) == 0) wl[hs][c] = ldstr(L, wa[hs][c] + bo);
          else wh[hs][c] = ldstr(L, wa[hs][c] + bo + 512u);
        }
      };
      f32x4 bv[4];
#pragma unroll
      for (int c = 0; c < 4; ++c) {
        const v2u bb = *(LAS v2u*)(L + SPARE + nq * 128u + c * 32u + g * 8u);
        bv[c] = f32x4{bflo(bb.x), bfhi(bb.x), bflo(bb.y), bfhi(bb.y)};
      }
      rep<0, 12>([&](auto I_) { rd(I_, IC<0>{}); });
#pragma unroll
      for (int c = 0; c < 4; ++c) pin(bv[c]);
      unsigned so = 0;
      auto epi = [&](int t, int u) {
        float hv[8];
#pragma unroll
        for (int j = 0; j < 8; ++j) {
          const float r = relu(acc[2 * u + (j >> 2)][t][j & 3]);
          hv[j] = r * r;
        }
        const v4u o = {pk(hv[0], hv[1]), pk(hv[2], hv[3]), pk(hv[4], hv[5]), pk(hv[6], hv[7])};
        st128(o, rws, lane * 16u + u * 1024u, so + t * A2_TT);
      };
      rep<0, 12>([&](auto HB_) {
        constexpr int HB = decltype(HB_)::value, hs = HB & 1;
        if constexpr (HB == 0) asm volatile("s_nop 1");
        else if constexpr (hs == 0) wait_l();
        rep<0, 16>([&](auto I_) {
          constexpr int I = decltype(I_)::value;
          constexpr int E = q_at(HB, I, FFN);
          d_m0(IC<E>{});
          if constexpr (HB == 0) mmac(acc[I >> 2][I & 3], cat(wl[hs][I >> 2], wh[hs][I >> 2]), xf[hs][I & 3], bv[I >> 2]);
          else mma(acc[I >> 2][I & 3], cat(wl[hs][I >> 2], wh[hs][I >> 2]), xf[hs][I & 3]);
          SCHED();
          rd(I_, IC<HB + 1>{});
          if constexpr (I == 14 && HB < 7) setup2(HB_, IC<0>{});
          if constexpr (I == 14 && HB == 7) {
            so = sopaque((8 * rb + 4 * mh) * A2_TT + (KS_A + 8 * cs + 2 * nq) * 1024u);
            fo_pub = sopaque(WS_F + rb * 1024u + cs * 64u);
          }
          if constexpr (I == 15 && HB == TOUCH2_HB) { if (w >= 6) do_touch(); }
          SCHED();
          d_go(IC<E>{});
        });
        if constexpr (hs == 0) mid(IC<(HB >> 1)>{});
      });
      wait_l();
      rep<0, 32>([&](auto M_) {
        constexpr int M = decltype(M_)::value, G = M >> 2, c = M & 3;
        constexpr int hs = G < 4 ? G >> 1 : G & 1, t = G < 4 ? G & 1 : G >> 1;
        constexpr int E = M < 8 ? q_at(12, M, FFN) : (M < 16 ? q_at(13, M - 8, FFN) : -1);
        d_m0(IC<E>{});
        mma(acc[c][t], cat(wl[hs][c], wh[hs][c]), xf[hs][t]);
        SCHED();
        if constexpr (M < 6) { rd(IC<2 * M>{}, IC<13>{}); rd(IC<2 * M + 1>{}, IC<13>{}); }
        if constexpr (M >= 8 && M < 14) {
          rep<0, (M < 12 ? 3 : 2)>([&](auto Q_) {
            constexpr int Q = (M < 12 ? 3 * (M - 8) : 12 + 2 * (M - 12)) + decltype(Q_)::value;
            b_rd(IC<(Q >> 3)>{}, IC<((Q >> 1) & 3)>{}, IC<(Q & 1)>{});
          });
        }
        if constexpr (M >= 14 && M < 32 && (M - 14) % 3 == 0) epi((M - 14) / 6, ((M - 14) / 3) & 1);
        SCHED();
        d_go(IC<E>{});
        if constexpr (M == 7) mid(IC<6>{});
      });
      STAMP(2);
      asm volatile("s_nop 5" ::: "memory");
      SCHED();
      epi(3, 0);
      epi(3, 1);
    } else {
      const unsigned tq = w & 3, hd = w >> 2, hp = cs;
      unsigned xa[2], wa[2][2], xh[2], wq[2];
#pragma unroll
      for (int hs = 0; hs < 2; ++hs) {
        xa[hs] = opaque((xa0 ^ (hs * 64u)) + tq * 2u * REG);
#pragma unroll
        for (int c = 0; c < 2; ++c) {
          const unsigned bo = 56u * hd + 32u * c + 8u * p4;
          wa[hs][c] = opaque((4u * hs + g) * REG + q4 * 128u + (((bo >> 4) ^ (fsw << 1)) << 4) + (bo & 8u));
        }
        xh[hs] = opaque((li >> 3) * REG + 5120u + (li & 7) * 128u + (((4u * hs + g) ^ ((li >> 1) & 7)) << 4));
        const unsigned bo = 56u * hd + 32u * (tq & 1) + 8u * p4;
        wq[hs] = opaque((4u * hs + g) * REG + q4 * 128u + (((bo >> 4) ^ (fsw << 1)) << 4) + (bo & 8u) + (3u + (tq >> 1)) * 1024u);
      }
      const unsigned ga = opaque(GW_OFF + g * 16u);
      second_stage();
      float mb[2][4];
      unsigned oa = 0, hwa = 0, nwa = 0, pm = 0;
      auto setupq = [&](auto K_) {
        constexpr int K = decltype(K_)::value, t = K & 1;
        if constexpr (K < 2) {
#pragma unroll
          for (int j = 0; j < 4; ++j) {
            const int kap = 16 * (j >> 1) + (int)(4 * g) + 2 * (j & 1) + (int)(li & 1) - 16;
            const int dl = (int)li - kap;
            const bool ok = (dl >= 0) && (dl <= 14) && (t == 1 || j >= 2 || (rb | tq) != 0);
            mb[t][j] = __uint_as_float(opaque(ok ? 0u : 0xff800000u));
          }
        } else if constexpr (K == 2) {
          oa = sopaque((8 * rb + 2 * tq) * A2_TT + (2 * hp + hd) * 1024u);
          fo_pub = sopaque(WS_F + rb * 1024u + (7 + hp) * 64u);
        } else if constexpr (K == 3) {
          hwa = opaque(hd * 4u * REG + (tq < 2 ? 4096u + lane * 16u + tq * 8u : 4096u + tq * 512u + li * 32u + g * 8u));
          nwa = opaque(wreg + (tq < 3 ? REG + 4096u : 2048u) + lane * 16u);
          pm = opaque(g == 3 ? 0u : 0xffffffffu);
        }
      };
      f32x4 ac[7][2], ah;
      bf16x8 xf[2][2], gf[2], hf[2];
      s16x4 wl[2][6], wh[2][6], hl[2], hh[2];
      auto rd = [&](auto I_, auto HB_) {
        constexpr int I = decltype(I_)::value, HB = decltype(HB_)::value, hs = HB & 1;
        constexpr unsigned bo = ((HB >> 1) % 3) * STG;
        if constexpr (I == 2 || I == 3) xf[hs][I - 2] = lds128(L, xa[hs] + (I - 2) * REG + bo);
        else if constexpr (I < 14) {
          constexpr int c = I < 2 ? 0 : (I - 2) >> 1;
          if constexpr ((I & 1) == 0) wl[hs][c] = ldstr(L, wa[hs][c & 1] + ppos(2 + (c >> 1)) * 1024u + bo);
          else wh[hs][c] = ldstr(L, wa[hs][c & 1] + ppos(2 + (c >> 1)) * 1024u + bo + 512u);
        } else if constexpr (I == 14) gf[hs] = lds128(L, ga + HB * 64u);
        else if constexpr (I == 15) hf[hs] = lds128(L, xh[hs] + bo);
        else if constexpr (I == 16) hl[hs] = ldstr(L, wq[hs] + bo);
        else if constexpr (I == 17) hh[hs] = ldstr(L, wq[hs] + bo + 512u);
      };
      bf16x8 qf[2], kf[2], kp;
      unsigned vh[2][2][2];
      float gv[2], og[2];
      v4u vp;
      auto pack_kv = [&](int t) {
        kf[t] = asfrag(pk(ac[2][t][0], ac[2][t][1]), pk(ac[2][t][2], ac[2][t][3]), pk(ac[3][t][0], ac[3][t][1]), pk(ac[3][t][2], ac[3][t][3]));
#pragma unroll
        for (int c = 0; c < 2; ++c) {
          vh[t][c][0] = pk(ac[4 + c][t][0], ac[4 + c][t][1]);
          vh[t][c][1] = pk(ac[4 + c][t][2], ac[4 + c][t][3]);
        }
      };
      auto pack_q = [&](int t) {
        qf[t] = asfrag(pk(ac[0][t][0], ac[0][t][1]), pk(ac[0][t][2], ac[0][t][3]), pm & pk(ac[1][t][0], ac[1][t][1]), pm & pk(ac[1][t][2], ac[1][t][3]));
        pin(qf[t]);
      };
      rep<0, 18>([&](auto I_) { rd(I_, IC<0>{}); });
      rep<0, 14>([&](auto HB_) {
        constexpr int HB = decltype(HB_)::value, hs = HB & 1;
        if constexpr (hs == 0 && HB > 0) wait_l();
        rep<0, 15>([&](auto I_) {
          constexpr int I = decltype(I_)::value;
          constexpr int c = HB < 13 ? I >> 1 : (I == 0 ? 7 : (I < 5 ? 1 + I : (I < 7 ? 6 : (I < 11 ? (I - 7) & 1 : I - 9))));
          constexpr int t = HB < 13 ? I & 1 : (I < 5 ? 1 : (I < 7 ? I - 5 : (I < 11 ? 1 - ((I - 7) >> 1) : 0)));
          constexpr int E = q_at(HB, I, FFN);
          d_m0(IC<E>{});
          if constexpr (c == 7) {
            if constexpr (HB == 0) mma0(ah, cat(hl[hs], hh[hs]), hf[hs]);
            else mma(ah, cat(hl[hs], hh[hs]), hf[hs]);
          } else {
            const bf16x8 wf = c < 6 ? cat(wl[hs][c < 6 ? c : 0], wh[hs][c < 6 ? c : 0]) : gf[hs];
            if constexpr (HB == 0) {
              if constexpr (c == 4 || c == 5) mma0(ac[c][t], xf[hs][t], wf);
              else mma0(ac[c][t], wf, xf[hs][t]);
            } else {
              if constexpr (c == 4 || c == 5) mma(ac[c][t], xf[hs][t], wf);
              else mma(ac[c][t], wf, xf[hs][t]);
            }
          }
          SCHED();
          if constexpr (HB < 13) {
            if constexpr (I < 7) { rd(IC<2 * I>{}, IC<HB + 1>{}); rd(IC<2 * I + 1>{}, IC<HB + 1>{}); }
            else rd(IC<I + 7>{}, IC<HB + 1>{});
            if constexpr (I == 12 && HB < 7) setup2(HB_, IC<1>{});
            if constexpr (I == 12 && HB >= 7 && HB < 11) setupq(IC<HB - 7>{});
            if constexpr (I == 14 && HB == TOUCH2_HB) { if (w >= 6) do_touch(); }
          } else {
            if constexpr (I < 8) {
              b_rd(IC<(I >> 2)>{}, IC<(I & 3)>{}, IC<0>{});
              b_rd(IC<(I >> 2)>{}, IC<(I & 3)>{}, IC<1>{});
            }
            if constexpr (I == 5) {
              *(LAS v2u*)(L + hwa) = v2u{pk(ah[0], ah[1]), pk(ah[2], ah[3])};
            }
            if constexpr (I == 8) {
              pack_kv(1);
              *(LAS bf16x8*)(L + nwa) = kf[1];
              *(LAS v4u*)(L + nwa + 1024u) = v4u{vh[1][0][0], vh[1][0][1], vh[1][1][0], vh[1][1][1]};
            }
            if constexpr (I == 10) {
#pragma unroll
              for (int t2 = 0; t2 < 2; ++t2) {
                gv[t2] = rbf(__builtin_amdgcn_rcpf(1.0f + __builtin_amdgcn_exp2f(rbf(ac[6][t2][0]) * -1.4426950408889634f)));
                og[t2] = rbf(1.0f - gv[t2]);
                if (RATIO) og[t2] = fmaxf(og[t2], 9.094947017729282e-13f);
                keep(ac[6][t2]);
                pin(gv[t2]);
                pin(og[t2]);
              }
            }
            if constexpr (I == 11) {
              wait_l();
              bar();
              kp = lds128(L, wreg + 4096u + lane * 16u);
              if (tq == 0) vp = __builtin_bit_cast(v4u, cat(ldstr(L, wreg + 5120u + lane * 8u), ldstr(L, wreg + 5632u + lane * 8u)));
              else vp = *(LAS v4u*)(L + wreg + 5120u + lane * 16u);
              pack_q(1);
            }
            if constexpr (I == 13) pack_q(0);
          }
          SCHED();
          d_go(IC<E>{});
        });
        if constexpr (hs == 0) mid(IC<(HB >> 1)>{});
      });
      STAMP(2);
      const f32x4 z = {0.f, 0.f, 0.f, 0.f};
      f32x4 s0[2], s1[2];
      s1[1] = mfma(kf[1], qf[1], z);
      s0[0] = mfma(kp, qf[0], z);
      SCHED();
      asm volatile("s_nop 4");
      SCHED();
      pack_kv(0);
      s0[1] = mfma(kf[0], qf[1], z);
      s1[0] = mfma(kf[0], qf[0], z);
      const bool odd = li & 1;
      const unsigned psh = odd ? 16u : 0u;
      float sv[2][4];
      auto pick = [&](f32x4 v, int u) {
        float lo = v[2 * u], hi = v[2 * u + 1];
        asm("" : "+v"(hi));
        return odd ? hi : lo;
      };
      const float SC = 0.18898223650461362f * 1.4426950408889634f;
#pragma unroll
      for (int u = 0; u < 2; ++u) {
        sv[1][2 + u] = __builtin_fmaf(pick(s1[1], u), SC, mb[1][2 + u]);
        sv[0][u] = __builtin_fmaf(pick(s0[0], u), SC, mb[0][u]);
      }
#pragma unroll
      for (int u = 0; u < 2; ++u) {
        sv[1][u] = __builtin_fmaf(pick(s0[1], u), SC, mb[1][u]);
        sv[0][2 + u] = __builtin_fmaf(pick(s1[0], u), SC, mb[0][2 + u]);
      }
      float m[2];
      {
        const float m0 = fmaxf(fmaxf(sv[0][0], sv[0][1]), fmaxf(sv[0][2], sv[0][3])), m1 = fmaxf(fmaxf(sv[1][0], sv[1][1]), fmaxf(sv[1][2], sv[1][3]));
        auto a = __builtin_amdgcn_permlane32_swap(__float_as_uint(m0), __float_as_uint(m1), false, false);
        const float x = fmx(__uint_as_float(a[0]), __uint_as_float(a[1]));
        auto b2 = __builtin_amdgcn_permlane16_swap(__float_as_uint(x), __float_as_uint(x), false, false);
        const float y = fmx(__uint_as_float(b2[0]), __uint_as_float(b2[1]));
        auto c2 = __builtin_amdgcn_permlane32_swap(__float_as_uint(y), __float_as_uint(y), false, false);
        m[0] = __uint_as_float(c2[0]);
        m[1] = __uint_as_float(c2[1]);
      }
      const bf16x8 ones = asfrag(0x3f803f80u, 0x3f803f80u, 0x3f803f80u, 0x3f803f80u);
      f32x4 o[2][2], sm[2];
#pragma unroll
      for (int t = 0; t < 2; ++t) {
        unsigned pw4[4];
#pragma unroll
        for (int j = 0; j < 4; ++j) pw4[j] = pk(__builtin_amdgcn_exp2f(sv[t][j] - m[t]), 0.f) << psh;
        const bf16x8 pf = asfrag(pw4[0], pw4[1], pw4[2], pw4[3]);
        sm[t] = mfma(ones, pf, z);
#pragma unroll
        for (int c = 0; c < 2; ++c) {
          const bf16x8 vf = t == 0 ? asfrag(vp[2 * c], vp[2 * c + 1], vh[0][c][0], vh[0][c][1]) : asfrag(vh[0][c][0], vh[0][c][1], vh[1][c][0], vh[1][c][1]);
          o[t][c] = mfma(vf, pf, z);
        }
      }
      v4u ov[2];
#pragma unroll
      for (int t = 0; t < 2; ++t) {
        const float sc = gv[t] * __builtin_amdgcn_rcpf(RATIO ? sm[t][0] * og[t] : sm[t][0]);
        const f32x4 o0 = o[t][0] * sc, o1 = o[t][1] * sc;
        ov[t] = v4u{pk(o0[0], o0[1]), pk(o0[2], o0[3]), pm & pk(o1[0], o1[1]), pm & pk(o1[2], o1[3])};
      }
      SCHED();
      st128(ov[0], rws, lane * 16u, oa);
      st128(ov[1], rws, lane * 16u, oa + A2_TT);
      if (hd == 0 && g < 2)
        __builtin_amdgcn_raw_buffer_store_b32(__float_as_uint(g ? og[1] : og[0]), rws, lane * 4u, WS_G + hp * 8192u + (128 * rb + 32 * tq) * 4u, 16);
    }
  };
  if (ffn) phase1(IC<1>{});
  else if (qkv) phase1(IC<0>{});
  else {
    if (w >= 3) do_touch();
    rep<0, 7>([&](auto K_) { setup2(K_, IC<0>{}); });
    for (int i = 0; i < FREE_SLEEP; ++i) __builtin_amdgcn_s_sleep(16);
    rep<0, 16>(b_dma);
    wait_v<0>();
    rd_step(IC<0>{});
    rd_step(IC<1>{});
    fo_pub = WS_F + 15 * 64u;
  }
  STAMP(3);
  ct = __builtin_amdgcn_readfirstlane(ct);
  tt0 = __builtin_amdgcn_readfirstlane(tt0);

  const u64 tag = (gm_dispatch_id() + 1) * 0x9E3779B97F4A7C15ull ^ (u64)__builtin_amdgcn_dispatch_ptr();
  unsigned va[4];
#pragma unroll
  for (int t = 0; t < 4; ++t) va[t] = opaque(lane * 16u + t * A2_TT);
  const unsigned aob = tt0 * A2_TT + (16u + 7u * w) * 1024u, aoa = tt0 * A2_TT + 2u * w * 1024u;
  const unsigned frow = WS_F + (tt0 >> 3) * 1024u, frows = (tt0 & 7) == 7 ? 1u : 0u;
  SCHED();
  if (!qkv) {
    wait_l();
    if (NMID) bar();
    rep<16, 16 + NMID>(b_dma);
  } else {
    rep<0, NMID>(r_dma);
  }
  wait_v<NMID>();
  rd_step(IC<2>{});
  rd_step(IC<3>{});
  SCHED();
  STAMP(4);
  bar();
  if (tid < 4) {
    const v2q tv = {tag, tag};
    st128(__builtin_bit_cast(v4u, tv), rws, lane * 16u, fo_pub);
  }
  const unsigned oct = (li & 4) | ((li & 1) << 1) | ((li >> 1) & 1);
  v4u b2v;
  ld128<0>(b2v, oct * 16u, mk(p.b2, D * 2), ct * 128u);
  wait_l();
  if (!qkv) rep<16 + NMID, 36>(b_dma);
  else rep<NMID, 8>(r_dma);
  STAMP(5);

  auto poll = [&](bool todo) {
#pragma unroll 1
    for (unsigned spin = 0; spin < SPIN_CAP; ++spin) {
      if (todo) {
        const v2u v = __builtin_amdgcn_raw_buffer_load_b64(rws, lane * 64u, frow, 16);
        todo = __builtin_bit_cast(u64, v) != tag;
      }
      asm volatile("" ::: "memory");
      if (!__any(todo)) break;
      __builtin_amdgcn_s_sleep(1);
      if (!ffn && !qkv) __builtin_amdgcn_s_sleep(12);
    }
  };
  poll(li >= ((7 * w) >> 3) && li <= ((7 * w + 6) >> 3) && g <= frows);
  STAMP(6);
  unsigned ra[2][2], oo = 0, ga2 = 0;
  bf16x8 sel[4];
  f32x4 blo, bhi;
  float gl;
  auto final_setup = [&](auto K_) {
    constexpr int K = decltype(K_)::value;
    if constexpr (K < 2) {
#pragma unroll
      for (int e = 0; e < 2; ++e)
        ra[K][e] = opaque((q4 + 4u * e) * REG + (w >> 1) * 2048u + ((g >> 1) * 32u + (p4 | ((((K | ((w & 1u) << 1)) + q4) & 3u) << 2))) * 16u + (g & 1) * 8u);
    } else if constexpr (K == 2) {
      const unsigned tk = 8 * w + 2 * g + (li >> 3);
      oo = opaque((16 * tt0 + tk) * 896u + ct * 128u + oct * 16u);
      if (!RATIO) ga2 = opaque(SPARE + tk * 4u);
    } else if constexpr (K == 4 || K == 5) {
#pragma unroll
      for (int u = 2 * (K - 4); u < 2 * (K - 4) + 2; ++u) {
        const unsigned one = li == 4 * u + g ? 0x3f803f80u : 0u;
        sel[u] = asfrag(one, one, one, one);
        pin(sel[u]);
      }
    } else if constexpr (K == 6) {
      pin(b2v);
      blo = f32x4{bflo(b2v[0]), bfhi(b2v[0]), bflo(b2v[1]), bfhi(b2v[1])};
      bhi = f32x4{bflo(b2v[2]), bfhi(b2v[2]), bflo(b2v[3]), bfhi(b2v[3])};
      pin(blo);
      pin(bhi);
    }
  };
  auto phase2 = [&](auto NT_) {
    constexpr int NT = decltype(NT_)::value;
    v4u af[2][NT], aa[2][NT];
    v2u fl = {(unsigned)tag, (unsigned)(tag >> 32)};
    const bool fme = li == 7 + w && g <= frows;
    auto a_load = [&](auto J_, auto T_) {
      constexpr int j = decltype(J_)::value, t = decltype(T_)::value;
      if constexpr (j < 4) ld128<j * 1024u>(af[j & 1][t], va[t], rws, aob);
      else if constexpr (j < 7) ld128<(j - 4) * 1024u>(af[j & 1][t], va[t], rws, aob + 4096u);
      else ld128<(j - 7) * 1024u>(aa[j - 7][t], va[t], rws, aoa);
    };
    __builtin_amdgcn_s_setprio(3);
    rep<0, 2 * NT>([&](auto I_) { constexpr int I = decltype(I_)::value; a_load(IC<I / NT>{}, IC<I % NT>{}); });
    f32x4 acc[4][NT];
    unsigned pa = 0;
    auto put = [&](int t) {
#pragma unroll
      for (int cp = 0; cp < 2; ++cp) {
        const f32x4 x = acc[2 * cp][t], y = acc[2 * cp + 1][t];
        *(LAS v4u*)(L + pa + (t * 2 + cp) * 1024u) = v4u{pk(x[0], x[1]), pk(x[2], x[3]), pk(y[0], y[1]), pk(y[2], y[3])};
      }
    };
    rep<0, 7>([&](auto J_) {
      constexpr int j = decltype(J_)::value;
      if constexpr (j == 2 || j == 4 || j == 6) __builtin_amdgcn_s_setprio(3 - j / 2);
      if constexpr (j == 5) {
        wait_for<p2_before(5, 0, NT) - p2_before(3, 1, NT)>(fl);
        const bool todo = __builtin_bit_cast(u64, fl) != tag;
        if (__any(todo)) poll(todo);
        SCHED();
      }
      if constexpr (j == 6) pa = opaque(wreg + (g * 16u + (((li >> 1) & 3u) | (((((li & 1u) | ((li >> 3) << 1)) + w) & 3u) << 2))) * 16u);
      if constexpr (j >= 4) wait_l();
      rep<0, NT>([&](auto T_) {
        constexpr int t = decltype(T_)::value, U = p2_unit(j, t, NT);
        if constexpr (U >= 0) b_m0(IC<U>{});
        wait_v<p2_before(j, t, NT) - p2_pos(j, t, NT) - 1>();
        const bf16x8 a = __builtin_bit_cast(bf16x8, af[j & 1][t]);
#pragma unroll
        for (int c = 0; c < 4; ++c) {
          if constexpr (j == 0) mma0(acc[c][t], cat(bl[0][c], bh[0][c]), a);
          else mma(acc[c][t], cat(bl[j & 3][c], bh[j & 3][c]), a);
        }
        SCHED();
        if constexpr (U >= 0) b_go(IC<U>{});
        if constexpr (j < 6) a_load(IC<j + 2>{}, T_);
        if constexpr (j == 5) a_load(IC<8>{}, T_);
        if constexpr (j == 5 && t == 0) ld32(gl, (RATIO ? 8 * w + 2 * g + (li >> 3) : lane) * 4u, rws, WS_G + w * 8192u + tt0 * 64u);
        if constexpr (j == 3 && t == 0) { if (fme) ld64(fl, lane * 64u, rws, frow); }
        if constexpr (j >= 2) {
          b_rd(IC<j + 2>{}, T_, IC<0>{});
          b_rd(IC<j + 2>{}, T_, IC<1>{});
          if constexpr (NT == 3 && t == 2) {
            b_rd(IC<j + 2>{}, IC<3>{}, IC<0>{});
            b_rd(IC<j + 2>{}, IC<3>{}, IC<1>{});
          }
        }
        if constexpr (j == 6) final_setup(T_);
        SCHED();
      });
      if constexpr (j == 0) STAMP(16);
      if constexpr (j == 2) STAMP(17);
      if constexpr (j == 4) STAMP(18);
      if constexpr (j == 6) STAMP(19);
    });
    constexpr int NALL = p2_before(6, 0, NT);
    float og[4];
    if (!RATIO) {
      wait_for<NALL - p2_before(5, 1, NT)>(gl);
      auto h2 = __builtin_amdgcn_permlane32_swap(__float_as_uint(gl), __float_as_uint(gl), false, false);
      auto lo2 = __builtin_amdgcn_permlane16_swap(h2[0], h2[0], false, false);
      auto hi2 = __builtin_amdgcn_permlane16_swap(h2[1], h2[1], false, false);
      og[0] = __uint_as_float(lo2[0]); og[1] = __uint_as_float(lo2[1]); og[2] = __uint_as_float(hi2[0]); og[3] = __uint_as_float(hi2[1]);
    }
    wait_l();
    STAMP(27);
    if (!RATIO) { if (w == 0) *(LAS float*)(L + SPARE + lane * 4u) = gl; }
    rep<0, NT>([&](auto T_) {
      constexpr int t = decltype(T_)::value;
      if (!RATIO) {
#pragma unroll
        for (int c = 0; c < 4; ++c) acc[c][t] *= og[t];
        SCHED();
        if constexpr (t >= 1) put(t - 1);
        SCHED();
      }
      wait_v<NALL - p2_pos(8, t, NT) - 1>();
      asm volatile("s_nop 0");
#pragma unroll
      for (int c = 0; c < 4; ++c) mma(acc[c][t], cat(bl[3][c], bh[3][c]), __builtin_bit_cast(bf16x8, aa[0][t]));
#pragma unroll
      for (int c = 0; c < 4; ++c) mma(acc[c][t], cat(bl[0][c], bh[0][c]), __builtin_bit_cast(bf16x8, aa[1][t]));
      SCHED();
      if constexpr (t >= 1 && RATIO) put(t - 1);
      final_setup(IC<4 + t>{});
      SCHED();
    });
    STAMP(7);
    asm volatile("s_nop 7" ::: "memory");
    SCHED();
    put(NT - 1);
  };
  if (qkv) phase2(IC<3>{});
  else phase2(IC<4>{});
  wait_l();
  bar();
  STAMP(8);
  if (!qkv || w < 6) {
    pin(gl);
    const float og = RATIO ? 1.0f : *(LAS float*)(L + ga2);
    bf16x8 pf[2][4];
#pragma unroll
    for (int u = 0; u < 4; ++u)
#pragma unroll
      for (int a = 0; a < 2; ++a) pf[a][u] = cat(ldstr(L, ra[u >> 1][0] + (u & 1) * 1024u + a * 256u), ldstr(L, ra[u >> 1][1] + (u & 1) * 1024u + a * 256u));
    f32x4 alo = blo * og, ahi = bhi * og;
#pragma unroll
    for (int u = 0; u < 4; ++u) {
      alo = mfma(pf[0][u], sel[u], alo);
      ahi = mfma(pf[1][u], sel[u], ahi);
    }
    if (RATIO) {
      alo *= gl;
      ahi *= gl;
    }
    const v4u o = {pk(alo[0], alo[1]), pk(alo[2], alo[3]), pk(ahi[0], ahi[1]), pk(ahi[2], ahi[3])};
    *(GAS v4u*)((char*)p.out + oo) = o;
  }
  STAMP(9);
}
}

extern "C" void kernel_launch(void* const* d_in, const int* in_sizes, int n_in, void* d_out, int out_size, void* d_ws, size_t ws_size,
                              hipStream_t stream) {
  gm::Params p;
  p.x = d_in[0];
  p.wqkv = d_in[1];
  p.wo = d_in[2];
  p.w1 = d_in[4];
  p.b1 = d_in[5];
  p.w2 = d_in[6];
  p.b2 = d_in[7];
  p.gw = d_in[8];
  p.out = d_out;
  p.ws = d_ws;
  gm::gm_kernel<<<256, 512, 0, stream>>>(p);
}
```

```cpp
#include <hip/hip_runtime.h>
#include <stdint.h>

typedef short bf16x8 __attribute__((ext_vector_type(8)));
typedef short s16x4 __attribute__((ext_vector_type(4)));
typedef float f32x4 __attribute__((ext_vector_type(4)));
typedef unsigned v4u __attribute__((ext_vector_type(4)));
typedef unsigned v2u __attribute__((ext_vector_type(2)));
typedef __bf16 bf2_t __attribute__((ext_vector_type(2)));
typedef float f32x2 __attribute__((ext_vector_type(2)));
typedef unsigned long long u64;
typedef u64 v2q __attribute__((ext_vector_type(2)));
typedef __amdgpu_buffer_rsrc_t rsrc_t;
#define LAS __attribute__((address_space(3)))
#define GAS __attribute__((address_space(1)))
#define DI __device__ __forceinline__

#define CODE_KB 23
#define NMID 4
#define T2HB 3
#define TMODE 1
#define TCNT 0x0C31151C38101520ull
#define TFIRST 0x00003F5400000000ull
#define TSH 2u
#define FROW 4352u
#define FSEC 256u
#define RATIO 1
#define FREE_SLEEP 0

extern "C" __device__ u64 gm_dispatch_id() __asm("llvm.amdgcn.dispatch.id");

namespace gm {
constexpr int S = 2048, D = 448, F = 1792, NQKV = 1344;
constexpr int KS_A = 16, KS = 72;
constexpr unsigned A2_TT = KS * 1024;
constexpr unsigned WS_G = 128 * A2_TT, WS_F = WS_G + 8 * 8192, WS_END = WS_F + 16 * FROW;
constexpr unsigned REG = 20480, STG = 6144, SPARE = 3 * STG;
constexpr int LDS_BYTES = 163840;
constexpr unsigned GW_OFF = 2 * REG + 5120;
constexpr unsigned SPIN_CAP = 1u << 16;
constexpr int TOUCH2_HB = T2HB;

struct Params {
  const void *x, *wqkv, *wo, *w1, *b1, *w2, *b2, *gw;
  void *out, *ws;
};
template <int N> struct IC { static constexpr int value = N; };
template <int I, int N, class Fn> DI void rep(Fn&& f) {
  if constexpr (I < N) {
    f(IC<I>{});
    rep<I + 1, N>(f);
  }
}

#define SCHED() __builtin_amdgcn_sched_barrier(0)
#define NOP1() asm volatile("s_nop 0")
DI rsrc_t mk(const void* p, unsigned n) { return __builtin_amdgcn_make_buffer_rsrc((void*)p, 0, n, 0x00020000); }
template <unsigned C> DI void set_m0(unsigned base) { asm volatile("s_add_u32 m0, %0, %1" ::"s"(base), "i"(C) : "scc", "memory"); }
template <unsigned IMM> DI void dma_go(unsigned voff, rsrc_t rs, unsigned soff) {
  asm volatile("buffer_load_dwordx4 %0, %1, %2 offen offset:%3 lds" ::"v"(voff), "s"(rs), "s"(soff), "i"(IMM) : "memory");
}
template <unsigned IMM> DI void dma_go0(unsigned voff, rsrc_t rs) {
  asm volatile("buffer_load_dwordx4 %0, %1, 0 offen offset:%2 lds" ::"v"(voff), "s"(rs), "i"(IMM) : "memory");
}
DI void touch(unsigned lds, unsigned voff, u64 base) {
  asm volatile("s_mov_b32 m0, %0\n\ts_nop 0\n\tglobal_load_lds_dword %1, %2" ::"s"(lds), "v"(voff), "s"(base) : "memory");
}
template <unsigned IMM> DI void ld128(v4u& d, unsigned voff, rsrc_t rs, unsigned soff) {
  asm volatile("buffer_load_dwordx4 %0, %1, %2, %3 offen offset:%4 sc1" : "=v"(d) : "v"(voff), "s"(rs), "s"(soff), "i"(IMM) : "memory");
}
DI void ld32(float& d, unsigned voff, rsrc_t rs, unsigned soff) {
  asm volatile("buffer_load_dword %0, %1, %2, %3 offen sc1" : "=v"(d) : "v"(voff), "s"(rs), "s"(soff) : "memory");
}
DI void ld64(v2u& d, unsigned voff, rsrc_t rs, unsigned soff) {
  asm volatile("buffer_load_dwordx2 %0, %1, %2, %3 offen sc1" : "+v"(d) : "v"(voff), "s"(rs), "s"(soff) : "memory");
}
DI void st128(v4u v, rsrc_t rs, unsigned voff, unsigned soff) {
  __builtin_amdgcn_raw_buffer_store_b128(v, rs, voff, soff, 16);
  SCHED();
  asm volatile("s_nop 1");
  SCHED();
}
template <int N> DI void wait_v() { asm volatile("s_waitcnt vmcnt(%0)" ::"i"(N) : "memory"); }
template <int N, class T> DI void wait_for(T& v) { asm volatile("s_waitcnt vmcnt(%1)" : "+v"(v) : "i"(N) : "memory"); }
DI void wait_l() {
  SCHED();
  __builtin_amdgcn_s_waitcnt(0xC07F);
  asm volatile("" ::: "memory");
  SCHED();
}
DI void bar() { asm volatile("s_barrier" ::: "memory"); }
DI f32x4 mfma(bf16x8 a, bf16x8 b, f32x4 c) { return __builtin_amdgcn_mfma_f32_16x16x32_bf16(a, b, c, 0, 0, 0); }
DI void mma(f32x4& c, bf16x8 a, bf16x8 b) { asm volatile("v_mfma_f32_16x16x32_bf16 %0, %1, %2, %0" : "+v"(c) : "v"(a), "v"(b)); }
DI void mma0(f32x4& c, bf16x8 a, bf16x8 b) { asm volatile("v_mfma_f32_16x16x32_bf16 %0, %1, %2, 0" : "=&v"(c) : "v"(a), "v"(b)); }
DI void mmac(f32x4& d, bf16x8 a, bf16x8 b, f32x4 c) { asm volatile("v_mfma_f32_16x16x32_bf16 %0, %1, %2, %3" : "=&v"(d) : "v"(a), "v"(b), "v"(c)); }
DI float relu(float v) {
  float r;
  asm("v_max_f32 %0, 0, %1" : "=v"(r) : "v"(v));
  return r;
}
DI float fmx(float a, float b) {
  float r;
  asm("v_max_f32 %0, %1, %2" : "=v"(r) : "v"(a), "v"(b));
  return r;
}
DI unsigned pk(float lo, float hi) {
  f32x2 v = {lo, hi};
  return __builtin_bit_cast(unsigned, __builtin_convertvector(v, bf2_t));
}
DI float rbf(float f) { return __uint_as_float(pk(f, 0.f) << 16); }
DI float bflo(unsigned u) { return __uint_as_float(u << 16); }
DI float bfhi(unsigned u) { return __uint_as_float(u & 0xffff0000u); }
DI bf16x8 asfrag(unsigned a, unsigned b, unsigned c, unsigned d) {
  v4u v = {a, b, c, d};
  return __builtin_bit_cast(bf16x8, v);
}
DI bf16x8 cat(s16x4 lo, s16x4 hi) { return __builtin_shufflevector(lo, hi, 0, 1, 2, 3, 4, 5, 6, 7); }
DI bf16x8 lds128(LAS char* L, unsigned a) { return *(LAS bf16x8*)(L + a); }
DI s16x4 ldstr(LAS char* L, unsigned a) { return __builtin_amdgcn_ds_read_tr16_b64_v4i16((LAS s16x4*)(L + a)); }
DI unsigned opaque(unsigned v) {
  asm volatile("" : "+v"(v));
  return v;
}
DI unsigned sopaque(unsigned v) {
  asm volatile("" : "+s"(v));
  return v;
}
template <class T> DI void pin(T& v) { asm volatile("" : "+v"(v)); }
DI void keep(f32x4 v) { asm volatile("" ::"v"(v)); }

#define STAMP(k)
#define FSTAMP(k)

constexpr unsigned hloc(int h) {
  h = h >= 10 ? h - 10 : h;
  return h == 0 ? SPARE : (h < 4 ? STG + (h - 1) * 2048u : (h < 7 ? 2 * STG + (h - 4) * 2048u : (h - 7) * 2048u));
}
constexpr unsigned ppos(int i) { return i < 2 ? i + 1 : (i == 2 ? 0 : i); }

constexpr int q_item(int i, bool ffn) { return i < 36 ? 8 * (1 + i / 6) + (ffn ? i % 6 : (i % 6 + 5) % 6) : 100 + (i - 36); }
constexpr int q_from(int item) {
  if (item < 100) return (item >> 3) <= 2 ? 0 : 2 * ((item >> 3) - 3) + 1;
  const int h = (item - 100) >> 1;
  return h == 0 ? 1 : (h < 4 ? 9 : (h < 7 ? 11 : 13));
}
constexpr int q_start(int hb) {
  int i = 6;
  for (int h = 0; h < hb; ++h)
    for (int k = 0; k < 4; ++k)
      if (i < 52 && q_from(q_item(i, true)) <= h) ++i;
  return i;
}
constexpr int q_at(int hb, int i, bool ffn) {
  return (i & 3) == 1 && q_start(hb) + (i >> 2) < q_start(hb + 1) ? q_item(q_start(hb) + (i >> 2), ffn) : -1;
}
constexpr int q_count(int from, int to, bool ffn) {
  int n = 0;
  for (int i = from; i < to; ++i) n += (ffn || i >= 36 || i % 6 != 0) ? 1 : 0;
  return n;
}
constexpr int q_wait(int s, bool ffn) { return q_count(s < 6 ? 6 * (s + 1) : 44, q_start(2 * s + 1), ffn); }
static_assert(q_start(14) == 52 && q_start(3) >= 12 && q_start(5) >= 18 && q_start(7) >= 24 && q_start(9) >= 30 && q_start(11) >= 36 &&
                  q_start(13) >= 44 && q_start(13) - q_start(12) <= 2 && q_start(14) - q_start(13) <= 2,
              "queue order");
constexpr int r_unit(int i) { return i < 2 ? 16 + i : (i < 6 ? 18 + i : 12 + i); }

constexpr int p2_unit(int j, int t, int nt) { return nt == 3 && 3 * j + t < 12 ? 24 + 3 * j + t : -1; }
constexpr int p2_after(int j, int t, int nt) {
  return (p2_unit(j, t, nt) >= 0 ? 1 : 0) + (j < 5 ? 1 : (j == 5 ? 2 : 0))
         + (j == 5 && t == 0 ? 1 : 0)
         + (j == 3 && t == 0 ? 1 : 0);
}
constexpr int p2_before(int j, int t, int nt) {
  int n = 2 * nt;
  for (int i = 0; i < nt * j + t; ++i) n += p2_after(i / nt, i % nt, nt);
  return n;
}
constexpr int p2_pos(int j, int t, int nt) {
  return j < 2 ? nt * j + t : (j == 8 ? p2_before(5, t, nt) + 1 : p2_before(j - 2, t, nt) + (p2_unit(j - 2, t, nt) >= 0 ? 1 : 0));
}

__global__ __launch_bounds__(512, 2) void gm_kernel(Params p) {
  __shared__ __attribute__((aligned(1024))) char smem[LDS_BYTES];
  LAS char* L = (LAS char*)smem;
  u64 px, pw, pv, pt;
  asm volatile("s_load_dwordx2 %0, %1, 0x0" : "=&s"(px) : "s"(__builtin_amdgcn_kernarg_segment_ptr()));
  SCHED();
  const unsigned tid = threadIdx.x, lane = tid & 63;
  const unsigned b = blockIdx.x, xcd = b & 7, slot = b >> 3;
  const bool ffn = b < 112, qkv = b >= 128;
  const unsigned w = __builtin_amdgcn_readfirstlane(tid >> 6);
  asm volatile("s_load_dwordx2 %0, %3, %4\n\ts_load_dwordx2 %1, %3, %5\n\ts_load_dwordx2 %2, %3, %6"
               : "=&s"(pw), "=&s"(pv), "=&s"(pt)
               : "s"(__builtin_amdgcn_kernarg_segment_ptr()), "s"(qkv ? 8u : 24u), "s"(qkv ? 56u : 32u), "s"(((0x25130130u >> (4 * w)) & 7u) * 8u));
  if (tid < CODE_KB) {
    u64 pc;
    asm volatile("s_getpc_b64 %0" : "=s"(pc));
    touch(SPARE + 1024u, ((slot & 7) * CODE_KB + tid) * 128u, pc);
  }
  const unsigned wreg = w * REG;
  const unsigned li = lane & 15, g = lane >> 4, rr = lane >> 3, pos = lane & 7;
  const unsigned rq = xcd >> 1, side = xcd & 1;
  const unsigned tcnt = (unsigned)(TCNT >> (8 * w)) & 0xffu;
  const unsigned tfirst = ((unsigned)(TFIRST >> (8 * w)) & 0xffu) * 64u;
  const unsigned tsh = w ? 6u : TSH;
  const unsigned tln = opaque(((32 * xcd + slot) * (w ? tcnt : 56u) + tfirst + (lane >> tsh) * 7u + (lane & ((1u << tsh) - 1u))) * 128u);
  auto do_touch = [&]() {
    if (lane < tcnt) touch(wreg + SPARE + 1024u, tln, pt);
  };
  const bool mid3 = (slot >> 2) == 3;
  const unsigned rb = 4 * rq + (mid3 ? 2 * side + (slot & 1) : slot & 3), cs = mid3 ? 3u : side * 4 + ((slot >> 2) & 3);
  unsigned vx[2];
#pragma unroll
  for (int h = 0; h < 2; ++h) vx[h] = opaque((128 * rb + 16 * w + 8 * h + rr) * 896u + ((pos ^ (4u * h + (rr >> 1))) << 4));
  const unsigned vw = opaque((8 * w + rr) * (qkv ? 2688u : 3584u) + (min(pos ^ ((((rr >> 1) & 1) | ((w & 1) << 1)) << 1), qkv ? 6u : 7u) << 4) +
                             cs * (qkv ? 112u : 512u));
  SCHED();
  asm volatile("s_waitcnt lgkmcnt(0)" : "+s"(px), "+s"(pw), "+s"(pv), "+s"(pt)::"memory");
  SCHED();
  STAMP(10);
  const rsrc_t rx = mk((const void*)px, S * D * 2);
  const rsrc_t rw = mk((const void*)pw, qkv ? D * NQKV * 2 : D * F * 2);
  if (w < 3) do_touch();
  if (ffn || qkv) {
    const unsigned wus = qkv ? 896u : 128u;
    rep<0, 5>([&](auto I_) {
      constexpr int I = decltype(I_)::value;
      set_m0<ppos(I) * 1024u>(wreg);
      NOP1();
      if constexpr (I < 2) dma_go0<0>(vx[I], rx);
      else dma_go<0>(vw, rw, (I - 2) * wus);
    });
  }
  SCHED();

  rsrc_t rws, rw2, rwo;
  unsigned vb[2], ba[4], fo_pub, ct = 0, tt0 = 0;
  const unsigned bo2 = w * (224u * 896u), boo = w * (56u * 896u);
  const unsigned q4 = (lane >> 2) & 3, p4 = lane & 3;
  const unsigned fsw = (q4 >> 1) | ((g & 1) << 1);
  s16x4 bl[4][4], bh[4][4];
  auto b_m0 = [&](auto N_) {
    constexpr int N = decltype(N_)::value;
    set_m0<hloc(N >> 1) + (N & 1) * 1024u>(wreg);
  };
  auto b_go = [&](auto N_) {
    constexpr int N = decltype(N_)::value, j = N >> 2;
    if constexpr (j < 7) dma_go<0>(vb[N & 1], rw2, bo2 + (N >> 1) * (16u * 896u));
    else dma_go<0>(vb[N & 1], rwo, boo + ((j - 7) * 28u + ((N >> 1) & 1) * 16u) * 896u);
  };
  auto b_dma = [&](auto N_) { b_m0(N_); NOP1(); b_go(N_); };
  auto r_dma = [&](auto I_) { b_dma(IC<r_unit(decltype(I_)::value)>{}); };
  auto b_rd = [&](auto J_, auto C_, auto E_) {
    constexpr int J = decltype(J_)::value, C = decltype(C_)::value, E = decltype(E_)::value;
    if constexpr (E == 0) bl[J & 3][C] = ldstr(L, ba[C] + hloc(2 * J));
    else bh[J & 3][C] = ldstr(L, ba[C] + hloc(2 * J + 1));
  };
  auto rd_step = [&](auto J_) { rep<0, 8>([&](auto I_) { constexpr int I = decltype(I_)::value; b_rd(J_, IC<(I >> 1)>{}, IC<(I & 1)>{}); }); };
  auto setup2 = [&](auto K_, auto Q_) {
    constexpr int K = decltype(K_)::value;
    constexpr bool Q = decltype(Q_)::value;
    if constexpr (K == 0) {
      ct = Q ? (slot >> 2) - 1 : (unsigned)max((int)(slot >> 2), (int)slot - 9);
      tt0 = 16 * xcd + (Q ? 4 + 3 * (slot & 3) : (slot < 12 ? 4 * (slot & 3) : 0u));
      rws = mk(p.ws, WS_END);
      rw2 = mk(p.w2, F * D * 2);
      rwo = mk(p.wo, D * D * 2);
    } else if constexpr (K == 2) {
      vb[0] = opaque(rr * 896u + ((pos ^ (((rr >> 1) & 3) << 1)) << 4) + ct * 128u);
      vb[1] = opaque(vb[0] + 8u * 896u);
    } else if constexpr (K >= 3 && K < 7) {
      ba[K - 3] = opaque(wreg + (4 * g + q4) * 128u + ((2u * ((K - 3) ^ fsw) + (p4 >> 1)) << 4) + (p4 & 1) * 8u);
    }
  };

  auto phase1 = [&](auto FFN_) {
    constexpr bool FFN = decltype(FFN_)::value;
    constexpr unsigned LDW = FFN ? 3584u : 2688u, WUS = FFN ? 128u : 896u;
    unsigned vh5 = 0;
    if (!FFN) vh5 = opaque(((rb ? 128 * rb - 16 : 0u) + 8 * (w & 1) + rr) * 896u + ((pos ^ (4u * (w & 1) + (rr >> 1))) << 4));
    auto d_m0 = [&](auto E_) {
      constexpr int E = decltype(E_)::value, I = E & 7, ST = E >> 3;
      if constexpr (E >= 100) b_m0(IC<E - 100>{});
      else if constexpr (E >= 0) set_m0<(ST % 3) * STG + ppos(I) * 1024u - ((I < 2 || (!FFN && I == 5)) ? ST * 128u : (I - 2) * WUS)>(wreg);
    };
    auto d_go = [&](auto E_) {
      constexpr int E = decltype(E_)::value, I = E & 7, ST = E >> 3;
      if constexpr (E >= 100) b_go(IC<E - 100>{});
      else if constexpr (E >= 0) {
        if constexpr (I < 2) dma_go0<ST * 128u>(vx[I], rx);
        else if constexpr (!FFN && I == 5) { if (w < 2) dma_go0<ST * 128u>(vh5, rx); }
        else if constexpr (ST == 0) dma_go0<(I - 2) * WUS>(vw, rw);
        else dma_go<(I - 2) * WUS>(vw, rw, ST * 64u * LDW);
      }
    };
    d_m0(IC<5>{});
    NOP1();
    d_go(IC<5>{});
    if (w == 2) {
      set_m0<FFN ? SPARE : GW_OFF>(0);
      NOP1();
      dma_go0<0>(lane * 16, mk((const char*)pv + (FFN ? cs * 512u : 0u), FFN ? 512 : 896));
    }
    SCHED();
    STAMP(11);
    const unsigned xa0 = 1024u + (li >> 3) * 1024u + (li & 7) * 128u + ((g ^ ((li >> 1) & 7)) << 4);
    auto second_stage = [&]() {
      SCHED();
      STAMP(12);
      bar();
      rep<0, 6>([&](auto I_) { constexpr int E = q_item(decltype(I_)::value, FFN); d_m0(IC<E>{}); NOP1(); d_go(IC<E>{}); });
      STAMP(14);
      if (w >= 3 && w < 6) {
        do_touch();
        wait_v<q_count(0, 6, FFN) + 1>();
      } else {
        wait_v<q_count(0, 6, FFN)>();
      }
      bar();
      STAMP(1);
    };
    auto mid = [&](auto S_) {
      constexpr int s = decltype(S_)::value;
      constexpr bool t2 = TOUCH2_HB <= 2 * s && q_start(TOUCH2_HB + 1) >= 6 * (s + 1);
      if ((s == 0 && w >= 3 && w < 6) || (t2 && w >= 6)) wait_v<q_wait(s, FFN) + 1>();
      else wait_v<q_wait(s, FFN)>();
      wait_l();
      bar();
      FSTAMP(20 + s);
    };

    if constexpr (FFN) {
      const unsigned mh = w >> 2, nq = w & 3;
      unsigned xa[2][2], wa[2][4];
#pragma unroll
      for (int hs = 0; hs < 2; ++hs) {
#pragma unroll
        for (int t2 = 0; t2 < 2; ++t2) xa[hs][t2] = opaque((xa0 ^ (hs * 64u)) + (mh * 4u + t2 * 2u) * REG);
#pragma unroll
        for (int c = 0; c < 4; ++c)
          wa[hs][c] = opaque((4u * hs + g) * REG + (nq ? 2u + nq : 0u) * 1024u + q4 * 128u + ((2u * (c ^ fsw) + (p4 >> 1)) << 4) + (p4 & 1) * 8u);
      }
      second_stage();
      bf16x8 xf[2][4];
      s16x4 wl[2][4], wh[2][4];
      f32x4 acc[4][4];
      auto rd = [&](auto I_, auto HB_) {
        constexpr int I = decltype(I_)::value, HB = decltype(HB_)::value, hs = HB & 1;
        constexpr unsigned bo = ((HB >> 1) % 3) * STG;
        if constexpr (I >= 2 && I < 6) xf[hs][I - 2] = lds128(L, xa[hs][(I - 2) >> 1] + ((I - 2) & 1) * REG + bo);
        else if constexpr (I < 12) {
          constexpr int c = I < 2 ? 0 : (I - 4) >> 1;
          if constexpr ((I & 1) == 0) wl[hs][c] = ldstr(L, wa[hs][c] + bo);
          else wh[hs][c] = ldstr(L, wa[hs][c] + bo + 512u);
        }
      };
      f32x4 bv[4];
#pragma unroll
      for (int c = 0; c < 4; ++c) {
        const v2u bb = *(LAS v2u*)(L + SPARE + nq * 128u + c * 32u + g * 8u);
        bv[c] = f32x4{bflo(bb.x), bfhi(bb.x), bflo(bb.y), bfhi(bb.y)};
      }
      rep<0, 12>([&](auto I_) { rd(I_, IC<0>{}); });
#pragma unroll
      for (int c = 0; c < 4; ++c) pin(bv[c]);
      unsigned so = 0;
      auto epi = [&](int t, int u) {
        float hv[8];
#pragma unroll
        for (int j = 0; j < 8; ++j) {
          const float r = relu(acc[2 * u + (j >> 2)][t][j & 3]);
          hv[j] = r * r;
        }
        const v4u o = {pk(hv[0], hv[1]), pk(hv[2], hv[3]), pk(hv[4], hv[5]), pk(hv[6], hv[7])};
        st128(o, rws, lane * 16u + u * 1024u, so + t * A2_TT);
      };
      rep<0, 12>([&](auto HB_) {
        constexpr int HB = decltype(HB_)::value, hs = HB & 1;
        if constexpr (HB == 0) asm volatile("s_nop 1");
        else if constexpr (hs == 0) wait_l();
        rep<0, 16>([&](auto I_) {
          constexpr int I = decltype(I_)::value;
          constexpr int E = q_at(HB, I, FFN);
          d_m0(IC<E>{});
          if constexpr (HB == 0) mmac(acc[I >> 2][I & 3], cat(wl[hs][I >> 2], wh[hs][I >> 2]), xf[hs][I & 3], bv[I >> 2]);
          else mma(acc[I >> 2][I & 3], cat(wl[hs][I >> 2], wh[hs][I >> 2]), xf[hs][I & 3]);
          SCHED();
          rd(I_, IC<HB + 1>{});
          if constexpr (I == 14 && HB < 7) setup2(HB_, IC<0>{});
          if constexpr (I == 14 && HB == 7) {
            so = sopaque((8 * rb + 4 * mh) * A2_TT + (KS_A + 8 * cs + 2 * nq) * 1024u);
            fo_pub = sopaque(WS_F + rb * FROW + cs * FSEC);
          }
          if constexpr (I == 15 && HB == TOUCH2_HB) { if (w >= 6) do_touch(); }
          SCHED();
          d_go(IC<E>{});
        });
        if constexpr (hs == 0) mid(IC<(HB >> 1)>{});
      });
      wait_l();
      rep<0, 32>([&](auto M_) {
        constexpr int M = decltype(M_)::value, G = M >> 2, c = M & 3;
        constexpr int hs = G < 4 ? G >> 1 : G & 1, t = G < 4 ? G & 1 : G >> 1;
        constexpr int E = M < 8 ? q_at(12, M, FFN) : (M < 16 ? q_at(13, M - 8, FFN) : -1);
        d_m0(IC<E>{});
        mma(acc[c][t], cat(wl[hs][c], wh[hs][c]), xf[hs][t]);
        SCHED();
        if constexpr (M < 6) { rd(IC<2 * M>{}, IC<13>{}); rd(IC<2 * M + 1>{}, IC<13>{}); }
        if constexpr (M >= 8 && M < 14) {
          rep<0, (M < 12 ? 3 : 2)>([&](auto Q_) {
            constexpr int Q = (M < 12 ? 3 * (M - 8) : 12 + 2 * (M - 12)) + decltype(Q_)::value;
            b_rd(IC<(Q >> 3)>{}, IC<((Q >> 1) & 3)>{}, IC<(Q & 1)>{});
          });
        }
        if constexpr (M >= 14 && M < 32 && (M - 14) % 3 == 0) epi((M - 14) / 6, ((M - 14) / 3) & 1);
        SCHED();
        d_go(IC<E>{});
        if constexpr (M == 7) mid(IC<6>{});
      });
      STAMP(2);
      asm volatile("s_nop 5" ::: "memory");
      SCHED();
      epi(3, 0);
      epi(3, 1);
    } else {
      const unsigned tq = w & 3, hd = w >> 2, hp = cs;
      unsigned xa[2], wa[2][2], xh[2], wq[2];
#pragma unroll
      for (int hs = 0; hs < 2; ++hs) {
        xa[hs] = opaque((xa0 ^ (hs * 64u)) + tq * 2u * REG);
#pragma unroll
        for (int c = 0; c < 2; ++c) {
          const unsigned bo = 56u * hd + 32u * c + 8u * p4;
          wa[hs][c] = opaque((4u * hs + g) * REG + q4 * 128u + (((bo >> 4) ^ (fsw << 1)) << 4) + (bo & 8u));
        }
        xh[hs] = opaque((li >> 3) * REG + 5120u + (li & 7) * 128u + (((4u * hs + g) ^ ((li >> 1) & 7)) << 4));
        const unsigned bo = 56u * hd + 32u * (tq & 1) + 8u * p4;
        wq[hs] = opaque((4u * hs + g) * REG + q4 * 128u + (((bo >> 4) ^ (fsw << 1)) << 4) + (bo & 8u) + (3u + (tq >> 1)) * 1024u);
      }
      const unsigned ga = opaque(GW_OFF + g * 16u);
      second_stage();
      float mb[2][4];
      unsigned oa = 0, hwa = 0, nwa = 0, pm = 0;
      auto setupq = [&](auto K_) {
        constexpr int K = decltype(K_)::value, t = K & 1;
        if constexpr (K < 2) {
#pragma unroll
          for (int j = 0; j < 4; ++j) {
            const int kap = 16 * (j >> 1) + (int)(4 * g) + 2 * (j & 1) + (int)(li & 1) - 16;
            const int dl = (int)li - kap;
            const bool ok = (dl >= 0) && (dl <= 14) && (t == 1 || j >= 2 || (rb | tq) != 0);
            mb[t][j] = __uint_as_float(opaque(ok ? 0u : 0xff800000u));
          }
        } else if constexpr (K == 2) {
          oa = sopaque((8 * rb + 2 * tq) * A2_TT + (2 * hp + hd) * 1024u);
          fo_pub = sopaque(WS_F + rb * FROW + (7 + hp) * FSEC);
        } else if constexpr (K == 3) {
          hwa = opaque(hd * 4u * REG + (tq < 2 ? 4096u + lane * 16u + tq * 8u : 4096u + tq * 512u + li * 32u + g * 8u));
          nwa = opaque(wreg + (tq < 3 ? REG + 4096u : 2048u) + lane * 16u);
          pm = opaque(g == 3 ? 0u : 0xffffffffu);
        }
      };
      f32x4 ac[7][2], ah;
      bf16x8 xf[2][2], gf[2], hf[2];
      s16x4 wl[2][6], wh[2][6], hl[2], hh[2];
      auto rd = [&](auto I_, auto HB_) {
        constexpr int I = decltype(I_)::value, HB = decltype(HB_)::value, hs = HB & 1;
        constexpr unsigned bo = ((HB >> 1) % 3) * STG;
        if constexpr (I == 2 || I == 3) xf[hs][I - 2] = lds128(L, xa[hs] + (I - 2) * REG + bo);
        else if constexpr (I < 14) {
          constexpr int c = I < 2 ? 0 : (I - 2) >> 1;
          if constexpr ((I & 1) == 0) wl[hs][c] = ldstr(L, wa[hs][c & 1] + ppos(2 + (c >> 1)) * 1024u + bo);
          else wh[hs][c] = ldstr(L, wa[hs][c & 1] + ppos(2 + (c >> 1)) * 1024u + bo + 512u);
        } else if constexpr (I == 14) gf[hs] = lds128(L, ga + HB * 64u);
        else if constexpr (I == 15) hf[hs] = lds128(L, xh[hs] + bo);
        else if constexpr (I == 16) hl[hs] = ldstr(L, wq[hs] + bo);
        else if constexpr (I == 17) hh[hs] = ldstr(L, wq[hs] + bo + 512u);
      };
      bf16x8 qf[2], kf[2], kp;
      unsigned vh[2][2][2];
      float gv[2], og[2];
      v4u vp;
      auto pack_kv = [&](int t) {
        kf[t] = asfrag(pk(ac[2][t][0], ac[2][t][1]), pk(ac[2][t][2], ac[2][t][3]), pk(ac[3][t][0], ac[3][t][1]), pk(ac[3][t][2], ac[3][t][3]));
#pragma unroll
        for (int c = 0; c < 2; ++c) {
          vh[t][c][0] = pk(ac[4 + c][t][0], ac[4 + c][t][1]);
          vh[t][c][1] = pk(ac[4 + c][t][2], ac[4 + c][t][3]);
        }
      };
      auto pack_q = [&](int t) {
        qf[t] = asfrag(pk(ac[0][t][0], ac[0][t][1]), pk(ac[0][t][2], ac[0][t][3]), pm & pk(ac[1][t][0], ac[1][t][1]), pm & pk(ac[1][t][2], ac[1][t][3]));
        pin(qf[t]);
      };
      rep<0, 18>([&](auto I_) { rd(I_, IC<0>{}); });
      rep<0, 14>([&](auto HB_) {
        constexpr int HB = decltype(HB_)::value, hs = HB & 1;
        if constexpr (hs == 0 && HB > 0) wait_l();
        rep<0, 15>([&](auto I_) {
          constexpr int I = decltype(I_)::value;
          constexpr int c = HB < 13 ? I >> 1 : (I == 0 ? 7 : (I < 5 ? 1 + I : (I < 7 ? 6 : (I < 11 ? (I - 7) & 1 : I - 9))));
          constexpr int t = HB < 13 ? I & 1 : (I < 5 ? 1 : (I < 7 ? I - 5 : (I < 11 ? 1 - ((I - 7) >> 1) : 0)));
          constexpr int E = q_at(HB, I, FFN);
          d_m0(IC<E>{});
          if constexpr (c == 7) {
            if constexpr (HB == 0) mma0(ah, cat(hl[hs], hh[hs]), hf[hs]);
            else mma(ah, cat(hl[hs], hh[hs]), hf[hs]);
          } else {
            const bf16x8 wf = c < 6 ? cat(wl[hs][c < 6 ? c : 0], wh[hs][c < 6 ? c : 0]) : gf[hs];
            if constexpr (HB == 0) {
              if constexpr (c == 4 || c == 5) mma0(ac[c][t], xf[hs][t], wf);
              else mma0(ac[c][t], wf, xf[hs][t]);
            } else {
              if constexpr (c == 4 || c == 5) mma(ac[c][t], xf[hs][t], wf);
              else mma(ac[c][t], wf, xf[hs][t]);
            }
          }
          SCHED();
          if constexpr (HB < 13) {
            if constexpr (I < 7) { rd(IC<2 * I>{}, IC<HB + 1>{}); rd(IC<2 * I + 1>{}, IC<HB + 1>{}); }
            else rd(IC<I + 7>{}, IC<HB + 1>{});
            if constexpr (I == 12 && HB < 7) setup2(HB_, IC<1>{});
            if constexpr (I == 12 && HB >= 7 && HB < 11) setupq(IC<HB - 7>{});
            if constexpr (I == 14 && HB == TOUCH2_HB) { if (w >= 6) do_touch(); }
          } else {
            if constexpr (I < 8) {
              b_rd(IC<(I >> 2)>{}, IC<(I & 3)>{}, IC<0>{});
              b_rd(IC<(I >> 2)>{}, IC<(I & 3)>{}, IC<1>{});
            }
            if constexpr (I == 5) {
              *(LAS v2u*)(L + hwa) = v2u{pk(ah[0], ah[1]), pk(ah[2], ah[3])};
            }
            if constexpr (I == 8) {
              pack_kv(1);
              *(LAS bf16x8*)(L + nwa) = kf[1];
              *(LAS v4u*)(L + nwa + 1024u) = v4u{vh[1][0][0], vh[1][0][1], vh[1][1][0], vh[1][1][1]};
            }
            if constexpr (I == 10) {
#pragma unroll
              for (int t2 = 0; t2 < 2; ++t2) {
                gv[t2] = rbf(__builtin_amdgcn_rcpf(1.0f + __builtin_amdgcn_exp2f(rbf(ac[6][t2][0]) * -1.4426950408889634f)));
                og[t2] = rbf(1.0f - gv[t2]);
                if (RATIO) og[t2] = fmaxf(og[t2], 9.094947017729282e-13f);
                keep(ac[6][t2]);
                pin(gv[t2]);
                pin(og[t2]);
              }
            }
            if constexpr (I == 11) {
              wait_l();
              bar();
              kp = lds128(L, wreg + 4096u + lane * 16u);
              if (tq == 0) vp = __builtin_bit_cast(v4u, cat(ldstr(L, wreg + 5120u + lane * 8u), ldstr(L, wreg + 5632u + lane * 8u)));
              else vp = *(LAS v4u*)(L + wreg + 5120u + lane * 16u);
              pack_q(1);
            }
            if constexpr (I == 13) pack_q(0);
          }
          SCHED();
          d_go(IC<E>{});
        });
        if constexpr (hs == 0) mid(IC<(HB >> 1)>{});
      });
      STAMP(2);
      const f32x4 z = {0.f, 0.f, 0.f, 0.f};
      f32x4 s0[2], s1[2];
      s1[1] = mfma(kf[1], qf[1], z);
      s0[0] = mfma(kp, qf[0], z);
      SCHED();
      asm volatile("s_nop 4");
      SCHED();
      pack_kv(0);
      s0[1] = mfma(kf[0], qf[1], z);
      s1[0] = mfma(kf[0], qf[0], z);
      const bool odd = li & 1;
      const unsigned psh = odd ? 16u : 0u;
      float sv[2][4];
      auto pick = [&](f32x4 v, int u) {
        float lo = v[2 * u], hi = v[2 * u + 1];
        asm("" : "+v"(hi));
        return odd ? hi : lo;
      };
      const float SC = 0.18898223650461362f * 1.4426950408889634f;
#pragma unroll
      for (int u = 0; u < 2; ++u) {
        sv[1][2 + u] = __builtin_fmaf(pick(s1[1], u), SC, mb[1][2 + u]);
        sv[0][u] = __builtin_fmaf(pick(s0[0], u), SC, mb[0][u]);
      }
#pragma unroll
      for (int u = 0; u < 2; ++u) {
        sv[1][u] = __builtin_fmaf(pick(s0[1], u), SC, mb[1][u]);
        sv[0][2 + u] = __builtin_fmaf(pick(s1[0], u), SC, mb[0][2 + u]);
      }
      float m[2];
      {
        const float m0 = fmaxf(fmaxf(sv[0][0], sv[0][1]), fmaxf(sv[0][2], sv[0][3])), m1 = fmaxf(fmaxf(sv[1][0], sv[1][1]), fmaxf(sv[1][2], sv[1][3]));
        auto a = __builtin_amdgcn_permlane32_swap(__float_as_uint(m0), __float_as_uint(m1), false, false);
        const float x = fmx(__uint_as_float(a[0]), __uint_as_float(a[1]));
        auto b2 = __builtin_amdgcn_permlane16_swap(__float_as_uint(x), __float_as_uint(x), false, false);
        const float y = fmx(__uint_as_float(b2[0]), __uint_as_float(b2[1]));
        auto c2 = __builtin_amdgcn_permlane32_swap(__float_as_uint(y), __float_as_uint(y), false, false);
        m[0] = __uint_as_float(c2[0]);
        m[1] = __uint_as_float(c2[1]);
      }
      const bf16x8 ones = asfrag(0x3f803f80u, 0x3f803f80u, 0x3f803f80u, 0x3f803f80u);
      f32x4 o[2][2], sm[2];
#pragma unroll
      for (int t = 0; t < 2; ++t) {
        unsigned pw4[4];
#pragma unroll
        for (int j = 0; j < 4; ++j) pw4[j] = pk(__builtin_amdgcn_exp2f(sv[t][j] - m[t]), 0.f) << psh;
        const bf16x8 pf = asfrag(pw4[0], pw4[1], pw4[2], pw4[3]);
        sm[t] = mfma(ones, pf, z);
#pragma unroll
        for (int c = 0; c < 2; ++c) {
          const bf16x8 vf = t == 0 ? asfrag(vp[2 * c], vp[2 * c + 1], vh[0][c][0], vh[0][c][1]) : asfrag(vh[0][c][0], vh[0][c][1], vh[1][c][0], vh[1][c][1]);
          o[t][c] = mfma(vf, pf, z);
        }
      }
      v4u ov[2];
#pragma unroll
      for (int t = 0; t < 2; ++t) {
        const float sc = gv[t] * __builtin_amdgcn_rcpf(RATIO ? sm[t][0] * og[t] : sm[t][0]);
        const f32x4 o0 = o[t][0] * sc, o1 = o[t][1] * sc;
        ov[t] = v4u{pk(o0[0], o0[1]), pk(o0[2], o0[3]), pm & pk(o1[0], o1[1]), pm & pk(o1[2], o1[3])};
      }
      SCHED();
      st128(ov[0], rws, lane * 16u, oa);
      st128(ov[1], rws, lane * 16u, oa + A2_TT);
      if (hd == 0 && g < 2)
        __builtin_amdgcn_raw_buffer_store_b32(__float_as_uint(g ? og[1] : og[0]), rws, lane * 4u, WS_G + hp * 8192u + (128 * rb + 32 * tq) * 4u, 16);
    }
  };
  if (ffn) phase1(IC<1>{});
  else if (qkv) phase1(IC<0>{});
  else {
    if (w >= 3) do_touch();
    rep<0, 7>([&](auto K_) { setup2(K_, IC<0>{}); });
    for (int i = 0; i < FREE_SLEEP; ++i) __builtin_amdgcn_s_sleep(16);
    rep<0, 16>(b_dma);
    wait_v<0>();
    rd_step(IC<0>{});
    rd_step(IC<1>{});
    fo_pub = WS_F + 15 * FSEC;
  }
  STAMP(3);
  ct = __builtin_amdgcn_readfirstlane(ct);
  tt0 = __builtin_amdgcn_readfirstlane(tt0);

  const u64 tag = (gm_dispatch_id() + 1) * 0x9E3779B97F4A7C15ull ^ (u64)__builtin_amdgcn_dispatch_ptr();
  unsigned va[4];
#pragma unroll
  for (int t = 0; t < 4; ++t) va[t] = opaque(lane * 16u + t * A2_TT);
  const unsigned aob = tt0 * A2_TT + (16u + 7u * w) * 1024u, aoa = tt0 * A2_TT + 2u * w * 1024u;
  const unsigned frow = WS_F + (tt0 >> 3) * FROW, frows = (tt0 & 7) == 7 ? 1u : 0u;
  SCHED();
  if (!qkv) {
    wait_l();
    if (NMID) bar();
    rep<16, 16 + NMID>(b_dma);
  } else {
    rep<0, NMID>(r_dma);
  }
  wait_v<NMID>();
  rd_step(IC<2>{});
  rd_step(IC<3>{});
  SCHED();
  STAMP(4);
  bar();
  if (tid < 4) {
    const v2q tv = {tag, tag};
    st128(__builtin_bit_cast(v4u, tv), rws, lane * 16u, fo_pub);
  }
  const unsigned oct = (li & 4) | ((li & 1) << 1) | ((li >> 1) & 1);
  v4u b2v;
  ld128<0>(b2v, oct * 16u, mk(p.b2, D * 2), ct * 128u);
  wait_l();
  if (!qkv) rep<16 + NMID, 36>(b_dma);
  else rep<NMID, 8>(r_dma);
  STAMP(5);

  const unsigned fln = opaque(li * FSEC + g * FROW);
  auto poll = [&](bool todo) {
#pragma unroll 1
    for (unsigned spin = 0; spin < SPIN_CAP; ++spin) {
      if (todo) {
        const v2u v = __builtin_amdgcn_raw_buffer_load_b64(rws, fln, frow, 16);
        todo = __builtin_bit_cast(u64, v) != tag;
      }
      asm volatile("" ::: "memory");
      if (!__any(todo)) break;
      __builtin_amdgcn_s_sleep(1);
      if (!ffn && !qkv) __builtin_amdgcn_s_sleep(12);
    }
  };
  poll(li >= ((7 * w) >> 3) && li <= ((7 * w + 6) >> 3) && g <= frows);
  STAMP(6);
  unsigned ra[2][2], oo = 0, ga2 = 0;
  bf16x8 sel[4];
  f32x4 blo, bhi;
  float gl;
  auto final_setup = [&](auto K_) {
    constexpr int K = decltype(K_)::value;
    if constexpr (K < 2) {
#pragma unroll
      for (int e = 0; e < 2; ++e)
        ra[K][e] = opaque((q4 + 4u * e) * REG + (w >> 1) * 2048u + ((g >> 1) * 32u + (p4 | ((((K | ((w & 1u) << 1)) + q4) & 3u) << 2))) * 16u + (g & 1) * 8u);
    } else if constexpr (K == 2) {
      const unsigned tk = 8 * w + 2 * g + (li >> 3);
      oo = opaque((16 * tt0 + tk) * 896u + ct * 128u + oct * 16u);
      if (!RATIO) ga2 = opaque(SPARE + tk * 4u);
    } else if constexpr (K == 4 || K == 5) {
#pragma unroll
      for (int u = 2 * (K - 4); u < 2 * (K - 4) + 2; ++u) {
        const unsigned one = li == 4 * u + g ? 0x3f803f80u : 0u;
        sel[u] = asfrag(one, one, one, one);
        pin(sel[u]);
      }
    } else if constexpr (K == 6) {
      pin(b2v);
      blo = f32x4{bflo(b2v[0]), bfhi(b2v[0]), bflo(b2v[1]), bfhi(b2v[1])};
      bhi = f32x4{bflo(b2v[2]), bfhi(b2v[2]), bflo(b2v[3]), bfhi(b2v[3])};
      pin(blo);
      pin(bhi);
    }
  };
  auto phase2 = [&](auto NT_) {
    constexpr int NT = decltype(NT_)::value;
    v4u af[2][NT], aa[2][NT];
    v2u fl = {(unsigned)tag, (unsigned)(tag >> 32)};
    const bool fme = li == 7 + w && g <= frows;
    auto a_load = [&](auto J_, auto T_) {
      constexpr int j = decltype(J_)::value, t = decltype(T_)::value;
      if constexpr (j < 4) ld128<j * 1024u>(af[j & 1][t], va[t], rws, aob);
      else if constexpr (j < 7) ld128<(j - 4) * 1024u>(af[j & 1][t], va[t], rws, aob + 4096u);
      else ld128<(j - 7) * 1024u>(aa[j - 7][t], va[t], rws, aoa);
    };
    __builtin_amdgcn_s_setprio(3);
    rep<0, 2 * NT>([&](auto I_) { constexpr int I = decltype(I_)::value; a_load(IC<I / NT>{}, IC<I % NT>{}); });
    f32x4 acc[4][NT];
    unsigned pa = 0;
    auto put = [&](int t) {
#pragma unroll
      for (int cp = 0; cp < 2; ++cp) {
        const f32x4 x = acc[2 * cp][t], y = acc[2 * cp + 1][t];
        *(LAS v4u*)(L + pa + (t * 2 + cp) * 1024u) = v4u{pk(x[0], x[1]), pk(x[2], x[3]), pk(y[0], y[1]), pk(y[2], y[3])};
      }
    };
    rep<0, 7>([&](auto J_) {
      constexpr int j = decltype(J_)::value;
      if constexpr (j == 2 || j == 4 || j == 6) __builtin_amdgcn_s_setprio(3 - j / 2);
      if constexpr (j == 5) {
        wait_for<p2_before(5, 0, NT) - p2_before(3, 1, NT)>(fl);
        const bool todo = __builtin_bit_cast(u64, fl) != tag;
        if (__any(todo)) poll(todo);
        SCHED();
      }
      if constexpr (j == 6) pa = opaque(wreg + (g * 16u + (((li >> 1) & 3u) | (((((li & 1u) | ((li >> 3) << 1)) + w) & 3u) << 2))) * 16u);
      if constexpr (j >= 4) wait_l();
      rep<0, NT>([&](auto T_) {
        constexpr int t = decltype(T_)::value, U = p2_unit(j, t, NT);
        if constexpr (U >= 0) b_m0(IC<U>{});
        wait_v<p2_before(j, t, NT) - p2_pos(j, t, NT) - 1>();
        const bf16x8 a = __builtin_bit_cast(bf16x8, af[j & 1][t]);
#pragma unroll
        for (int c = 0; c < 4; ++c) {
          if constexpr (j == 0) mma0(acc[c][t], cat(bl[0][c], bh[0][c]), a);
          else mma(acc[c][t], cat(bl[j & 3][c], bh[j & 3][c]), a);
        }
        SCHED();
        if constexpr (U >= 0) b_go(IC<U>{});
        if constexpr (j < 6) a_load(IC<j + 2>{}, T_);
        if constexpr (j == 5) a_load(IC<8>{}, T_);
        if constexpr (j == 5 && t == 0) ld32(gl, (RATIO ? 8 * w + 2 * g + (li >> 3) : lane) * 4u, rws, WS_G + w * 8192u + tt0 * 64u);
        if constexpr (j == 3 && t == 0) { if (fme) ld64(fl, fln, rws, frow); }
        if constexpr (j >= 2) {
          b_rd(IC<j + 2>{}, T_, IC<0>{});
          b_rd(IC<j + 2>{}, T_, IC<1>{});
          if constexpr (NT == 3 && t == 2) {
            b_rd(IC<j + 2>{}, IC<3>{}, IC<0>{});
            b_rd(IC<j + 2>{}, IC<3>{}, IC<1>{});
          }
        }
        if constexpr (j == 6) final_setup(T_);
        SCHED();
      });
      if constexpr (j == 0) STAMP(16);
      if constexpr (j == 2) STAMP(17);
      if constexpr (j == 4) STAMP(18);
      if constexpr (j == 6) STAMP(19);
    });
    constexpr int NALL = p2_before(6, 0, NT);
    float og[4];
    if (!RATIO) {
      wait_for<NALL - p2_before(5, 1, NT)>(gl);
      auto h2 = __builtin_amdgcn_permlane32_swap(__float_as_uint(gl), __float_as_uint(gl), false, false);
      auto lo2 = __builtin_amdgcn_permlane16_swap(h2[0], h2[0], false, false);
      auto hi2 = __builtin_amdgcn_permlane16_swap(h2[1], h2[1], false, false);
      og[0] = __uint_as_float(lo2[0]); og[1] = __uint_as_float(lo2[1]); og[2] = __uint_as_float(hi2[0]); og[3] = __uint_as_float(hi2[1]);
    }
    wait_l();
    STAMP(27);
    if (!RATIO) { if (w == 0) *(LAS float*)(L + SPARE + lane * 4u) = gl; }
    rep<0, NT>([&](auto T_) {
      constexpr int t = decltype(T_)::value;
      if (!RATIO) {
#pragma unroll
        for (int c = 0; c < 4; ++c) acc[c][t] *= og[t];
        SCHED();
        if constexpr (t >= 1) put(t - 1);
        SCHED();
      }
      wait_v<NALL - p2_pos(8, t, NT) - 1>();
      asm volatile("s_nop 0");
#pragma unroll
      for (int c = 0; c < 4; ++c) mma(acc[c][t], cat(bl[3][c], bh[3][c]), __builtin_bit_cast(bf16x8, aa[0][t]));
#pragma unroll
      for (int c = 0; c < 4; ++c) mma(acc[c][t], cat(bl[0][c], bh[0][c]), __builtin_bit_cast(bf16x8, aa[1][t]));
      SCHED();
      if constexpr (t >= 1 && RATIO) put(t - 1);
      final_setup(IC<4 + t>{});
      SCHED();
    });
    STAMP(7);
    asm volatile("s_nop 7" ::: "memory");
    SCHED();
    put(NT - 1);
  };
  if (qkv) phase2(IC<3>{});
  else phase2(IC<4>{});
  wait_l();
  bar();
  STAMP(8);
  if (!qkv || w < 6) {
    pin(gl);
    const float og = RATIO ? 1.0f : *(LAS float*)(L + ga2);
    bf16x8 pf[2][4];
#pragma unroll
    for (int u = 0; u < 4; ++u)
#pragma unroll
      for (int a = 0; a < 2; ++a) pf[a][u] = cat(ldstr(L, ra[u >> 1][0] + (u & 1) * 1024u + a * 256u), ldstr(L, ra[u >> 1][1] + (u & 1) * 1024u + a * 256u));
    f32x4 alo = blo * og, ahi = bhi * og;
#pragma unroll
    for (int u = 0; u < 4; ++u) {
      alo = mfma(pf[0][u], sel[u], alo);
      ahi = mfma(pf[1][u], sel[u], ahi);
    }
    if (RATIO) {
      alo *= gl;
      ahi *= gl;
    }
    const v4u o = {pk(alo[0], alo[1]), pk(alo[2], alo[3]), pk(ahi[0], ahi[1]), pk(ahi[2], ahi[3])};
    *(GAS v4u*)((char*)p.out + oo) = o;
  }
  STAMP(9);
}
}

extern "C" void kernel_launch(void* const* d_in, const int* in_sizes, int n_in, void* d_out, int out_size, void* d_ws, size_t ws_size,
                              hipStream_t stream) {
  gm::Params p;
  p.x = d_in[0];
  p.wqkv = d_in[1];
  p.wo = d_in[2];
  p.w1 = d_in[4];
  p.b1 = d_in[5];
  p.w2 = d_in[6];
  p.b2 = d_in[7];
  p.gw = d_in[8];
  p.out = d_out;
  p.ws = d_ws;
  gm::gm_kernel<<<256, 512, 0, stream>>>(p);
}
```

```cpp
#include <hip/hip_runtime.h>
#include <stdint.h>

typedef short bf16x8 __attribute__((ext_vector_type(8)));
typedef short s16x4 __attribute__((ext_vector_type(4)));
typedef float f32x4 __attribute__((ext_vector_type(4)));
typedef unsigned v4u __attribute__((ext_vector_type(4)));
typedef unsigned v2u __attribute__((ext_vector_type(2)));
typedef __bf16 bf2_t __attribute__((ext_vector_type(2)));
typedef float f32x2 __attribute__((ext_vector_type(2)));
typedef unsigned long long u64;
typedef u64 v2q __attribute__((ext_vector_type(2)));
typedef __amdgpu_buffer_rsrc_t rsrc_t;
#define LAS __attribute__((address_space(3)))
#define GAS __attribute__((address_space(1)))
#define DI __device__ __forceinline__

extern "C" __device__ u64 gm_dispatch_id() __asm("llvm.amdgcn.dispatch.id");

namespace gm {
constexpr int S = 2048, D = 448, F = 1792, NQKV = 1344;
constexpr int KS_A = 16, KS = 72;
constexpr unsigned A2_TT = KS * 1024;
constexpr unsigned FROW = 4352, FSEC = 256;
constexpr unsigned WS_G = 128 * A2_TT, WS_F = WS_G + 8 * 8192, WS_END = WS_F + 16 * FROW;
constexpr unsigned REG = 20480, STG = 6144, SPARE = 3 * STG;
constexpr int LDS_BYTES = 163840;
constexpr unsigned GW_OFF = 2 * REG + 5120;
constexpr unsigned SPIN_CAP = 1u << 16;
constexpr int TOUCH2_HB = 3;
constexpr unsigned CODE_KB = 23;
constexpr int NMID = 4;

struct Params {
  const void *x, *wqkv, *wo, *w1, *b1, *w2, *b2, *gw;
  void *out, *ws;
};
template <int N> struct IC { static constexpr int value = N; };
template <int I, int N, class Fn> DI void rep(Fn&& f) {
  if constexpr (I < N) {
    f(IC<I>{});
    rep<I + 1, N>(f);
  }
}

#define SCHED() __builtin_amdgcn_sched_barrier(0)
#define NOP1() asm volatile("s_nop 0")
DI rsrc_t mk(const void* p, unsigned n) { return __builtin_amdgcn_make_buffer_rsrc((void*)p, 0, n, 0x00020000); }
template <unsigned C> DI void set_m0(unsigned base) { asm volatile("s_add_u32 m0, %0, %1" ::"s"(base), "i"(C) : "scc", "memory"); }
template <unsigned IMM> DI void dma_go(unsigned voff, rsrc_t rs, unsigned soff) {
  asm volatile("buffer_load_dwordx4 %0, %1, %2 offen offset:%3 lds" ::"v"(voff), "s"(rs), "s"(soff), "i"(IMM) : "memory");
}
template <unsigned IMM> DI void dma_go0(unsigned voff, rsrc_t rs) {
  asm volatile("buffer_load_dwordx4 %0, %1, 0 offen offset:%2 lds" ::"v"(voff), "s"(rs), "i"(IMM) : "memory");
}
DI void touch(unsigned lds, unsigned voff, u64 base) {
  asm volatile("s_mov_b32 m0, %0\n\ts_nop 0\n\tglobal_load_lds_dword %1, %2" ::"s"(lds), "v"(voff), "s"(base) : "memory");
}
template <unsigned IMM> DI void ld128(v4u& d, unsigned voff, rsrc_t rs, unsigned soff) {
  asm volatile("buffer_load_dwordx4 %0, %1, %2, %3 offen offset:%4 sc1" : "=v"(d) : "v"(voff), "s"(rs), "s"(soff), "i"(IMM) : "memory");
}
DI void ld32(float& d, unsigned voff, rsrc_t rs, unsigned soff) {
  asm volatile("buffer_load_dword %0, %1, %2, %3 offen sc1" : "=v"(d) : "v"(voff), "s"(rs), "s"(soff) : "memory");
}
DI void ld64(v2u& d, unsigned voff, rsrc_t rs, unsigned soff) {
  asm volatile("buffer_load_dwordx2 %0, %1, %2, %3 offen sc1" : "+v"(d) : "v"(voff), "s"(rs), "s"(soff) : "memory");
}
DI void st128(v4u v, rsrc_t rs, unsigned voff, unsigned soff) {
  __builtin_amdgcn_raw_buffer_store_b128(v, rs, voff, soff, 16);
  SCHED();
  asm volatile("s_nop 1");
  SCHED();
}
template <int N> DI void wait_v() { asm volatile("s_waitcnt vmcnt(%0)" ::"i"(N) : "memory"); }
template <int N, class T> DI void wait_for(T& v) { asm volatile("s_waitcnt vmcnt(%1)" : "+v"(v) : "i"(N) : "memory"); }
DI void wait_l() {
  SCHED();
  __builtin_amdgcn_s_waitcnt(0xC07F);
  asm volatile("" ::: "memory");
  SCHED();
}
DI void bar() { asm volatile("s_barrier" ::: "memory"); }
DI f32x4 mfma(bf16x8 a, bf16x8 b, f32x4 c) { return __builtin_amdgcn_mfma_f32_16x16x32_bf16(a, b, c, 0, 0, 0); }
DI void mma(f32x4& c, bf16x8 a, bf16x8 b) { asm volatile("v_mfma_f32_16x16x32_bf16 %0, %1, %2, %0" : "+v"(c) : "v"(a), "v"(b)); }
DI void mma0(f32x4& c, bf16x8 a, bf16x8 b) { asm volatile("v_mfma_f32_16x16x32_bf16 %0, %1, %2, 0" : "=&v"(c) : "v"(a), "v"(b)); }
DI void mmac(f32x4& d, bf16x8 a, bf16x8 b, f32x4 c) { asm volatile("v_mfma_f32_16x16x32_bf16 %0, %1, %2, %3" : "=&v"(d) : "v"(a), "v"(b), "v"(c)); }
DI float relu(float v) {
  float r;
  asm("v_max_f32 %0, 0, %1" : "=v"(r) : "v"(v));
  return r;
}
DI float fmx(float a, float b) {
  float r;
  asm("v_max_f32 %0, %1, %2" : "=v"(r) : "v"(a), "v"(b));
  return r;
}
DI unsigned pk(float lo, float hi) {
  f32x2 v = {lo, hi};
  return __builtin_bit_cast(unsigned, __builtin_convertvector(v, bf2_t));
}
DI float rbf(float f) { return __uint_as_float(pk(f, 0.f) << 16); }
DI float bflo(unsigned u) { return __uint_as_float(u << 16); }
DI float bfhi(unsigned u) { return __uint_as_float(u & 0xffff0000u); }
DI bf16x8 asfrag(unsigned a, unsigned b, unsigned c, unsigned d) {
  v4u v = {a, b, c, d};
  return __builtin_bit_cast(bf16x8, v);
}
DI bf16x8 cat(s16x4 lo, s16x4 hi) { return __builtin_shufflevector(lo, hi, 0, 1, 2, 3, 4, 5, 6, 7); }
DI bf16x8 lds128(LAS char* L, unsigned a) { return *(LAS bf16x8*)(L + a); }
DI s16x4 ldstr(LAS char* L, unsigned a) { return __builtin_amdgcn_ds_read_tr16_b64_v4i16((LAS s16x4*)(L + a)); }
DI unsigned opaque(unsigned v) {
  asm volatile("" : "+v"(v));
  return v;
}
DI unsigned sopaque(unsigned v) {
  asm volatile("" : "+s"(v));
  return v;
}
template <class T> DI void pin(T& v) { asm volatile("" : "+v"(v)); }
DI void keep(f32x4 v) { asm volatile("" ::"v"(v)); }


constexpr unsigned hloc(int h) {
  h = h >= 10 ? h - 10 : h;
  return h == 0 ? SPARE : (h < 4 ? STG + (h - 1) * 2048u : (h < 7 ? 2 * STG + (h - 4) * 2048u : (h - 7) * 2048u));
}
constexpr unsigned ppos(int i) { return i < 2 ? i + 1 : (i == 2 ? 0 : i); }

constexpr int q_item(int i, bool ffn) { return i < 36 ? 8 * (1 + i / 6) + (ffn ? i % 6 : (i % 6 + 5) % 6) : 100 + (i - 36); }
constexpr int q_from(int item) {
  if (item < 100) return (item >> 3) <= 2 ? 0 : 2 * ((item >> 3) - 3) + 1;
  const int h = (item - 100) >> 1;
  return h == 0 ? 1 : (h < 4 ? 9 : (h < 7 ? 11 : 13));
}
constexpr int q_start(int hb) {
  int i = 6;
  for (int h = 0; h < hb; ++h)
    for (int k = 0; k < 4; ++k)
      if (i < 52 && q_from(q_item(i, true)) <= h) ++i;
  return i;
}
constexpr int q_at(int hb, int i, bool ffn) {
  return (i & 3) == 1 && q_start(hb) + (i >> 2) < q_start(hb + 1) ? q_item(q_start(hb) + (i >> 2), ffn) : -1;
}
constexpr int q_count(int from, int to, bool ffn) {
  int n = 0;
  for (int i = from; i < to; ++i) n += (ffn || i >= 36 || i % 6 != 0) ? 1 : 0;
  return n;
}
constexpr int q_wait(int s, bool ffn) { return q_count(s < 6 ? 6 * (s + 1) : 44, q_start(2 * s + 1), ffn); }
static_assert(q_start(14) == 52 && q_start(3) >= 12 && q_start(5) >= 18 && q_start(7) >= 24 && q_start(9) >= 30 && q_start(11) >= 36 &&
                  q_start(13) >= 44 && q_start(13) - q_start(12) <= 2 && q_start(14) - q_start(13) <= 2,
              "queue order");
constexpr int r_unit(int i) { return i < 2 ? 16 + i : (i < 6 ? 18 + i : 12 + i); }

constexpr int p2_unit(int j, int t, int nt) { return nt == 3 && 3 * j + t < 12 ? 24 + 3 * j + t : -1; }
constexpr int p2_after(int j, int t, int nt) {
  return (p2_unit(j, t, nt) >= 0 ? 1 : 0) + (j < 5 ? 1 : (j == 5 ? 2 : 0))
         + (j == 5 && t == 0 ? 1 : 0)
         + (j == 3 && t == 0 ? 1 : 0);
}
constexpr int p2_before(int j, int t, int nt) {
  int n = 2 * nt;
  for (int i = 0; i < nt * j + t; ++i) n += p2_after(i / nt, i % nt, nt);
  return n;
}
constexpr int p2_pos(int j, int t, int nt) {
  return j < 2 ? nt * j + t : (j == 8 ? p2_before(5, t, nt) + 1 : p2_before(j - 2, t, nt) + (p2_unit(j - 2, t, nt) >= 0 ? 1 : 0));
}

__global__ __launch_bounds__(512, 2) void gm_kernel(Params p) {
  __shared__ __attribute__((aligned(1024))) char smem[LDS_BYTES];
  LAS char* L = (LAS char*)smem;
  u64 px, pw, pv, pt;
  asm volatile("s_load_dwordx2 %0, %1, 0x0" : "=&s"(px) : "s"(__builtin_amdgcn_kernarg_segment_ptr()));
  SCHED();
  const unsigned tid = threadIdx.x, lane = tid & 63;
  const unsigned b = blockIdx.x, xcd = b & 7, slot = b >> 3;
  const bool ffn = b < 112, qkv = b >= 128;
  const unsigned w = __builtin_amdgcn_readfirstlane(tid >> 6);
  asm volatile("s_load_dwordx2 %0, %3, %4\n\ts_load_dwordx2 %1, %3, %5\n\ts_load_dwordx2 %2, %3, %6"
               : "=&s"(pw), "=&s"(pv), "=&s"(pt)
               : "s"(__builtin_amdgcn_kernarg_segment_ptr()), "s"(qkv ? 8u : 24u), "s"(qkv ? 56u : 32u), "s"(((0x25130130u >> (4 * w)) & 7u) * 8u));
  if (tid < CODE_KB) {
    u64 pc;
    asm volatile("s_getpc_b64 %0" : "=s"(pc));
    touch(SPARE + 1024u, ((slot & 7) * CODE_KB + tid + 1) * 128u, pc);
  }
  const unsigned wreg = w * REG;
  const unsigned li = lane & 15, g = lane >> 4, rr = lane >> 3, pos = lane & 7;
  const unsigned rq = xcd >> 1, side = xcd & 1;
  const unsigned tcnt = (unsigned)(0x0C31151C38101520ull >> (8 * w)) & 0xffu;
  const unsigned tfirst = ((unsigned)(0x00003F5400000000ull >> (8 * w)) & 0xffu) * 64u;
  const unsigned tsh = w ? 6u : 2u;
  const unsigned tln = opaque(((32 * xcd + slot) * (w ? tcnt : 56u) + tfirst + (lane >> tsh) * 7u + (lane & ((1u << tsh) - 1u))) * 128u);
  auto do_touch = [&]() {
    if (lane < tcnt) touch(wreg + SPARE + 1024u, tln, pt);
  };
  const bool mid3 = (slot >> 2) == 3;
  const unsigned rb = 4 * rq + (mid3 ? 2 * side + (slot & 1) : slot & 3), cs = mid3 ? 3u : side * 4 + ((slot >> 2) & 3);
  unsigned vx[2];
#pragma unroll
  for (int h = 0; h < 2; ++h) vx[h] = opaque((128 * rb + 16 * w + 8 * h + rr) * 896u + ((pos ^ (4u * h + (rr >> 1))) << 4));
  const unsigned vw = opaque((8 * w + rr) * (qkv ? 2688u : 3584u) + (min(pos ^ ((((rr >> 1) & 1) | ((w & 1) << 1)) << 1), qkv ? 6u : 7u) << 4) +
                             cs * (qkv ? 112u : 512u));
  SCHED();
  asm volatile("s_waitcnt lgkmcnt(0)" : "+s"(px), "+s"(pw), "+s"(pv), "+s"(pt)::"memory");
  SCHED();
  const rsrc_t rx = mk((const void*)px, S * D * 2);
  const rsrc_t rw = mk((const void*)pw, qkv ? D * NQKV * 2 : D * F * 2);
  if (w < 3) do_touch();
  if (ffn || qkv) {
    const unsigned wus = qkv ? 896u : 128u;
    rep<0, 5>([&](auto I_) {
      constexpr int I = decltype(I_)::value;
      set_m0<ppos(I) * 1024u>(wreg);
      NOP1();
      if constexpr (I < 2) dma_go0<0>(vx[I], rx);
      else dma_go<0>(vw, rw, (I - 2) * wus);
    });
  }
  SCHED();

  rsrc_t rws, rw2, rwo;
  unsigned vb[2], ba[4], fo_pub, ct = 0, tt0 = 0;
  const unsigned bo2 = w * (224u * 896u), boo = w * (56u * 896u);
  const unsigned q4 = (lane >> 2) & 3, p4 = lane & 3;
  const unsigned fsw = (q4 >> 1) | ((g & 1) << 1);
  s16x4 bl[4][4], bh[4][4];
  auto b_m0 = [&](auto N_) {
    constexpr int N = decltype(N_)::value;
    set_m0<hloc(N >> 1) + (N & 1) * 1024u>(wreg);
  };
  auto b_go = [&](auto N_) {
    constexpr int N = decltype(N_)::value, j = N >> 2;
    if constexpr (j < 7) dma_go<0>(vb[N & 1], rw2, bo2 + (N >> 1) * (16u * 896u));
    else dma_go<0>(vb[N & 1], rwo, boo + ((j - 7) * 28u + ((N >> 1) & 1) * 16u) * 896u);
  };
  auto b_dma = [&](auto N_) { b_m0(N_); NOP1(); b_go(N_); };
  auto r_dma = [&](auto I_) { b_dma(IC<r_unit(decltype(I_)::value)>{}); };
  auto b_rd = [&](auto J_, auto C_, auto E_) {
    constexpr int J = decltype(J_)::value, C = decltype(C_)::value, E = decltype(E_)::value;
    if constexpr (E == 0) bl[J & 3][C] = ldstr(L, ba[C] + hloc(2 * J));
    else bh[J & 3][C] = ldstr(L, ba[C] + hloc(2 * J + 1));
  };
  auto rd_step = [&](auto J_) { rep<0, 8>([&](auto I_) { constexpr int I = decltype(I_)::value; b_rd(J_, IC<(I >> 1)>{}, IC<(I & 1)>{}); }); };
  auto setup2 = [&](auto K_, auto Q_) {
    constexpr int K = decltype(K_)::value;
    constexpr bool Q = decltype(Q_)::value;
    if constexpr (K == 0) {
      ct = Q ? (slot >> 2) - 1 : (unsigned)max((int)(slot >> 2), (int)slot - 9);
      tt0 = 16 * xcd + (Q ? 4 + 3 * (slot & 3) : (slot < 12 ? 4 * (slot & 3) : 0u));
      rws = mk(p.ws, WS_END);
      rw2 = mk(p.w2, F * D * 2);
      rwo = mk(p.wo, D * D * 2);
    } else if constexpr (K == 2) {
      vb[0] = opaque(rr * 896u + ((pos ^ (((rr >> 1) & 3) << 1)) << 4) + ct * 128u);
      vb[1] = opaque(vb[0] + 8u * 896u);
    } else if constexpr (K >= 3 && K < 7) {
      ba[K - 3] = opaque(wreg + (4 * g + q4) * 128u + ((2u * ((K - 3) ^ fsw) + (p4 >> 1)) << 4) + (p4 & 1) * 8u);
    }
  };

  auto phase1 = [&](auto FFN_) {
    constexpr bool FFN = decltype(FFN_)::value;
    constexpr unsigned LDW = FFN ? 3584u : 2688u, WUS = FFN ? 128u : 896u;
    unsigned vh5 = 0;
    if (!FFN) vh5 = opaque(((rb ? 128 * rb - 16 : 0u) + 8 * (w & 1) + rr) * 896u + ((pos ^ (4u * (w & 1) + (rr >> 1))) << 4));
    auto d_m0 = [&](auto E_) {
      constexpr int E = decltype(E_)::value, I = E & 7, ST = E >> 3;
      if constexpr (E >= 100) b_m0(IC<E - 100>{});
      else if constexpr (E >= 0) set_m0<(ST % 3) * STG + ppos(I) * 1024u - ((I < 2 || (!FFN && I == 5)) ? ST * 128u : (I - 2) * WUS)>(wreg);
    };
    auto d_go = [&](auto E_) {
      constexpr int E = decltype(E_)::value, I = E & 7, ST = E >> 3;
      if constexpr (E >= 100) b_go(IC<E - 100>{});
      else if constexpr (E >= 0) {
        if constexpr (I < 2) dma_go0<ST * 128u>(vx[I], rx);
        else if constexpr (!FFN && I == 5) { if (w < 2) dma_go0<ST * 128u>(vh5, rx); }
        else if constexpr (ST == 0) dma_go0<(I - 2) * WUS>(vw, rw);
        else dma_go<(I - 2) * WUS>(vw, rw, ST * 64u * LDW);
      }
    };
    d_m0(IC<5>{});
    NOP1();
    d_go(IC<5>{});
    if (w == 2) {
      set_m0<FFN ? SPARE : GW_OFF>(0);
      NOP1();
      dma_go0<0>(lane * 16, mk((const char*)pv + (FFN ? cs * 512u : 0u), FFN ? 512 : 896));
    }
    SCHED();
    const unsigned xa0 = 1024u + (li >> 3) * 1024u + (li & 7) * 128u + ((g ^ ((li >> 1) & 7)) << 4);
    auto second_stage = [&]() {
      SCHED();
      bar();
      rep<0, 6>([&](auto I_) { constexpr int E = q_item(decltype(I_)::value, FFN); d_m0(IC<E>{}); NOP1(); d_go(IC<E>{}); });
      if (w >= 3 && w < 6) {
        do_touch();
        wait_v<q_count(0, 6, FFN) + 1>();
      } else {
        wait_v<q_count(0, 6, FFN)>();
      }
      bar();
    };
    auto mid = [&](auto S_) {
      constexpr int s = decltype(S_)::value;
      constexpr bool t2 = TOUCH2_HB <= 2 * s && q_start(TOUCH2_HB + 1) >= 6 * (s + 1);
      if ((s == 0 && w >= 3 && w < 6) || (t2 && w >= 6)) wait_v<q_wait(s, FFN) + 1>();
      else wait_v<q_wait(s, FFN)>();
      wait_l();
      bar();
    };

    if constexpr (FFN) {
      const unsigned mh = w >> 2, nq = w & 3;
      unsigned xa[2][2], wa[2][4];
#pragma unroll
      for (int hs = 0; hs < 2; ++hs) {
#pragma unroll
        for (int t2 = 0; t2 < 2; ++t2) xa[hs][t2] = opaque((xa0 ^ (hs * 64u)) + (mh * 4u + t2 * 2u) * REG);
#pragma unroll
        for (int c = 0; c < 4; ++c)
          wa[hs][c] = opaque((4u * hs + g) * REG + (nq ? 2u + nq : 0u) * 1024u + q4 * 128u + ((2u * (c ^ fsw) + (p4 >> 1)) << 4) + (p4 & 1) * 8u);
      }
      second_stage();
      bf16x8 xf[2][4];
      s16x4 wl[2][4], wh[2][4];
      f32x4 acc[4][4];
      auto rd = [&](auto I_, auto HB_) {
        constexpr int I = decltype(I_)::value, HB = decltype(HB_)::value, hs = HB & 1;
        constexpr unsigned bo = ((HB >> 1) % 3) * STG;
        if constexpr (I >= 2 && I < 6) xf[hs][I - 2] = lds128(L, xa[hs][(I - 2) >> 1] + ((I - 2) & 1) * REG + bo);
        else if constexpr (I < 12) {
          constexpr int c = I < 2 ? 0 : (I - 4) >> 1;
          if constexpr ((I & 1) == 0) wl[hs][c] = ldstr(L, wa[hs][c] + bo);
          else wh[hs][c] = ldstr(L, wa[hs][c] + bo + 512u);
        }
      };
      f32x4 bv[4];
#pragma unroll
      for (int c = 0; c < 4; ++c) {
        const v2u bb = *(LAS v2u*)(L + SPARE + nq * 128u + c * 32u + g * 8u);
        bv[c] = f32x4{bflo(bb.x), bfhi(bb.x), bflo(bb.y), bfhi(bb.y)};
      }
      rep<0, 12>([&](auto I_) { rd(I_, IC<0>{}); });
#pragma unroll
      for (int c = 0; c < 4; ++c) pin(bv[c]);
      unsigned so = 0;
      auto epi = [&](int t, int u) {
        float hv[8];
#pragma unroll
        for (int j = 0; j < 8; ++j) {
          const float r = relu(acc[2 * u + (j >> 2)][t][j & 3]);
          hv[j] = r * r;
        }
        const v4u o = {pk(hv[0], hv[1]), pk(hv[2], hv[3]), pk(hv[4], hv[5]), pk(hv[6], hv[7])};
        st128(o, rws, lane * 16u + u * 1024u, so + t * A2_TT);
      };
      rep<0, 12>([&](auto HB_) {
        constexpr int HB = decltype(HB_)::value, hs = HB & 1;
        if constexpr (HB == 0) asm volatile("s_nop 1");
        else if constexpr (hs == 0) wait_l();
        rep<0, 16>([&](auto I_) {
          constexpr int I = decltype(I_)::value;
          constexpr int E = q_at(HB, I, FFN);
          d_m0(IC<E>{});
          if constexpr (HB == 0) mmac(acc[I >> 2][I & 3], cat(wl[hs][I >> 2], wh[hs][I >> 2]), xf[hs][I & 3], bv[I >> 2]);
          else mma(acc[I >> 2][I & 3], cat(wl[hs][I >> 2], wh[hs][I >> 2]), xf[hs][I & 3]);
          SCHED();
          rd(I_, IC<HB + 1>{});
          if constexpr (I == 14 && HB < 7) setup2(HB_, IC<0>{});
          if constexpr (I == 14 && HB == 7) {
            so = sopaque((8 * rb + 4 * mh) * A2_TT + (KS_A + 8 * cs + 2 * nq) * 1024u);
            fo_pub = sopaque(WS_F + rb * FROW + cs * FSEC);
          }
          if constexpr (I == 15 && HB == TOUCH2_HB) { if (w >= 6) do_touch(); }
          SCHED();
          d_go(IC<E>{});
        });
        if constexpr (hs == 0) mid(IC<(HB >> 1)>{});
      });
      wait_l();
      rep<0, 32>([&](auto M_) {
        constexpr int M = decltype(M_)::value, G = M >> 2, c = M & 3;
        constexpr int hs = G < 4 ? G >> 1 : G & 1, t = G < 4 ? G & 1 : G >> 1;
        constexpr int E = M < 8 ? q_at(12, M, FFN) : (M < 16 ? q_at(13, M - 8, FFN) : -1);
        d_m0(IC<E>{});
        mma(acc[c][t], cat(wl[hs][c], wh[hs][c]), xf[hs][t]);
        SCHED();
        if constexpr (M < 6) { rd(IC<2 * M>{}, IC<13>{}); rd(IC<2 * M + 1>{}, IC<13>{}); }
        if constexpr (M >= 8 && M < 14) {
          rep<0, (M < 12 ? 3 : 2)>([&](auto Q_) {
            constexpr int Q = (M < 12 ? 3 * (M - 8) : 12 + 2 * (M - 12)) + decltype(Q_)::value;
            b_rd(IC<(Q >> 3)>{}, IC<((Q >> 1) & 3)>{}, IC<(Q & 1)>{});
          });
        }
        if constexpr (M >= 14 && M < 32 && (M - 14) % 3 == 0) epi((M - 14) / 6, ((M - 14) / 3) & 1);
        SCHED();
        d_go(IC<E>{});
        if constexpr (M == 7) mid(IC<6>{});
      });
      asm volatile("s_nop 5" ::: "memory");
      SCHED();
      epi(3, 0);
      epi(3, 1);
    } else {
      const unsigned tq = w & 3, hd = w >> 2, hp = cs;
      unsigned xa[2], wa[2][2], xh[2], wq[2];
#pragma unroll
      for (int hs = 0; hs < 2; ++hs) {
        xa[hs] = opaque((xa0 ^ (hs * 64u)) + tq * 2u * REG);
#pragma unroll
        for (int c = 0; c < 2; ++c) {
          const unsigned bo = 56u * hd + 32u * c + 8u * p4;
          wa[hs][c] = opaque((4u * hs + g) * REG + q4 * 128u + (((bo >> 4) ^ (fsw << 1)) << 4) + (bo & 8u));
        }
        xh[hs] = opaque((li >> 3) * REG + 5120u + (li & 7) * 128u + (((4u * hs + g) ^ ((li >> 1) & 7)) << 4));
        const unsigned bo = 56u * hd + 32u * (tq & 1) + 8u * p4;
        wq[hs] = opaque((4u * hs + g) * REG + q4 * 128u + (((bo >> 4) ^ (fsw << 1)) << 4) + (bo & 8u) + (3u + (tq >> 1)) * 1024u);
      }
      const unsigned ga = opaque(GW_OFF + g * 16u);
      second_stage();
      float mb[2][4];
      unsigned oa = 0, hwa = 0, nwa = 0, pm = 0;
      auto setupq = [&](auto K_) {
        constexpr int K = decltype(K_)::value, t = K & 1;
        if constexpr (K < 2) {
#pragma unroll
          for (int j = 0; j < 4; ++j) {
            const int kap = 16 * (j >> 1) + (int)(4 * g) + 2 * (j & 1) + (int)(li & 1) - 16;
            const int dl = (int)li - kap;
            const bool ok = (dl >= 0) && (dl <= 14) && (t == 1 || j >= 2 || (rb | tq) != 0);
            mb[t][j] = __uint_as_float(opaque(ok ? 0u : 0xff800000u));
          }
        } else if constexpr (K == 2) {
          oa = sopaque((8 * rb + 2 * tq) * A2_TT + (2 * hp + hd) * 1024u);
          fo_pub = sopaque(WS_F + rb * FROW + (7 + hp) * FSEC);
        } else if constexpr (K == 3) {
          hwa = opaque(hd * 4u * REG + (tq < 2 ? 4096u + lane * 16u + tq * 8u : 4096u + tq * 512u + li * 32u + g * 8u));
          nwa = opaque(wreg + (tq < 3 ? REG + 4096u : 2048u) + lane * 16u);
          pm = opaque(g == 3 ? 0u : 0xffffffffu);
        }
      };
      f32x4 ac[7][2], ah;
      bf16x8 xf[2][2], gf[2], hf[2];
      s16x4 wl[2][6], wh[2][6], hl[2], hh[2];
      auto rd = [&](auto I_, auto HB_) {
        constexpr int I = decltype(I_)::value, HB = decltype(HB_)::value, hs = HB & 1;
        constexpr unsigned bo = ((HB >> 1) % 3) * STG;
        if constexpr (I == 2 || I == 3) xf[hs][I - 2] = lds128(L, xa[hs] + (I - 2) * REG + bo);
        else if constexpr (I < 14) {
          constexpr int c = I < 2 ? 0 : (I - 2) >> 1;
          if constexpr ((I & 1) == 0) wl[hs][c] = ldstr(L, wa[hs][c & 1] + ppos(2 + (c >> 1)) * 1024u + bo);
          else wh[hs][c] = ldstr(L, wa[hs][c & 1] + ppos(2 + (c >> 1)) * 1024u + bo + 512u);
        } else if constexpr (I == 14) gf[hs] = lds128(L, ga + HB * 64u);
        else if constexpr (I == 15) hf[hs] = lds128(L, xh[hs] + bo);
        else if constexpr (I == 16) hl[hs] = ldstr(L, wq[hs] + bo);
        else if constexpr (I == 17) hh[hs] = ldstr(L, wq[hs] + bo + 512u);
      };
      bf16x8 qf[2], kf[2], kp;
      unsigned vh[2][2][2];
      float gv[2], og[2];
      v4u vp;
      auto pack_kv = [&](int t) {
        kf[t] = asfrag(pk(ac[2][t][0], ac[2][t][1]), pk(ac[2][t][2], ac[2][t][3]), pk(ac[3][t][0], ac[3][t][1]), pk(ac[3][t][2], ac[3][t][3]));
#pragma unroll
        for (int c = 0; c < 2; ++c) {
          vh[t][c][0] = pk(ac[4 + c][t][0], ac[4 + c][t][1]);
          vh[t][c][1] = pk(ac[4 + c][t][2], ac[4 + c][t][3]);
        }
      };
      auto pack_q = [&](int t) {
        qf[t] = asfrag(pk(ac[0][t][0], ac[0][t][1]), pk(ac[0][t][2], ac[0][t][3]), pm & pk(ac[1][t][0], ac[1][t][1]), pm & pk(ac[1][t][2], ac[1][t][3]));
        pin(qf[t]);
      };
      rep<0, 18>([&](auto I_) { rd(I_, IC<0>{}); });
      rep<0, 14>([&](auto HB_) {
        constexpr int HB = decltype(HB_)::value, hs = HB & 1;
        if constexpr (hs == 0 && HB > 0) wait_l();
        rep<0, 15>([&](auto I_) {
          constexpr int I = decltype(I_)::value;
          constexpr int c = HB < 13 ? I >> 1 : (I == 0 ? 7 : (I < 5 ? 1 + I : (I < 7 ? 6 : (I < 11 ? (I - 7) & 1 : I - 9))));
          constexpr int t = HB < 13 ? I & 1 : (I < 5 ? 1 : (I < 7 ? I - 5 : (I < 11 ? 1 - ((I - 7) >> 1) : 0)));
          constexpr int E = q_at(HB, I, FFN);
          d_m0(IC<E>{});
          if constexpr (c == 7) {
            if constexpr (HB == 0) mma0(ah, cat(hl[hs], hh[hs]), hf[hs]);
            else mma(ah, cat(hl[hs], hh[hs]), hf[hs]);
          } else {
            const bf16x8 wf = c < 6 ? cat(wl[hs][c < 6 ? c : 0], wh[hs][c < 6 ? c : 0]) : gf[hs];
            if constexpr (HB == 0) {
              if constexpr (c == 4 || c == 5) mma0(ac[c][t], xf[hs][t], wf);
              else mma0(ac[c][t], wf, xf[hs][t]);
            } else {
              if constexpr (c == 4 || c == 5) mma(ac[c][t], xf[hs][t], wf);
              else mma(ac[c][t], wf, xf[hs][t]);
            }
          }
          SCHED();
          if constexpr (HB < 13) {
            if constexpr (I < 7) { rd(IC<2 * I>{}, IC<HB + 1>{}); rd(IC<2 * I + 1>{}, IC<HB + 1>{}); }
            else rd(IC<I + 7>{}, IC<HB + 1>{});
            if constexpr (I == 12 && HB < 7) setup2(HB_, IC<1>{});
            if constexpr (I == 12 && HB >= 7 && HB < 11) setupq(IC<HB - 7>{});
            if constexpr (I == 14 && HB == TOUCH2_HB) { if (w >= 6) do_touch(); }
          } else {
            if constexpr (I < 8) {
              b_rd(IC<(I >> 2)>{}, IC<(I & 3)>{}, IC<0>{});
              b_rd(IC<(I >> 2)>{}, IC<(I & 3)>{}, IC<1>{});
            }
            if constexpr (I == 5) {
              *(LAS v2u*)(L + hwa) = v2u{pk(ah[0], ah[1]), pk(ah[2], ah[3])};
            }
            if constexpr (I == 8) {
              pack_kv(1);
              *(LAS bf16x8*)(L + nwa) = kf[1];
              *(LAS v4u*)(L + nwa + 1024u) = v4u{vh[1][0][0], vh[1][0][1], vh[1][1][0], vh[1][1][1]};
            }
            if constexpr (I == 10) {
#pragma unroll
              for (int t2 = 0; t2 < 2; ++t2) {
                gv[t2] = rbf(__builtin_amdgcn_rcpf(1.0f + __builtin_amdgcn_exp2f(rbf(ac[6][t2][0]) * -1.4426950408889634f)));
                og[t2] = fmaxf(rbf(1.0f - gv[t2]), 9.094947017729282e-13f);
                keep(ac[6][t2]);
                pin(gv[t2]);
                pin(og[t2]);
              }
            }
            if constexpr (I == 11) {
              wait_l();
              bar();
              kp = lds128(L, wreg + 4096u + lane * 16u);
              if (tq == 0) vp = __builtin_bit_cast(v4u, cat(ldstr(L, wreg + 5120u + lane * 8u), ldstr(L, wreg + 5632u + lane * 8u)));
              else vp = *(LAS v4u*)(L + wreg + 5120u + lane * 16u);
              pack_q(1);
            }
            if constexpr (I == 13) pack_q(0);
          }
          SCHED();
          d_go(IC<E>{});
        });
        if constexpr (hs == 0) mid(IC<(HB >> 1)>{});
      });
      const f32x4 z = {0.f, 0.f, 0.f, 0.f};
      f32x4 s0[2], s1[2];
      s1[1] = mfma(kf[1], qf[1], z);
      s0[0] = mfma(kp, qf[0], z);
      SCHED();
      asm volatile("s_nop 4");
      SCHED();
      pack_kv(0);
      s0[1] = mfma(kf[0], qf[1], z);
      s1[0] = mfma(kf[0], qf[0], z);
      const bool odd = li & 1;
      const unsigned psh = odd ? 16u : 0u;
      float sv[2][4];
      auto pick = [&](f32x4 v, int u) {
        float lo = v[2 * u], hi = v[2 * u + 1];
        asm("" : "+v"(hi));
        return odd ? hi : lo;
      };
      const float SC = 0.18898223650461362f * 1.4426950408889634f;
#pragma unroll
      for (int u = 0; u < 2; ++u) {
        sv[1][2 + u] = __builtin_fmaf(pick(s1[1], u), SC, mb[1][2 + u]);
        sv[0][u] = __builtin_fmaf(pick(s0[0], u), SC, mb[0][u]);
      }
#pragma unroll
      for (int u = 0; u < 2; ++u) {
        sv[1][u] = __builtin_fmaf(pick(s0[1], u), SC, mb[1][u]);
        sv[0][2 + u] = __builtin_fmaf(pick(s1[0], u), SC, mb[0][2 + u]);
      }
      float m[2];
      {
        const float m0 = fmaxf(fmaxf(sv[0][0], sv[0][1]), fmaxf(sv[0][2], sv[0][3])), m1 = fmaxf(fmaxf(sv[1][0], sv[1][1]), fmaxf(sv[1][2], sv[1][3]));
        auto a = __builtin_amdgcn_permlane32_swap(__float_as_uint(m0), __float_as_uint(m1), false, false);
        const float x = fmx(__uint_as_float(a[0]), __uint_as_float(a[1]));
        auto b2 = __builtin_amdgcn_permlane16_swap(__float_as_uint(x), __float_as_uint(x), false, false);
        const float y = fmx(__uint_as_float(b2[0]), __uint_as_float(b2[1]));
        auto c2 = __builtin_amdgcn_permlane32_swap(__float_as_uint(y), __float_as_uint(y), false, false);
        m[0] = __uint_as_float(c2[0]);
        m[1] = __uint_as_float(c2[1]);
      }
      const bf16x8 ones = asfrag(0x3f803f80u, 0x3f803f80u, 0x3f803f80u, 0x3f803f80u);
      f32x4 o[2][2], sm[2];
#pragma unroll
      for (int t = 0; t < 2; ++t) {
        unsigned pw4[4];
#pragma unroll
        for (int j = 0; j < 4; ++j) pw4[j] = pk(__builtin_amdgcn_exp2f(sv[t][j] - m[t]), 0.f) << psh;
        const bf16x8 pf = asfrag(pw4[0], pw4[1], pw4[2], pw4[3]);
        sm[t] = mfma(ones, pf, z);
#pragma unroll
        for (int c = 0; c < 2; ++c) {
          const bf16x8 vf = t == 0 ? asfrag(vp[2 * c], vp[2 * c + 1], vh[0][c][0], vh[0][c][1]) : asfrag(vh[0][c][0], vh[0][c][1], vh[1][c][0], vh[1][c][1]);
          o[t][c] = mfma(vf, pf, z);
        }
      }
      v4u ov[2];
#pragma unroll
      for (int t = 0; t < 2; ++t) {
        const float sc = gv[t] * __builtin_amdgcn_rcpf(sm[t][0] * og[t]);
        const f32x4 o0 = o[t][0] * sc, o1 = o[t][1] * sc;
        ov[t] = v4u{pk(o0[0], o0[1]), pk(o0[2], o0[3]), pm & pk(o1[0], o1[1]), pm & pk(o1[2], o1[3])};
      }
      SCHED();
      st128(ov[0], rws, lane * 16u, oa);
      st128(ov[1], rws, lane * 16u, oa + A2_TT);
      if (hd == 0 && g < 2)
        __builtin_amdgcn_raw_buffer_store_b32(__float_as_uint(g ? og[1] : og[0]), rws, lane * 4u, WS_G + hp * 8192u + (128 * rb + 32 * tq) * 4u, 16);
    }
  };
  if (ffn) phase1(IC<1>{});
  else if (qkv) phase1(IC<0>{});
  else {
    if (w >= 3) do_touch();
    rep<0, 7>([&](auto K_) { setup2(K_, IC<0>{}); });
    rep<0, 16>(b_dma);
    wait_v<0>();
    rd_step(IC<0>{});
    rd_step(IC<1>{});
    fo_pub = WS_F + 15 * FSEC;
  }
  ct = __builtin_amdgcn_readfirstlane(ct);
  tt0 = __builtin_amdgcn_readfirstlane(tt0);

  const u64 tag = (gm_dispatch_id() + 1) * 0x9E3779B97F4A7C15ull ^ (u64)__builtin_amdgcn_dispatch_ptr();
  unsigned va[4];
#pragma unroll
  for (int t = 0; t < 4; ++t) va[t] = opaque(lane * 16u + t * A2_TT);
  const unsigned aob = tt0 * A2_TT + (16u + 7u * w) * 1024u, aoa = tt0 * A2_TT + 2u * w * 1024u;
  const unsigned frow = WS_F + (tt0 >> 3) * FROW, frows = (tt0 & 7) == 7 ? 1u : 0u;
  SCHED();
  if (!qkv) {
    wait_l();
    bar();
    rep<16, 16 + NMID>(b_dma);
  } else {
    rep<0, NMID>(r_dma);
  }
  wait_v<NMID>();
  rd_step(IC<2>{});
  rd_step(IC<3>{});
  SCHED();
  bar();
  if (tid < 4) {
    const v2q tv = {tag, tag};
    st128(__builtin_bit_cast(v4u, tv), rws, lane * 16u, fo_pub);
  }
  const unsigned oct = (li & 4) | ((li & 1) << 1) | ((li >> 1) & 1);
  v4u b2v;
  ld128<0>(b2v, oct * 16u, mk(p.b2, D * 2), ct * 128u);
  wait_l();
  if (!qkv) rep<16 + NMID, 36>(b_dma);
  else rep<NMID, 8>(r_dma);

  const unsigned fln = opaque(li * FSEC + g * FROW);
  auto poll = [&](bool todo) {
#pragma unroll 1
    for (unsigned spin = 0; spin < SPIN_CAP; ++spin) {
      if (todo) {
        const v2u v = __builtin_amdgcn_raw_buffer_load_b64(rws, fln, frow, 16);
        todo = __builtin_bit_cast(u64, v) != tag;
      }
      asm volatile("" ::: "memory");
      if (!__any(todo)) break;
      __builtin_amdgcn_s_sleep(1);
      if (!ffn && !qkv) __builtin_amdgcn_s_sleep(12);
    }
  };
  poll(li >= ((7 * w) >> 3) && li <= ((7 * w + 6) >> 3) && g <= frows);
  unsigned ra[2][2], oo = 0;
  bf16x8 sel[4];
  f32x4 blo, bhi;
  float gl;
  auto final_setup = [&](auto K_) {
    constexpr int K = decltype(K_)::value;
    if constexpr (K < 2) {
#pragma unroll
      for (int e = 0; e < 2; ++e)
        ra[K][e] = opaque((q4 + 4u * e) * REG + (w >> 1) * 2048u + ((g >> 1) * 32u + (p4 | ((((K | ((w & 1u) << 1)) + q4) & 3u) << 2))) * 16u + (g & 1) * 8u);
    } else if constexpr (K == 2) {
      const unsigned tk = 8 * w + 2 * g + (li >> 3);
      oo = opaque((16 * tt0 + tk) * 896u + ct * 128u + oct * 16u);
    } else if constexpr (K == 4 || K == 5) {
#pragma unroll
      for (int u = 2 * (K - 4); u < 2 * (K - 4) + 2; ++u) {
        const unsigned one = li == 4 * u + g ? 0x3f803f80u : 0u;
        sel[u] = asfrag(one, one, one, one);
        pin(sel[u]);
      }
    } else if constexpr (K == 6) {
      pin(b2v);
      blo = f32x4{bflo(b2v[0]), bfhi(b2v[0]), bflo(b2v[1]), bfhi(b2v[1])};
      bhi = f32x4{bflo(b2v[2]), bfhi(b2v[2]), bflo(b2v[3]), bfhi(b2v[3])};
      pin(blo);
      pin(bhi);
    }
  };
  auto phase2 = [&](auto NT_) {
    constexpr int NT = decltype(NT_)::value;
    v4u af[2][NT], aa[2][NT];
    v2u fl = {(unsigned)tag, (unsigned)(tag >> 32)};
    const bool fme = li == 7 + w && g <= frows;
    auto a_load = [&](auto J_, auto T_) {
      constexpr int j = decltype(J_)::value, t = decltype(T_)::value;
      if constexpr (j < 4) ld128<j * 1024u>(af[j & 1][t], va[t], rws, aob);
      else if constexpr (j < 7) ld128<(j - 4) * 1024u>(af[j & 1][t], va[t], rws, aob + 4096u);
      else ld128<(j - 7) * 1024u>(aa[j - 7][t], va[t], rws, aoa);
    };
    __builtin_amdgcn_s_setprio(3);
    rep<0, 2 * NT>([&](auto I_) { constexpr int I = decltype(I_)::value; a_load(IC<I / NT>{}, IC<I % NT>{}); });
    f32x4 acc[4][NT];
    unsigned pa = 0;
    auto put = [&](int t) {
#pragma unroll
      for (int cp = 0; cp < 2; ++cp) {
        const f32x4 x = acc[2 * cp][t], y = acc[2 * cp + 1][t];
        *(LAS v4u*)(L + pa + (t * 2 + cp) * 1024u) = v4u{pk(x[0], x[1]), pk(x[2], x[3]), pk(y[0], y[1]), pk(y[2], y[3])};
      }
    };
    rep<0, 7>([&](auto J_) {
      constexpr int j = decltype(J_)::value;
      if constexpr (j == 2 || j == 4 || j == 6) __builtin_amdgcn_s_setprio(3 - j / 2);
      if constexpr (j == 5) {
        wait_for<p2_before(5, 0, NT) - p2_before(3, 1, NT)>(fl);
        const bool todo = __builtin_bit_cast(u64, fl) != tag;
        if (__any(todo)) poll(todo);
        SCHED();
      }
      if constexpr (j == 6) pa = opaque(wreg + (g * 16u + (((li >> 1) & 3u) | (((((li & 1u) | ((li >> 3) << 1)) + w) & 3u) << 2))) * 16u);
      if constexpr (j >= 4) wait_l();
      rep<0, NT>([&](auto T_) {
        constexpr int t = decltype(T_)::value, U = p2_unit(j, t, NT);
        if constexpr (U >= 0) b_m0(IC<U>{});
        wait_v<p2_before(j, t, NT) - p2_pos(j, t, NT) - 1>();
        const bf16x8 a = __builtin_bit_cast(bf16x8, af[j & 1][t]);
#pragma unroll
        for (int c = 0; c < 4; ++c) {
          if constexpr (j == 0) mma0(acc[c][t], cat(bl[0][c], bh[0][c]), a);
          else mma(acc[c][t], cat(bl[j & 3][c], bh[j & 3][c]), a);
        }
        SCHED();
        if constexpr (U >= 0) b_go(IC<U>{});
        if constexpr (j < 6) a_load(IC<j + 2>{}, T_);
        if constexpr (j == 5) a_load(IC<8>{}, T_);
        if constexpr (j == 5 && t == 0) ld32(gl, (8 * w + 2 * g + (li >> 3)) * 4u, rws, WS_G + w * 8192u + tt0 * 64u);
        if constexpr (j == 3 && t == 0) { if (fme) ld64(fl, fln, rws, frow); }
        if constexpr (j >= 2) {
          b_rd(IC<j + 2>{}, T_, IC<0>{});
          b_rd(IC<j + 2>{}, T_, IC<1>{});
          if constexpr (NT == 3 && t == 2) {
            b_rd(IC<j + 2>{}, IC<3>{}, IC<0>{});
            b_rd(IC<j + 2>{}, IC<3>{}, IC<1>{});
          }
        }
        if constexpr (j == 6) final_setup(T_);
        SCHED();
      });
    });
    constexpr int NALL = p2_before(6, 0, NT);
    wait_l();
    rep<0, NT>([&](auto T_) {
      constexpr int t = decltype(T_)::value;
      wait_v<NALL - p2_pos(8, t, NT) - 1>();
#pragma unroll
      for (int c = 0; c < 4; ++c) mma(acc[c][t], cat(bl[3][c], bh[3][c]), __builtin_bit_cast(bf16x8, aa[0][t]));
#pragma unroll
      for (int c = 0; c < 4; ++c) mma(acc[c][t], cat(bl[0][c], bh[0][c]), __builtin_bit_cast(bf16x8, aa[1][t]));
      SCHED();
      if constexpr (t >= 1) put(t - 1);
      final_setup(IC<4 + t>{});
      SCHED();
    });
    asm volatile("s_nop 7" ::: "memory");
    SCHED();
    put(NT - 1);
  };
  if (qkv) phase2(IC<3>{});
  else phase2(IC<4>{});
  wait_l();
  bar();
  if (!qkv || w < 6) {
    pin(gl);
    bf16x8 pf[2][4];
#pragma unroll
    for (int u = 0; u < 4; ++u)
#pragma unroll
      for (int a = 0; a < 2; ++a) pf[a][u] = cat(ldstr(L, ra[u >> 1][0] + (u & 1) * 1024u + a * 256u), ldstr(L, ra[u >> 1][1] + (u & 1) * 1024u + a * 256u));
    f32x4 alo = blo, ahi = bhi;
#pragma unroll
    for (int u = 0; u < 4; ++u) {
      alo = mfma(pf[0][u], sel[u], alo);
      ahi = mfma(pf[1][u], sel[u], ahi);
    }
    alo *= gl;
    ahi *= gl;
    const v4u o = {pk(alo[0], alo[1]), pk(alo[2], alo[3]), pk(ahi[0], ahi[1]), pk(ahi[2], ahi[3])};
    *(GAS v4u*)((char*)p.out + oo) = o;
  }
}
}

extern "C" void kernel_launch(void* const* d_in, const int* in_sizes, int n_in, void* d_out, int out_size, void* d_ws, size_t ws_size,
                              hipStream_t stream) {
  gm::Params p;
  p.x = d_in[0];
  p.wqkv = d_in[1];
  p.wo = d_in[2];
  p.w1 = d_in[4];
  p.b1 = d_in[5];
  p.w2 = d_in[6];
  p.b2 = d_in[7];
  p.gw = d_in[8];
  p.out = d_out;
  p.ws = d_ws;
  gm::gm_kernel<<<256, 512, 0, stream>>>(p);
}
```

```cpp
#include <hip/hip_runtime.h>
#include <stdint.h>

typedef short bf16x8 __attribute__((ext_vector_type(8)));
typedef short s16x4 __attribute__((ext_vector_type(4)));
typedef float f32x4 __attribute__((ext_vector_type(4)));
typedef unsigned v4u __attribute__((ext_vector_type(4)));
typedef unsigned v2u __attribute__((ext_vector_type(2)));
typedef __bf16 bf2_t __attribute__((ext_vector_type(2)));
typedef float f32x2 __attribute__((ext_vector_type(2)));
typedef unsigned long long u64;
typedef u64 v2q __attribute__((ext_vector_type(2)));
typedef __amdgpu_buffer_rsrc_t rsrc_t;
#define LAS __attribute__((address_space(3)))
#define GAS __attribute__((address_space(1)))
#define DI __device__ __forceinline__

extern "C" __device__ u64 gm_dispatch_id() __asm("llvm.amdgcn.dispatch.id");

namespace gm {
constexpr int S = 2048, D = 448, F = 1792, NQKV = 1344;
constexpr int KS_A = 16, KS = 72;
constexpr unsigned A2_TT = KS * 1024;
constexpr unsigned FROW = 4352, FSEC = 256;
constexpr unsigned WS_G = 128 * A2_TT, WS_F = WS_G + 8 * 8192, WS_END = WS_F + 16 * FROW;
constexpr unsigned REG = 20480, STG = 6144, SPARE = 3 * STG;
constexpr int LDS_BYTES = 163840;
constexpr unsigned GW_OFF = 2 * REG + 5120;
constexpr unsigned SPIN_CAP = 1u << 18;
constexpr unsigned CODE_KB = 23;
constexpr int NMID = 4;

struct Params {
  const void *x, *wqkv, *wo, *w1, *b1, *w2, *b2, *gw;
  void *out, *ws;
};
template <int N> struct IC { static constexpr int value = N; };
template <int I, int N, class Fn> DI void rep(Fn&& f) {
  if constexpr (I < N) {
    f(IC<I>{});
    rep<I + 1, N>(f);
  }
}

#define SCHED() __builtin_amdgcn_sched_barrier(0)
#define NOP1() asm volatile("s_nop 0")
DI rsrc_t mk(const void* p, unsigned n) { return __builtin_amdgcn_make_buffer_rsrc((void*)p, 0, n, 0x00020000); }
template <unsigned C> DI void set_m0(unsigned base) { asm volatile("s_add_u32 m0, %0, %1" ::"s"(base), "i"(C) : "scc", "memory"); }
template <unsigned IMM> DI void dma_go(unsigned voff, rsrc_t rs, unsigned soff) {
  asm volatile("buffer_load_dwordx4 %0, %1, %2 offen offset:%3 lds" ::"v"(voff), "s"(rs), "s"(soff), "i"(IMM) : "memory");
}
template <unsigned IMM> DI void dma_go0(unsigned voff, rsrc_t rs) {
  asm volatile("buffer_load_dwordx4 %0, %1, 0 offen offset:%2 lds" ::"v"(voff), "s"(rs), "i"(IMM) : "memory");
}
DI void touch(unsigned lds, unsigned voff, u64 base) {
  asm volatile("s_mov_b32 m0, %0\n\ts_nop 0\n\tglobal_load_lds_dword %1, %2" ::"s"(lds), "v"(voff), "s"(base) : "memory");
}
template <unsigned IMM> DI void ld128(v4u& d, unsigned voff, rsrc_t rs, unsigned soff) {
  asm volatile("buffer_load_dwordx4 %0, %1, %2, %3 offen offset:%4 sc1" : "=v"(d) : "v"(voff), "s"(rs), "s"(soff), "i"(IMM) : "memory");
}
DI void ld32(float& d, unsigned voff, rsrc_t rs, unsigned soff) {
  asm volatile("buffer_load_dword %0, %1, %2, %3 offen sc1" : "=v"(d) : "v"(voff), "s"(rs), "s"(soff) : "memory");
}
DI void ld64(v2u& d, unsigned voff, rsrc_t rs, unsigned soff) {
  asm volatile("buffer_load_dwordx2 %0, %1, %2, %3 offen sc1" : "+v"(d) : "v"(voff), "s"(rs), "s"(soff) : "memory");
}
DI void st128(v4u v, rsrc_t rs, unsigned voff, unsigned soff) {
  __builtin_amdgcn_raw_buffer_store_b128(v, rs, voff, soff, 16);
  SCHED();
  asm volatile("s_nop 1");
  SCHED();
}
template <int N> DI void wait_v() { asm volatile("s_waitcnt vmcnt(%0)" ::"i"(N) : "memory"); }
template <int N, class T> DI void wait_for(T& v) { asm volatile("s_waitcnt vmcnt(%1)" : "+v"(v) : "i"(N) : "memory"); }
DI void wait_l() {
  SCHED();
  __builtin_amdgcn_s_waitcnt(0xC07F);
  asm volatile("" ::: "memory");
  SCHED();
}
DI void bar() { asm volatile("s_barrier" ::: "memory"); }
DI f32x4 mfma(bf16x8 a, bf16x8 b, f32x4 c) { return __builtin_amdgcn_mfma_f32_16x16x32_bf16(a, b, c, 0, 0, 0); }
DI void mma(f32x4& c, bf16x8 a, bf16x8 b) { asm volatile("v_mfma_f32_16x16x32_bf16 %0, %1, %2, %0" : "+v"(c) : "v"(a), "v"(b)); }
DI void mma0(f32x4& c, bf16x8 a, bf16x8 b) { asm volatile("v_mfma_f32_16x16x32_bf16 %0, %1, %2, 0" : "=&v"(c) : "v"(a), "v"(b)); }
DI void mmac(f32x4& d, bf16x8 a, bf16x8 b, f32x4 c) { asm volatile("v_mfma_f32_16x16x32_bf16 %0, %1, %2, %3" : "=&v"(d) : "v"(a), "v"(b), "v"(c)); }
DI float relu(float v) {
  float r;
  asm("v_max_f32 %0, 0, %1" : "=v"(r) : "v"(v));
  return r;
}
DI float fmx(float a, float b) {
  float r;
  asm("v_max_f32 %0, %1, %2" : "=v"(r) : "v"(a), "v"(b));
  return r;
}
DI unsigned pk(float lo, float hi) {
  f32x2 v = {lo, hi};
  return __builtin_bit_cast(unsigned, __builtin_convertvector(v, bf2_t));
}
DI float rbf(float f) { return __uint_as_float(pk(f, 0.f) << 16); }
DI float bflo(unsigned u) { return __uint_as_float(u << 16); }
DI float bfhi(unsigned u) { return __uint_as_float(u & 0xffff0000u); }
DI bf16x8 asfrag(unsigned a, unsigned b, unsigned c, unsigned d) {
  v4u v = {a, b, c, d};
  return __builtin_bit_cast(bf16x8, v);
}
DI bf16x8 cat(s16x4 lo, s16x4 hi) { return __builtin_shufflevector(lo, hi, 0, 1, 2, 3, 4, 5, 6, 7); }
DI bf16x8 lds128(LAS char* L, unsigned a) { return *(LAS bf16x8*)(L + a); }
DI s16x4 ldstr(LAS char* L, unsigned a) { return __builtin_amdgcn_ds_read_tr16_b64_v4i16((LAS s16x4*)(L + a)); }
DI unsigned opaque(unsigned v) {
  asm volatile("" : "+v"(v));
  return v;
}
DI unsigned sopaque(unsigned v) {
  asm volatile("" : "+s"(v));
  return v;
}
template <class T> DI void pin(T& v) { asm volatile("" : "+v"(v)); }
DI void spin(rsrc_t r) { asm volatile("" ::"s"(r)); }
DI void keep(f32x4 v) { asm volatile("" ::"v"(v)); }


constexpr unsigned hloc(int h) {
  h = h >= 10 ? h - 10 : h;
  return h == 0 ? SPARE : (h < 4 ? STG + (h - 1) * 2048u : (h < 7 ? 2 * STG + (h - 4) * 2048u : (h - 7) * 2048u));
}
constexpr unsigned ppos(int i) { return i < 2 ? i + 1 : (i == 2 ? 0 : i); }

constexpr int q_item(int i, bool ffn) { return i < 36 ? 8 * (1 + i / 6) + (ffn ? i % 6 : (i % 6 + 5) % 6) : 100 + (i - 36); }
constexpr int q_from(int item) {
  if (item < 100) return (item >> 3) <= 2 ? 0 : 2 * ((item >> 3) - 3) + 1;
  const int h = (item - 100) >> 1;
  return h == 0 ? 1 : (h < 4 ? 9 : (h < 7 ? 11 : 13));
}
constexpr int q_start(int hb) {
  int i = 6;
  for (int h = 0; h < hb; ++h)
    for (int k = 0; k < 4; ++k)
      if (i < 52 && q_from(q_item(i, true)) <= h) ++i;
  return i;
}
constexpr int q_at(int hb, int i, bool ffn) {
  return (i & 3) == 1 && q_start(hb) + (i >> 2) < q_start(hb + 1) ? q_item(q_start(hb) + (i >> 2), ffn) : -1;
}
constexpr int q_count(int from, int to, bool ffn) {
  int n = 0;
  for (int i = from; i < to; ++i) n += (ffn || i >= 36 || i % 6 != 0) ? 1 : 0;
  return n;
}
constexpr int q_wait(int s, bool ffn) { return q_count(s < 6 ? 6 * (s + 1) : 44, q_start(2 * s + 1), ffn); }
static_assert(q_start(14) == 52 && q_start(3) >= 12 && q_start(5) >= 18 && q_start(7) >= 24 && q_start(9) >= 30 && q_start(11) >= 36 &&
                  q_start(13) >= 44 && q_start(13) - q_start(12) <= 2 && q_start(14) - q_start(13) <= 2,
              "queue order");
constexpr int r_unit(int i) { return i < 2 ? 16 + i : (i < 6 ? 18 + i : 12 + i); }

constexpr int p2_unit(int j, int t, int nt) { return nt == 3 && 3 * j + t < 12 ? 24 + 3 * j + t : -1; }
constexpr int p2_after(int j, int t, int nt) {
  return (p2_unit(j, t, nt) >= 0 ? 1 : 0) + (j < 5 ? 1 : (j == 5 ? 2 : 0))
         + (j == 5 && t == 0 ? 1 : 0)
         + (j == 3 && t == 0 ? 1 : 0);
}
constexpr int p2_before(int j, int t, int nt) {
  int n = 2 * nt;
  for (int i = 0; i < nt * j + t; ++i) n += p2_after(i / nt, i % nt, nt);
  return n;
}
constexpr int p2_pos(int j, int t, int nt) {
  return j < 2 ? nt * j + t : (j == 8 ? p2_before(5, t, nt) + 1 : p2_before(j - 2, t, nt) + (p2_unit(j - 2, t, nt) >= 0 ? 1 : 0));
}

__global__ __launch_bounds__(512, 2) void gm_kernel(Params p) {
  __shared__ __attribute__((aligned(1024))) char smem[LDS_BYTES];
  LAS char* L = (LAS char*)smem;
  u64 px, pw, pv, pt;
  asm volatile("s_load_dwordx2 %0, %1, 0x0" : "=&s"(px) : "s"(__builtin_amdgcn_kernarg_segment_ptr()));
  SCHED();
  const unsigned tid = threadIdx.x, lane = tid & 63;
  const unsigned b = blockIdx.x, xcd = b & 7, slot = b >> 3;
  const bool ffn = b < 112, qkv = b >= 128;
  const unsigned w = __builtin_amdgcn_readfirstlane(tid >> 6);
  asm volatile("s_load_dwordx2 %0, %3, %4\n\ts_load_dwordx2 %1, %3, %5\n\ts_load_dwordx2 %2, %3, %6"
               : "=&s"(pw), "=&s"(pv), "=&s"(pt)
               : "s"(__builtin_amdgcn_kernarg_segment_ptr()), "s"(qkv ? 8u : 24u), "s"(qkv ? 56u : 32u), "s"(((0x00130130u >> (4 * w)) & 7u) * 8u));
  if (tid < CODE_KB) {
    u64 pc;
    asm volatile("s_getpc_b64 %0" : "=s"(pc));
    touch(SPARE + 1024u, ((slot & 7) * CODE_KB + tid + 1) * 128u, pc);
  }
  const unsigned wreg = w * REG;
  const unsigned li = lane & 15, g = lane >> 4, rr = lane >> 3, pos = lane & 7;
  const unsigned rq = xcd >> 1, side = xcd & 1;
  const unsigned tcnt = (unsigned)(0x0000151C38101520ull >> (8 * w)) & 0xffu;
  const unsigned tfirst = ((unsigned)(0x00003F5400000000ull >> (8 * w)) & 0xffu) * 64u;
  const unsigned tsh = w ? 6u : 2u;
  const unsigned tln = opaque(((32 * xcd + slot) * (w ? tcnt : 56u) + tfirst + (lane >> tsh) * 7u + (lane & ((1u << tsh) - 1u))) * 128u);
  auto do_touch = [&]() {
    if (lane < tcnt) touch(wreg + SPARE + 1024u, tln, pt);
  };
  const bool mid3 = (slot >> 2) == 3;
  const unsigned rb = 4 * rq + (mid3 ? 2 * side + (slot & 1) : slot & 3), cs = mid3 ? 3u : side * 4 + ((slot >> 2) & 3);
  unsigned vx[2];
#pragma unroll
  for (int h = 0; h < 2; ++h) vx[h] = opaque((128 * rb + 16 * w + 8 * h + rr) * 896u + ((pos ^ (4u * h + (rr >> 1))) << 4));
  const unsigned vw = opaque((8 * w + rr) * (qkv ? 2688u : 3584u) + (min(pos ^ ((((rr >> 1) & 1) | ((w & 1) << 1)) << 1), qkv ? 6u : 7u) << 4) +
                             cs * (qkv ? 112u : 512u));
  SCHED();
  asm volatile("s_waitcnt lgkmcnt(0)" : "+s"(px), "+s"(pw), "+s"(pv), "+s"(pt)::"memory");
  SCHED();
  const rsrc_t rx = mk((const void*)px, S * D * 2);
  const rsrc_t rw = mk((const void*)pw, qkv ? D * NQKV * 2 : D * F * 2);
  if (w < 3) do_touch();
  if (ffn || qkv) {
    const unsigned wus = qkv ? 896u : 128u;
    rep<0, 5>([&](auto I_) {
      constexpr int I = decltype(I_)::value;
      set_m0<ppos(I) * 1024u>(wreg);
      NOP1();
      if constexpr (I < 2) dma_go0<0>(vx[I], rx);
      else dma_go<0>(vw, rw, (I - 2) * wus);
    });
  }
  SCHED();

  rsrc_t rws, rw2, rwo;
  unsigned vb[2], ba[4], fo_pub, ct = 0, tt0 = 0;
  const unsigned bo2 = w * (224u * 896u), boo = w * (56u * 896u);
  const unsigned q4 = (lane >> 2) & 3, p4 = lane & 3;
  const unsigned fsw = (q4 >> 1) | ((g & 1) << 1);
  s16x4 bl[4][4], bh[4][4];
  auto b_m0 = [&](auto N_) {
    constexpr int N = decltype(N_)::value;
    set_m0<hloc(N >> 1) + (N & 1) * 1024u>(wreg);
  };
  auto b_go = [&](auto N_) {
    constexpr int N = decltype(N_)::value, j = N >> 2;
    if constexpr (j < 7) dma_go<0>(vb[N & 1], rw2, bo2 + (N >> 1) * (16u * 896u));
    else dma_go<0>(vb[N & 1], rwo, boo + ((j - 7) * 28u + ((N >> 1) & 1) * 16u) * 896u);
  };
  auto b_dma = [&](auto N_) { b_m0(N_); NOP1(); b_go(N_); };
  auto r_dma = [&](auto I_) { b_dma(IC<r_unit(decltype(I_)::value)>{}); };
  auto b_rd = [&](auto J_, auto C_, auto E_) {
    constexpr int J = decltype(J_)::value, C = decltype(C_)::value, E = decltype(E_)::value;
    if constexpr (E == 0) bl[J & 3][C] = ldstr(L, ba[C] + hloc(2 * J));
    else bh[J & 3][C] = ldstr(L, ba[C] + hloc(2 * J + 1));
  };
  auto rd_step = [&](auto J_) { rep<0, 8>([&](auto I_) { constexpr int I = decltype(I_)::value; b_rd(J_, IC<(I >> 1)>{}, IC<(I & 1)>{}); }); };
  auto setup2 = [&](auto K_, auto Q_) {
    constexpr int K = decltype(K_)::value;
    constexpr bool Q = decltype(Q_)::value;
    if constexpr (K == 0) {
      ct = Q ? (slot >> 2) - 1 : (unsigned)max((int)(slot >> 2), (int)slot - 9);
      tt0 = 16 * xcd + (Q ? 4 + 3 * (slot & 3) : (slot < 12 ? 4 * (slot & 3) : 0u));
      rws = mk(p.ws, WS_END);
      rw2 = mk(p.w2, F * D * 2);
      rwo = mk(p.wo, D * D * 2);
    } else if constexpr (K == 2) {
      vb[0] = opaque(rr * 896u + ((pos ^ (((rr >> 1) & 3) << 1)) << 4) + ct * 128u);
      vb[1] = opaque(vb[0] + 8u * 896u);
    } else if constexpr (K >= 3 && K < 7) {
      ba[K - 3] = opaque(wreg + (4 * g + q4) * 128u + ((2u * ((K - 3) ^ fsw) + (p4 >> 1)) << 4) + (p4 & 1) * 8u);
    }
  };

  auto phase1 = [&](auto FFN_) {
    constexpr bool FFN = decltype(FFN_)::value;
    constexpr unsigned LDW = FFN ? 3584u : 2688u, WUS = FFN ? 128u : 896u;
    unsigned vh5 = 0;
    if (!FFN) vh5 = opaque(((rb ? 128 * rb - 16 : 0u) + 8 * (w & 1) + rr) * 896u + ((pos ^ (4u * (w & 1) + (rr >> 1))) << 4));
    auto d_m0 = [&](auto E_) {
      constexpr int E = decltype(E_)::value, I = E & 7, ST = E >> 3;
      if constexpr (E >= 100) b_m0(IC<E - 100>{});
      else if constexpr (E >= 0) set_m0<(ST % 3) * STG + ppos(I) * 1024u - ((I < 2 || (!FFN && I == 5)) ? ST * 128u : (I - 2) * WUS)>(wreg);
    };
    auto d_go = [&](auto E_) {
      constexpr int E = decltype(E_)::value, I = E & 7, ST = E >> 3;
      if constexpr (E >= 100) b_go(IC<E - 100>{});
      else if constexpr (E >= 0) {
        if constexpr (I < 2) dma_go0<ST * 128u>(vx[I], rx);
        else if constexpr (!FFN && I == 5) { if (w < 2) dma_go0<ST * 128u>(vh5, rx); }
        else if constexpr (ST == 0) dma_go0<(I - 2) * WUS>(vw, rw);
        else dma_go<(I - 2) * WUS>(vw, rw, ST * 64u * LDW);
      }
    };
    d_m0(IC<5>{});
    NOP1();
    d_go(IC<5>{});
    if (w == 2) {
      set_m0<FFN ? SPARE : GW_OFF>(0);
      NOP1();
      dma_go0<0>(lane * 16, mk((const char*)pv + (FFN ? cs * 512u : 0u), FFN ? 512 : 896));
    }
    SCHED();
    const unsigned xa0 = 1024u + (li >> 3) * 1024u + (li & 7) * 128u + ((g ^ ((li >> 1) & 7)) << 4);
    auto second_stage = [&]() {
      SCHED();
      bar();
      rep<0, 6>([&](auto I_) { constexpr int E = q_item(decltype(I_)::value, FFN); d_m0(IC<E>{}); NOP1(); d_go(IC<E>{}); });
      setup2(IC<0>{}, IC<!FFN>{});
      spin(rws);
      spin(rw2);
      spin(rwo);
      SCHED();
      if (w >= 3 && w < 6) {
        do_touch();
        wait_v<q_count(0, 6, FFN) + 1>();
      } else {
        wait_v<q_count(0, 6, FFN)>();
      }
      bar();
    };
    auto mid = [&](auto S_) {
      constexpr int s = decltype(S_)::value;
      if (s == 0 && w >= 3 && w < 6) wait_v<q_wait(s, FFN) + 1>();
      else wait_v<q_wait(s, FFN)>();
      wait_l();
      bar();
    };

    if constexpr (FFN) {
      const unsigned mh = w >> 2, nq = w & 3;
      unsigned xa[2][2], wa[2][4];
#pragma unroll
      for (int hs = 0; hs < 2; ++hs) {
#pragma unroll
        for (int t2 = 0; t2 < 2; ++t2) xa[hs][t2] = opaque((xa0 ^ (hs * 64u)) + (mh * 4u + t2 * 2u) * REG);
#pragma unroll
        for (int c = 0; c < 4; ++c)
          wa[hs][c] = opaque((4u * hs + g) * REG + (nq ? 2u + nq : 0u) * 1024u + q4 * 128u + ((2u * (c ^ fsw) + (p4 >> 1)) << 4) + (p4 & 1) * 8u);
      }
      const unsigned bia = opaque(SPARE + nq * 128u + g * 8u);
      second_stage();
      bf16x8 xf[2][4];
      s16x4 wl[2][4], wh[2][4];
      f32x4 acc[4][4];
      auto rd = [&](auto I_, auto HB_) {
        constexpr int I = decltype(I_)::value, HB = decltype(HB_)::value, hs = HB & 1;
        constexpr unsigned bo = ((HB >> 1) % 3) * STG;
        if constexpr (I >= 2 && I < 6) xf[hs][I - 2] = lds128(L, xa[hs][(I - 2) >> 1] + ((I - 2) & 1) * REG + bo);
        else if constexpr (I < 12) {
          constexpr int c = I < 2 ? 0 : (I - 4) >> 1;
          if constexpr ((I & 1) == 0) wl[hs][c] = ldstr(L, wa[hs][c] + bo);
          else wh[hs][c] = ldstr(L, wa[hs][c] + bo + 512u);
        }
      };
      f32x4 bv[4];
      v2u bb[4];
#pragma unroll
      for (int c = 0; c < 4; ++c) bb[c] = *(LAS v2u*)(L + bia + c * 32u);
      rep<0, 12>([&](auto I_) { rd(I_, IC<0>{}); });
      SCHED();
#pragma unroll
      for (int c = 0; c < 4; ++c) {
        bv[c] = f32x4{bflo(bb[c].x), bfhi(bb[c].x), bflo(bb[c].y), bfhi(bb[c].y)};
        pin(bv[c]);
      }
      unsigned so = 0;
      auto epi = [&](int t, int u) {
        float hv[8];
#pragma unroll
        for (int j = 0; j < 8; ++j) {
          const float r = relu(acc[2 * u + (j >> 2)][t][j & 3]);
          hv[j] = r * r;
        }
        const v4u o = {pk(hv[0], hv[1]), pk(hv[2], hv[3]), pk(hv[4], hv[5]), pk(hv[6], hv[7])};
        st128(o, rws, lane * 16u + u * 1024u, so + t * A2_TT);
      };
      rep<0, 12>([&](auto HB_) {
        constexpr int HB = decltype(HB_)::value, hs = HB & 1;
        if constexpr (HB == 0) asm volatile("s_nop 1");
        else if constexpr (hs == 0) wait_l();
        rep<0, 16>([&](auto I_) {
          constexpr int I = decltype(I_)::value;
          constexpr int E = q_at(HB, I, FFN);
          d_m0(IC<E>{});
          if constexpr (HB == 0) mmac(acc[I >> 2][I & 3], cat(wl[hs][I >> 2], wh[hs][I >> 2]), xf[hs][I & 3], bv[I >> 2]);
          else mma(acc[I >> 2][I & 3], cat(wl[hs][I >> 2], wh[hs][I >> 2]), xf[hs][I & 3]);
          SCHED();
          rd(I_, IC<HB + 1>{});
          if constexpr (I == 14 && HB > 0 && HB < 7) setup2(HB_, IC<0>{});
          if constexpr (I == 14 && HB == 7) {
            so = sopaque((8 * rb + 4 * mh) * A2_TT + (KS_A + 8 * cs + 2 * nq) * 1024u);
            fo_pub = sopaque(WS_F + rb * FROW + cs * FSEC);
          }
          SCHED();
          d_go(IC<E>{});
        });
        if constexpr (hs == 0) mid(IC<(HB >> 1)>{});
      });
      wait_l();
      rep<0, 32>([&](auto M_) {
        constexpr int M = decltype(M_)::value, G = M >> 2, c = M & 3;
        constexpr int hs = G < 4 ? G >> 1 : G & 1, t = G < 4 ? G & 1 : G >> 1;
        constexpr int E = M < 8 ? q_at(12, M, FFN) : (M < 16 ? q_at(13, M - 8, FFN) : -1);
        d_m0(IC<E>{});
        mma(acc[c][t], cat(wl[hs][c], wh[hs][c]), xf[hs][t]);
        SCHED();
        if constexpr (M < 6) { rd(IC<2 * M>{}, IC<13>{}); rd(IC<2 * M + 1>{}, IC<13>{}); }
        if constexpr (M >= 8 && M < 14) {
          rep<0, (M < 12 ? 3 : 2)>([&](auto Q_) {
            constexpr int Q = (M < 12 ? 3 * (M - 8) : 12 + 2 * (M - 12)) + decltype(Q_)::value;
            b_rd(IC<(Q >> 3)>{}, IC<((Q >> 1) & 3)>{}, IC<(Q & 1)>{});
          });
        }
        if constexpr (M >= 14 && M < 32 && (M - 14) % 3 == 0) epi((M - 14) / 6, ((M - 14) / 3) & 1);
        SCHED();
        d_go(IC<E>{});
        if constexpr (M == 7) mid(IC<6>{});
      });
      asm volatile("s_nop 5" ::: "memory");
      SCHED();
      epi(3, 0);
      epi(3, 1);
    } else {
      const unsigned tq = w & 3, hd = w >> 2, hp = cs;
      unsigned xa[2], wa[2][2], xh[2], wq[2];
#pragma unroll
      for (int hs = 0; hs < 2; ++hs) {
        xa[hs] = opaque((xa0 ^ (hs * 64u)) + tq * 2u * REG);
#pragma unroll
        for (int c = 0; c < 2; ++c) {
          const unsigned bo = 56u * hd + 32u * c + 8u * p4;
          wa[hs][c] = opaque((4u * hs + g) * REG + q4 * 128u + (((bo >> 4) ^ (fsw << 1)) << 4) + (bo & 8u));
        }
        xh[hs] = opaque((li >> 3) * REG + 5120u + (li & 7) * 128u + (((4u * hs + g) ^ ((li >> 1) & 7)) << 4));
        const unsigned bo = 56u * hd + 32u * (tq & 1) + 8u * p4;
        wq[hs] = opaque((4u * hs + g) * REG + q4 * 128u + (((bo >> 4) ^ (fsw << 1)) << 4) + (bo & 8u) + (3u + (tq >> 1)) * 1024u);
      }
      const unsigned ga = opaque(GW_OFF + g * 16u);
      second_stage();
      float mb[2][4];
      unsigned oa = 0, hwa = 0, nwa = 0, pm = 0;
      auto setupq = [&](auto K_) {
        constexpr int K = decltype(K_)::value, t = K & 1;
        if constexpr (K < 2) {
#pragma unroll
          for (int j = 0; j < 4; ++j) {
            const int kap = 16 * (j >> 1) + (int)(4 * g) + 2 * (j & 1) + (int)(li & 1) - 16;
            const int dl = (int)li - kap;
            const bool ok = (dl >= 0) && (dl <= 14) && (t == 1 || j >= 2 || (rb | tq) != 0);
            mb[t][j] = __uint_as_float(opaque(ok ? 0u : 0xff800000u));
          }
        } else if constexpr (K == 2) {
          oa = sopaque((8 * rb + 2 * tq) * A2_TT + (2 * hp + hd) * 1024u);
          fo_pub = sopaque(WS_F + rb * FROW + (7 + hp) * FSEC);
        } else if constexpr (K == 3) {
          hwa = opaque(hd * 4u * REG + (tq < 2 ? 4096u + lane * 16u + tq * 8u : 4096u + tq * 512u + li * 32u + g * 8u));
          nwa = opaque(wreg + (tq < 3 ? REG + 4096u : 2048u) + lane * 16u);
          pm = opaque(g == 3 ? 0u : 0xffffffffu);
        }
      };
      f32x4 ac[7][2], ah;
      bf16x8 xf[2][2], gf[2], hf[2];
      s16x4 wl[2][6], wh[2][6], hl[2], hh[2];
      auto rd = [&](auto I_, auto HB_) {
        constexpr int I = decltype(I_)::value, HB = decltype(HB_)::value, hs = HB & 1;
        constexpr unsigned bo = ((HB >> 1) % 3) * STG;
        if constexpr (I == 2 || I == 3) xf[hs][I - 2] = lds128(L, xa[hs] + (I - 2) * REG + bo);
        else if constexpr (I < 14) {
          constexpr int c = I < 2 ? 0 : (I - 2) >> 1;
          if constexpr ((I & 1) == 0) wl[hs][c] = ldstr(L, wa[hs][c & 1] + ppos(2 + (c >> 1)) * 1024u + bo);
          else wh[hs][c] = ldstr(L, wa[hs][c & 1] + ppos(2 + (c >> 1)) * 1024u + bo + 512u);
        } else if constexpr (I == 14) gf[hs] = lds128(L, ga + HB * 64u);
        else if constexpr (I == 15) hf[hs] = lds128(L, xh[hs] + bo);
        else if constexpr (I == 16) hl[hs] = ldstr(L, wq[hs] + bo);
        else if constexpr (I == 17) hh[hs] = ldstr(L, wq[hs] + bo + 512u);
      };
      bf16x8 qf[2], kf[2], kp;
      unsigned vh[2][2][2];
      float gv[2], og[2];
      v4u vp;
      auto pack_kv = [&](int t) {
        kf[t] = asfrag(pk(ac[2][t][0], ac[2][t][1]), pk(ac[2][t][2], ac[2][t][3]), pk(ac[3][t][0], ac[3][t][1]), pk(ac[3][t][2], ac[3][t][3]));
#pragma unroll
        for (int c = 0; c < 2; ++c) {
          vh[t][c][0] = pk(ac[4 + c][t][0], ac[4 + c][t][1]);
          vh[t][c][1] = pk(ac[4 + c][t][2], ac[4 + c][t][3]);
        }
      };
      auto pack_q = [&](int t) {
        qf[t] = asfrag(pk(ac[0][t][0], ac[0][t][1]), pk(ac[0][t][2], ac[0][t][3]), pm & pk(ac[1][t][0], ac[1][t][1]), pm & pk(ac[1][t][2], ac[1][t][3]));
        pin(qf[t]);
      };
      rep<0, 18>([&](auto I_) { rd(I_, IC<0>{}); });
      rep<0, 14>([&](auto HB_) {
        constexpr int HB = decltype(HB_)::value, hs = HB & 1;
        if constexpr (hs == 0 && HB > 0) wait_l();
        rep<0, 15>([&](auto I_) {
          constexpr int I = decltype(I_)::value;
          constexpr int c = HB < 13 ? I >> 1 : (I == 0 ? 7 : (I < 5 ? 1 + I : (I < 7 ? 6 : (I < 11 ? (I - 7) & 1 : I - 9))));
          constexpr int t = HB < 13 ? I & 1 : (I < 5 ? 1 : (I < 7 ? I - 5 : (I < 11 ? 1 - ((I - 7) >> 1) : 0)));
          constexpr int E = q_at(HB, I, FFN);
          d_m0(IC<E>{});
          if constexpr (c == 7) {
            if constexpr (HB == 0) mma0(ah, cat(hl[hs], hh[hs]), hf[hs]);
            else mma(ah, cat(hl[hs], hh[hs]), hf[hs]);
          } else {
            const bf16x8 wf = c < 6 ? cat(wl[hs][c < 6 ? c : 0], wh[hs][c < 6 ? c : 0]) : gf[hs];
            if constexpr (HB == 0) {
              if constexpr (c == 4 || c == 5) mma0(ac[c][t], xf[hs][t], wf);
              else mma0(ac[c][t], wf, xf[hs][t]);
            } else {
              if constexpr (c == 4 || c == 5) mma(ac[c][t], xf[hs][t], wf);
              else mma(ac[c][t], wf, xf[hs][t]);
            }
          }
          SCHED();
          if constexpr (HB < 13) {
            if constexpr (I < 7) { rd(IC<2 * I>{}, IC<HB + 1>{}); rd(IC<2 * I + 1>{}, IC<HB + 1>{}); }
            else rd(IC<I + 7>{}, IC<HB + 1>{});
            if constexpr (I == 12 && HB > 0 && HB < 7) setup2(HB_, IC<1>{});
            if constexpr (I == 12 && HB >= 7 && HB < 11) setupq(IC<HB - 7>{});
          } else {
            if constexpr (I < 8) {
              b_rd(IC<(I >> 2)>{}, IC<(I & 3)>{}, IC<0>{});
              b_rd(IC<(I >> 2)>{}, IC<(I & 3)>{}, IC<1>{});
            }
            if constexpr (I == 5) {
              *(LAS v2u*)(L + hwa) = v2u{pk(ah[0], ah[1]), pk(ah[2], ah[3])};
            }
            if constexpr (I == 8) {
              pack_kv(1);
              *(LAS bf16x8*)(L + nwa) = kf[1];
              *(LAS v4u*)(L + nwa + 1024u) = v4u{vh[1][0][0], vh[1][0][1], vh[1][1][0], vh[1][1][1]};
            }
            if constexpr (I == 10) {
#pragma unroll
              for (int t2 = 0; t2 < 2; ++t2) {
                gv[t2] = rbf(__builtin_amdgcn_rcpf(1.0f + __builtin_amdgcn_exp2f(rbf(ac[6][t2][0]) * -1.4426950408889634f)));
                og[t2] = fmaxf(rbf(1.0f - gv[t2]), 9.094947017729282e-13f);
                keep(ac[6][t2]);
                pin(gv[t2]);
                pin(og[t2]);
              }
            }
            if constexpr (I == 11) {
              wait_l();
              bar();
              kp = lds128(L, wreg + 4096u + lane * 16u);
              if (tq == 0) vp = __builtin_bit_cast(v4u, cat(ldstr(L, wreg + 5120u + lane * 8u), ldstr(L, wreg + 5632u + lane * 8u)));
              else vp = *(LAS v4u*)(L + wreg + 5120u + lane * 16u);
              pack_q(1);
            }
            if constexpr (I == 13) pack_q(0);
          }
          SCHED();
          d_go(IC<E>{});
        });
        if constexpr (hs == 0) mid(IC<(HB >> 1)>{});
      });
      const f32x4 z = {0.f, 0.f, 0.f, 0.f};
      f32x4 s0[2], s1[2];
      s1[1] = mfma(kf[1], qf[1], z);
      s0[0] = mfma(kp, qf[0], z);
      SCHED();
      asm volatile("s_nop 4");
      SCHED();
      pack_kv(0);
      s0[1] = mfma(kf[0], qf[1], z);
      s1[0] = mfma(kf[0], qf[0], z);
      const bool odd = li & 1;
      const unsigned psh = odd ? 16u : 0u;
      float sv[2][4];
      auto pick = [&](f32x4 v, int u) {
        float lo = v[2 * u], hi = v[2 * u + 1];
        asm("" : "+v"(hi));
        return odd ? hi : lo;
      };
      const float SC = 0.18898223650461362f * 1.4426950408889634f;
#pragma unroll
      for (int u = 0; u < 2; ++u) {
        sv[1][2 + u] = __builtin_fmaf(pick(s1[1], u), SC, mb[1][2 + u]);
        sv[0][u] = __builtin_fmaf(pick(s0[0], u), SC, mb[0][u]);
      }
#pragma unroll
      for (int u = 0; u < 2; ++u) {
        sv[1][u] = __builtin_fmaf(pick(s0[1], u), SC, mb[1][u]);
        sv[0][2 + u] = __builtin_fmaf(pick(s1[0], u), SC, mb[0][2 + u]);
      }
      float m[2];
      {
        const float m0 = fmaxf(fmaxf(sv[0][0], sv[0][1]), fmaxf(sv[0][2], sv[0][3])), m1 = fmaxf(fmaxf(sv[1][0], sv[1][1]), fmaxf(sv[1][2], sv[1][3]));
        auto a = __builtin_amdgcn_permlane32_swap(__float_as_uint(m0), __float_as_uint(m1), false, false);
        const float x = fmx(__uint_as_float(a[0]), __uint_as_float(a[1]));
        auto b2 = __builtin_amdgcn_permlane16_swap(__float_as_uint(x), __float_as_uint(x), false, false);
        const float y = fmx(__uint_as_float(b2[0]), __uint_as_float(b2[1]));
        auto c2 = __builtin_amdgcn_permlane32_swap(__float_as_uint(y), __float_as_uint(y), false, false);
        m[0] = __uint_as_float(c2[0]);
        m[1] = __uint_as_float(c2[1]);
      }
      const bf16x8 ones = asfrag(0x3f803f80u, 0x3f803f80u, 0x3f803f80u, 0x3f803f80u);
      f32x4 o[2][2], sm[2];
#pragma unroll
      for (int t = 0; t < 2; ++t) {
        unsigned pw4[4];
#pragma unroll
        for (int j = 0; j < 4; ++j) pw4[j] = pk(__builtin_amdgcn_exp2f(sv[t][j] - m[t]), 0.f) << psh;
        const bf16x8 pf = asfrag(pw4[0], pw4[1], pw4[2], pw4[3]);
        sm[t] = mfma(ones, pf, z);
#pragma unroll
        for (int c = 0; c < 2; ++c) {
          const bf16x8 vf = t == 0 ? asfrag(vp[2 * c], vp[2 * c + 1], vh[0][c][0], vh[0][c][1]) : asfrag(vh[0][c][0], vh[0][c][1], vh[1][c][0], vh[1][c][1]);
          o[t][c] = mfma(vf, pf, z);
        }
      }
      v4u ov[2];
#pragma unroll
      for (int t = 0; t < 2; ++t) {
        const float sc = gv[t] * __builtin_amdgcn_rcpf(sm[t][0] * og[t]);
        const f32x4 o0 = o[t][0] * sc, o1 = o[t][1] * sc;
        ov[t] = v4u{pk(o0[0], o0[1]), pk(o0[2], o0[3]), pm & pk(o1[0], o1[1]), pm & pk(o1[2], o1[3])};
      }
      SCHED();
      st128(ov[0], rws, lane * 16u, oa);
      st128(ov[1], rws, lane * 16u, oa + A2_TT);
      if (hd == 0 && g < 2)
        __builtin_amdgcn_raw_buffer_store_b32(__float_as_uint(g ? og[1] : og[0]), rws, lane * 4u, WS_G + hp * 8192u + (128 * rb + 32 * tq) * 4u, 16);
    }
  };
  if (ffn) phase1(IC<1>{});
  else if (qkv) phase1(IC<0>{});
  else {
    if (w >= 3) do_touch();
    rep<0, 7>([&](auto K_) { setup2(K_, IC<0>{}); });
    __builtin_amdgcn_s_sleep(48);
    {
      const unsigned fw = (xcd * 2 + (slot & 1)) * 8 + w;
      if (lane < 49) {
        touch(wreg + SPARE + 1024u, (fw * 98u + lane) * 128u, (u64)p.w2);
        touch(wreg + SPARE + 1024u, (fw * 98u + 49u + lane) * 128u, (u64)p.w2);
      }
      if (lane < 25) touch(wreg + SPARE + 1024u, min(fw * 25u + lane, 3135u) * 128u, (u64)p.wo);
    }
    rep<0, 16>(b_dma);
    wait_v<0>();
    rd_step(IC<0>{});
    rd_step(IC<1>{});
    fo_pub = WS_F + 15 * FSEC;
  }
  ct = __builtin_amdgcn_readfirstlane(ct);
  tt0 = __builtin_amdgcn_readfirstlane(tt0);

  const u64 tag = (gm_dispatch_id() + 1) * 0x9E3779B97F4A7C15ull ^ (u64)__builtin_amdgcn_dispatch_ptr();
  unsigned va[4];
#pragma unroll
  for (int t = 0; t < 4; ++t) va[t] = opaque(lane * 16u + t * A2_TT);
  const unsigned aob = tt0 * A2_TT + (16u + 7u * w) * 1024u, aoa = tt0 * A2_TT + 2u * w * 1024u;
  const unsigned frow = WS_F + (tt0 >> 3) * FROW, frows = (tt0 & 7) == 7 ? 1u : 0u;
  SCHED();
  if (!qkv) {
    wait_l();
    bar();
    rep<16, 16 + NMID>(b_dma);
  } else {
    rep<0, NMID>(r_dma);
  }
  wait_v<NMID>();
  rd_step(IC<2>{});
  rd_step(IC<3>{});
  SCHED();
  bar();
  if (tid < 4) {
    const v2q tv = {tag, tag};
    st128(__builtin_bit_cast(v4u, tv), rws, lane * 16u, fo_pub);
  }
  const unsigned oct = (li & 4) | ((li & 1) << 1) | ((li >> 1) & 1);
  v4u b2v;
  ld128<0>(b2v, oct * 16u, mk(p.b2, D * 2), ct * 128u);
  wait_l();
  if (!qkv) rep<16 + NMID, 36>(b_dma);
  else rep<NMID, 8>(r_dma);

  const unsigned fln = opaque(li * FSEC + g * FROW);
  auto poll = [&](bool todo) {
#pragma unroll 1
    for (unsigned spin = 0; spin < SPIN_CAP; ++spin) {
      if (todo) {
        const v2u v = __builtin_amdgcn_raw_buffer_load_b64(rws, fln, frow, 16);
        todo = __builtin_bit_cast(u64, v) != tag;
      }
      asm volatile("" ::: "memory");
      if (!__any(todo)) break;
      __builtin_amdgcn_s_sleep(1);
      if (!ffn && !qkv) __builtin_amdgcn_s_sleep(12);
    }
  };
  poll(li >= ((7 * w) >> 3) && li <= ((7 * w + 6) >> 3) && g <= frows);
  unsigned ra[2][2], oo = 0;
  bf16x8 sel[4];
  f32x4 blo, bhi;
  float gl;
  auto final_setup = [&](auto K_) {
    constexpr int K = decltype(K_)::value;
    if constexpr (K < 2) {
#pragma unroll
      for (int e = 0; e < 2; ++e)
        ra[K][e] = opaque((q4 + 4u * e) * REG + (w >> 1) * 2048u + ((g >> 1) * 32u + (p4 | ((((K | ((w & 1u) << 1)) + q4) & 3u) << 2))) * 16u + (g & 1) * 8u);
    } else if constexpr (K == 2) {
      const unsigned tk = 8 * w + 2 * g + (li >> 3);
      oo = opaque((16 * tt0 + tk) * 896u + ct * 128u + oct * 16u);
    } else if constexpr (K == 4 || K == 5) {
#pragma unroll
      for (int u = 2 * (K - 4); u < 2 * (K - 4) + 2; ++u) {
        const unsigned one = li == 4 * u + g ? 0x3f803f80u : 0u;
        sel[u] = asfrag(one, one, one, one);
        pin(sel[u]);
      }
    } else if constexpr (K == 6) {
      pin(b2v);
      blo = f32x4{bflo(b2v[0]), bfhi(b2v[0]), bflo(b2v[1]), bfhi(b2v[1])};
      bhi = f32x4{bflo(b2v[2]), bfhi(b2v[2]), bflo(b2v[3]), bfhi(b2v[3])};
      pin(blo);
      pin(bhi);
    }
  };
  auto phase2 = [&](auto NT_) {
    constexpr int NT = decltype(NT_)::value;
    v4u af[2][NT], aa[2][NT];
    v2u fl = {(unsigned)tag, (unsigned)(tag >> 32)};
    const bool fme = li == 7 + w && g <= frows;
    auto a_load = [&](auto J_, auto T_) {
      constexpr int j = decltype(J_)::value, t = decltype(T_)::value;
      if constexpr (j < 4) ld128<j * 1024u>(af[j & 1][t], va[t], rws, aob);
      else if constexpr (j < 7) ld128<(j - 4) * 1024u>(af[j & 1][t], va[t], rws, aob + 4096u);
      else ld128<(j - 7) * 1024u>(aa[j - 7][t], va[t], rws, aoa);
    };
    __builtin_amdgcn_s_setprio(3);
    rep<0, 2 * NT>([&](auto I_) { constexpr int I = decltype(I_)::value; a_load(IC<I / NT>{}, IC<I % NT>{}); });
    f32x4 acc[4][NT];
    unsigned pa = 0;
    auto put = [&](int t) {
#pragma unroll
      for (int cp = 0; cp < 2; ++cp) {
        const f32x4 x = acc[2 * cp][t], y = acc[2 * cp + 1][t];
        *(LAS v4u*)(L + pa + (t * 2 + cp) * 1024u) = v4u{pk(x[0], x[1]), pk(x[2], x[3]), pk(y[0], y[1]), pk(y[2], y[3])};
      }
    };
    rep<0, 7>([&](auto J_) {
      constexpr int j = decltype(J_)::value;
      if constexpr (j == 2 || j == 4 || j == 6) __builtin_amdgcn_s_setprio(3 - j / 2);
      if constexpr (j == 5) {
        wait_for<p2_before(5, 0, NT) - p2_before(3, 1, NT)>(fl);
        const bool todo = __builtin_bit_cast(u64, fl) != tag;
        if (__any(todo)) poll(todo);
        SCHED();
      }
      if constexpr (j == 6) pa = opaque(wreg + (g * 16u + (((li >> 1) & 3u) | (((((li & 1u) | ((li >> 3) << 1)) + w) & 3u) << 2))) * 16u);
      if constexpr (j >= 4) wait_l();
      rep<0, NT>([&](auto T_) {
        constexpr int t = decltype(T_)::value, U = p2_unit(j, t, NT);
        if constexpr (U >= 0) b_m0(IC<U>{});
        wait_v<p2_before(j, t, NT) - p2_pos(j, t, NT) - 1>();
        const bf16x8 a = __builtin_bit_cast(bf16x8, af[j & 1][t]);
#pragma unroll
        for (int c = 0; c < 4; ++c) {
          if constexpr (j == 0) mma0(acc[c][t], cat(bl[0][c], bh[0][c]), a);
          else mma(acc[c][t], cat(bl[j & 3][c], bh[j & 3][c]), a);
        }
        SCHED();
        if constexpr (U >= 0) b_go(IC<U>{});
        if constexpr (j < 6) a_load(IC<j + 2>{}, T_);
        if constexpr (j == 5) a_load(IC<8>{}, T_);
        if constexpr (j == 5 && t == 0) ld32(gl, (8 * w + 2 * g + (li >> 3)) * 4u, rws, WS_G + w * 8192u + tt0 * 64u);
        if constexpr (j == 3 && t == 0) { if (fme) ld64(fl, fln, rws, frow); }
        if constexpr (j >= 2) {
          b_rd(IC<j + 2>{}, T_, IC<0>{});
          b_rd(IC<j + 2>{}, T_, IC<1>{});
          if constexpr (NT == 3 && t == 2) {
            b_rd(IC<j + 2>{}, IC<3>{}, IC<0>{});
            b_rd(IC<j + 2>{}, IC<3>{}, IC<1>{});
          }
        }
        if constexpr (j == 6) final_setup(T_);
        SCHED();
      });
    });
    constexpr int NALL = p2_before(6, 0, NT);
    wait_l();
    rep<0, NT>([&](auto T_) {
      constexpr int t = decltype(T_)::value;
      wait_v<NALL - p2_pos(8, t, NT) - 1>();
#pragma unroll
      for (int c = 0; c < 4; ++c) mma(acc[c][t], cat(bl[3][c], bh[3][c]), __builtin_bit_cast(bf16x8, aa[0][t]));
#pragma unroll
      for (int c = 0; c < 4; ++c) mma(acc[c][t], cat(bl[0][c], bh[0][c]), __builtin_bit_cast(bf16x8, aa[1][t]));
      SCHED();
      if constexpr (t >= 1) put(t - 1);
      final_setup(IC<4 + t>{});
      SCHED();
    });
    asm volatile("s_nop 7" ::: "memory");
    SCHED();
    put(NT - 1);
  };
  if (qkv) phase2(IC<3>{});
  else phase2(IC<4>{});
  wait_l();
  bar();
  if (!qkv || w < 6) {
    pin(gl);
    bf16x8 pf[2][4];
#pragma unroll
    for (int u = 0; u < 4; ++u)
#pragma unroll
      for (int a = 0; a < 2; ++a) pf[a][u] = cat(ldstr(L, ra[u >> 1][0] + (u & 1) * 1024u + a * 256u), ldstr(L, ra[u >> 1][1] + (u & 1) * 1024u + a * 256u));
    f32x4 alo = blo, ahi = bhi;
#pragma unroll
    for (int u = 0; u < 4; ++u) {
      alo = mfma(pf[0][u], sel[u], alo);
      ahi = mfma(pf[1][u], sel[u], ahi);
    }
    alo *= gl;
    ahi *= gl;
    const v4u o = {pk(alo[0], alo[1]), pk(alo[2], alo[3]), pk(ahi[0], ahi[1]), pk(ahi[2], ahi[3])};
    *(GAS v4u*)((char*)p.out + oo) = o;
  }
}
}

extern "C" void kernel_launch(void* const* d_in, const int* in_sizes, int n_in, void* d_out, int out_size, void* d_ws, size_t ws_size,
                              hipStream_t stream) {
  gm::Params p;
  p.x = d_in[0];
  p.wqkv = d_in[1];
  p.wo = d_in[2];
  p.w1 = d_in[4];
  p.b1 = d_in[5];
  p.w2 = d_in[6];
  p.b2 = d_in[7];
  p.gw = d_in[8];
  p.out = d_out;
  p.ws = d_ws;
  gm::gm_kernel<<<256, 512, 0, stream>>>(p);
}
```
